```python
import torch

def _softmax(x, axis=-1):
    return torch.softmax(x, dim=axis)
def _gelu(x):
    return torch.nn.functional.gelu(x, approximate="tanh")
def _silu(x):
    return torch.nn.functional.silu(x)
def _sigmoid(x):
    return torch.sigmoid(x)
def _layernorm(x, axis=-1, eps=1e-5):
    m = x.mean(dim=axis, keepdim=True)
    v = x.var(dim=axis, keepdim=True, unbiased=False)
    return (x - m) / torch.sqrt(v + eps)
def _scatter_add(tgt, idx, src, axis=0):
    return tgt.clone().index_add_(axis, idx, src)
def _segment_sum(src, seg, n):
    out = torch.zeros((n,) + src.shape[1:], dtype=src.dtype)
    return out.index_add_(0, seg, src)
def _pad(x, pw):
    flat = tuple(v for pair in reversed(pw) for v in pair)
    return torch.nn.functional.pad(x, flat)

kernel_name = 'sequential_preLN__ssm_conv_h3__glu_gated_swiglu__prompt'

def setup_inputs():

    _w = (lambda a: (lambda t: t.to(torch.bfloat16) if t.is_floating_point() else t)(torch.as_tensor(a)))
    def _A_setup():
        B, S, D = 1, 2048, 1024
        ks = 4
        return {
            "q": _w((lambda *s: torch.randn(*s, dtype=torch.bfloat16) * 0.05)(B, S, D)),
            "k": _w((lambda *s: torch.randn(*s, dtype=torch.bfloat16) * 0.2)(B, S, D)),
            "v": _w((lambda *s: torch.randn(*s, dtype=torch.bfloat16) * 0.2)(B, S, D)),
            "shift_k": _w((lambda *s: torch.randn(*s, dtype=torch.bfloat16) * 0.2)(D, ks) * 0.5),
            "ssm_k": _w((lambda *s: torch.randn(*s, dtype=torch.bfloat16) * 0.4)(D, S) * (1.0 / S ** 0.5)),
            "ssm_D": _w((lambda *s: torch.randn(*s, dtype=torch.bfloat16) * 0.2)(D)),
        }
    _A_in = _A_setup()

    def _B_setup():
        B, S, D, F = 1, 2048, 1024, 3584
        return {
            "x": _w((lambda *s: torch.randn(*s, dtype=torch.bfloat16) * 0.05)(B, S, D)),
            "w1": _w((lambda *s: torch.randn(*s, dtype=torch.bfloat16) * 0.2)(D, F) * (2.0 / D) ** 0.5),
            "w3": _w((lambda *s: torch.randn(*s, dtype=torch.bfloat16) * 0.2)(D, F) * (2.0 / D) ** 0.5),
            "w2": _w((lambda *s: torch.randn(*s, dtype=torch.bfloat16) * 0.2)(F, D) * (2.0 / F) ** 0.5),
        }
    _B_in = _B_setup()

    return {
        "A_q": _A_in["q"],
        "A_k": _A_in["k"],
        "A_v": _A_in["v"],
        "A_shift_k": _A_in["shift_k"],
        "A_ssm_k": _A_in["ssm_k"],
        "A_ssm_D": _A_in["ssm_D"],
        "B_x": _B_in["x"],
        "B_w1": _B_in["w1"],
        "B_w3": _B_in["w3"],
        "B_w2": _B_in["w2"],
    }

def reference(A_q, A_k, A_v, A_shift_k, A_ssm_k, A_ssm_D, B_x, B_w1, B_w3, B_w2):
    def _A_ref(q, k, v, shift_k, ssm_k, ssm_D):
        B, S, D = q.shape
        ks = shift_k.shape[-1]
        k_pad = _pad(k, ((0, 0), (ks - 1, 0), (0, 0)))
        k_shift = torch.zeros_like(k)
        for t in range(ks):
            k_shift = k_shift + k_pad[:, t:t + S, :] * shift_k[:, ks - 1 - t][None, None, :]
        kv = k_shift * v
        L = 2 * S
        kv_pad = _pad(kv, ((0, 0), (0, S), (0, 0)))
        kf_pad = _pad(ssm_k, ((0, 0), (0, S)))
        kv_f = (lambda x, axes=(-2,-1): torch.fft.rfft2(x.float(), dim=axes))(kv_pad[:, None, :, :], axes=(1, 2))
        kf_f = (lambda x, axes=(-2,-1): torch.fft.rfft2(x.float(), dim=axes))(kf_pad[:, None, :], axes=(1, 2))
        kf_f = (lambda x, *d: x.permute(*d))(kf_f, 1, 2, 0)
        y_f = kv_f * kf_f[None, :, :, :]
        y = (lambda x, s, axes=(-2,-1): torch.fft.irfft2(x, s=s, dim=axes).to(torch.bfloat16))(y_f, (1, L), axes=(1, 2))[:, 0, :S, :]
        y = y + kv * ssm_D[None, None, :]
        return q * y

    def _B_ref(x, w1, w3, w2):
        gate = torch.einsum("bsd,df->bsf", x, w1)
        up = torch.einsum("bsd,df->bsf", x, w3)
        h = _silu(gate) * up
        out = torch.einsum("bsf,fd->bsd", h, w2)
        return out

    x = A_q
    h = x + _A_ref(_layernorm(x, axis=-1), A_k, A_v, A_shift_k, A_ssm_k, A_ssm_D)
    h2 = h + _B_ref(_layernorm(h, axis=-1), B_w1, B_w3, B_w2)
    return h2
```

```cpp
#include <hip/hip_runtime.h>
#include <hip/hip_bf16.h>
typedef unsigned short u16;
constexpr int S=2048, D=1024, F=3584;
__device__ __forceinline__ float bf2f(u16 b){ return __uint_as_float(((unsigned)b)<<16); }
__device__ __forceinline__ u16 f2bf(float f){ unsigned u=__float_as_uint(f); u += 0x7FFFu + ((u>>16)&1u); return (u16)(u>>16); }
__device__ __forceinline__ float rbf(float f){ return bf2f(f2bf(f)); }

__global__ void k_kv(const u16* k,const u16* v,const u16* sk,u16* kv){
  int i=blockIdx.x*256+threadIdx.x; int s=i/D,d=i%D;
  float acc=0.f;
  for(int t=0;t<4;++t){ int ss=s+t-3; float kk= ss>=0? bf2f(k[ss*D+d]):0.f; acc = rbf(acc + rbf(kk*bf2f(sk[d*4+3-t]))); }
  kv[i]=f2bf(acc*bf2f(v[i]));
}
__global__ void k_conv(const u16* kv,const u16* h,const u16* Dp,u16* z){
  int d=blockIdx.x*64+(threadIdx.x&63); int s=blockIdx.y*4+(threadIdx.x>>6);
  float acc=0.f;
  const u16* hp=h+(size_t)d*S;
  for(int j=0;j<=s;++j) acc += bf2f(kv[(s-j)*D+d])*bf2f(hp[j]);
  float y=rbf(acc);
  y = rbf(y + rbf(bf2f(kv[s*D+d])*bf2f(Dp[d])));
  z[s*D+d]=f2bf(y);
}
__device__ float bsum(float v,float* sh){
  for(int o=32;o>=1;o>>=1) v+=__shfl_xor(v,o);
  __syncthreads();
  if((threadIdx.x&63)==0) sh[threadIdx.x>>6]=v;
  __syncthreads();
  return sh[0]+sh[1]+sh[2]+sh[3];
}
__global__ void k_row(const u16* x,const u16* z,u16* hout,u16* hn){
  __shared__ float sh[4];
  int s=blockIdx.x; int t=threadIdx.x;
  float xv[4],hv[4]; float a=0;
  for(int i=0;i<4;++i){ xv[i]=bf2f(x[s*D+t*4+i]); a+=xv[i]; }
  float mf=bsum(a,sh)/D; float m=rbf(mf);
  a=0; for(int i=0;i<4;++i){ float dd=xv[i]-mf; a+=dd*dd; }
  float var=rbf(bsum(a,sh)/D);
  float sd=rbf(sqrtf(rbf(var+1e-5f)));
  a=0;
  for(int i=0;i<4;++i){ float q=rbf(rbf(xv[i]-m)/sd); float o=rbf(q*bf2f(z[s*D+t*4+i])); hv[i]=rbf(xv[i]+o); hout[s*D+t*4+i]=f2bf(hv[i]); a+=hv[i]; }
  mf=bsum(a,sh)/D; m=rbf(mf);
  a=0; for(int i=0;i<4;++i){ float dd=hv[i]-mf; a+=dd*dd; }
  var=rbf(bsum(a,sh)/D);
  sd=rbf(sqrtf(rbf(var+1e-5f)));
  for(int i=0;i<4;++i) hn[s*D+t*4+i]=f2bf(rbf(hv[i]-m)/sd);
}
template<int MODE>
__global__ void k_gemm(const u16* A,const u16* B,const u16* B2,const u16* R,u16* C,int M,int N,int K){
  __shared__ float As[16][65],Bs[16][65],Bs2[16][65];
  int tx=threadIdx.x&15,ty=threadIdx.x>>4; int m0=blockIdx.y*64,n0=blockIdx.x*64;
  float acc[4][4]={},acc2[4][4]={};
  for(int k0=0;k0<K;k0+=16){
    for(int i=threadIdx.x;i<1024;i+=256){ int r=i>>4,c=i&15; As[c][r]=bf2f(A[(size_t)(m0+r)*K+k0+c]); int kk=i>>6,nn=i&63; Bs[kk][nn]=bf2f(B[(size_t)(k0+kk)*N+n0+nn]); if(MODE==0) Bs2[kk][nn]=bf2f(B2[(size_t)(k0+kk)*N+n0+nn]); }
    __syncthreads();
    for(int kk=0;kk<16;++kk){ for(int i=0;i<4;++i) for(int j=0;j<4;++j){ acc[i][j]+=As[kk][ty*4+i]*Bs[kk][tx*4+j]; if(MODE==0) acc2[i][j]+=As[kk][ty*4+i]*Bs2[kk][tx*4+j]; } }
    __syncthreads();
  }
  for(int i=0;i<4;++i) for(int j=0;j<4;++j){ size_t o=(size_t)(m0+ty*4+i)*N+n0+tx*4+j;
    if(MODE==0){ float g=rbf(acc[i][j]),u=rbf(acc2[i][j]); float sg=rbf(g/(1.f+__expf(-g))); C[o]=f2bf(sg*u); }
    else { C[o]=f2bf(bf2f(R[o])+rbf(acc[i][j])); } }
}
extern "C" void kernel_launch(void* const* d_in, const int* in_sizes, int n_in, void* d_out, int out_size, void* d_ws, size_t ws_size, hipStream_t stream){
  const u16* q=(const u16*)d_in[0]; const u16* k=(const u16*)d_in[1]; const u16* v=(const u16*)d_in[2];
  const u16* sk=(const u16*)d_in[3]; const u16* h=(const u16*)d_in[4]; const u16* Dp=(const u16*)d_in[5];
  const u16* w1=(const u16*)d_in[7]; const u16* w3=(const u16*)d_in[8]; const u16* w2=(const u16*)d_in[9];
  u16* ws=(u16*)d_ws; u16* kv=ws; u16* z=kv+S*D; u16* hh=z+S*D; u16* hn=hh+S*D; u16* g=hn+S*D;
  k_kv<<<S*D/256,256,0,stream>>>(k,v,sk,kv);
  k_conv<<<dim3(D/64,S/4),256,0,stream>>>(kv,h,Dp,z);
  k_row<<<S,256,0,stream>>>(q,z,hh,hn);
  k_gemm<0><<<dim3(F/64,S/64),256,0,stream>>>(hn,w1,w3,nullptr,g,S,F,D);
  k_gemm<1><<<dim3(D/64,S/64),256,0,stream>>>(g,w2,nullptr,hh,(u16*)d_out,S,D,F);
}
```

```cpp
#include <hip/hip_runtime.h>
#include <hip/hip_bf16.h>
typedef unsigned short u16;
typedef unsigned long long u64;
typedef unsigned u32x4 __attribute__((ext_vector_type(4)));
typedef unsigned u32x16 __attribute__((ext_vector_type(16)));
typedef unsigned u32x2 __attribute__((ext_vector_type(2)));
typedef float f32x32 __attribute__((ext_vector_type(32)));
typedef float f32x4 __attribute__((ext_vector_type(4)));
typedef float f32x2 __attribute__((ext_vector_type(2)));
typedef __bf16 bf16x2_t __attribute__((ext_vector_type(2)));
#define LAS __attribute__((address_space(3)))
#define GAS __attribute__((address_space(1)))
typedef __amdgpu_buffer_rsrc_t rsrc_t;
constexpr int S = 2048, D = 1024, F = 3584;
constexpr unsigned WS_EP = 0, WS_F1 = 32768, WS_F2 = 655360, WS_F3 = 98304, WS_F4 = 131072, WS_F4B = 262144, WS_F5 = 393216;
constexpr unsigned WS_UR = 1u << 20, WS_Z = 5u << 20, WS_H = 9u << 20, WS_HN = 13u << 20, WS_HH = 17u << 20, WS_PART = 32u << 20, WS_END = 48u << 20;
constexpr unsigned L_G = 65536, CV_G1 = 4160, CV_CH = 8320, L_U = L_G + 4 * CV_CH, CV_UCH = 4608, L_Y = L_U + 4 * CV_UCH, L_ST = L_Y + 16384;
constexpr int NREP = 8, D1 = 6, D2 = 16;
constexpr unsigned LDS_BYTES = 163840, SPIN_LIMIT = 1u << 18;
constexpr unsigned OOB = 0x40000000u;

#define G1_ASM \
  "s_mov_b32 s40, %[pa0]\n" \
  "s_mov_b32 s41, %[pa1]\n" \
  "s_mov_b32 s42, %[sza]\n" \
  "s_mov_b32 s43, 0x20000\n" \
  "s_mov_b32 s44, %[pw00]\n" \
  "s_mov_b32 s45, %[pw01]\n" \
  "s_mov_b32 s46, %[szw]\n" \
  "s_mov_b32 s47, 0x20000\n" \
  "s_mov_b32 s48, %[pw10]\n" \
  "s_mov_b32 s49, %[pw11]\n" \
  "s_mov_b32 s50, %[szw]\n" \
  "s_mov_b32 s51, 0x20000\n" \
  "s_mov_b32 s56, 917504\n" \
  "s_mov_b32 s57, 946176\n" \
  "s_mov_b32 s58, 974848\n" \
  "s_mov_b32 s59, 1003520\n" \
  "s_add_u32 s61, %[ldsw], 65536\n" \
  "s_mov_b32 s52, 0\n" \
  "s_mov_b32 s53, 32768\n" \
  "s_mov_b32 s54, 65536\n" \
  "s_mov_b32 s55, 98304\n" \
  "s_mov_b32 s60, 0\n" \
  "v_mov_b32 v24, %[vl0]\n" \
  "v_mov_b32 v25, %[vl1]\n" \
  "v_mov_b32 v26, %[vl2]\n" \
  "v_mov_b32 v27, %[vl3]\n" \
  "v_mov_b32 v28, %[vl4]\n" \
  "v_mov_b32 v29, %[vl5]\n" \
  "v_mov_b32 v30, %[vl6]\n" \
  "v_mov_b32 v32, 0\n" \
  "v_mov_b32 v33, 0\n" \
  "v_mov_b32 v34, 0\n" \
  "v_mov_b32 v35, 0\n" \
  "v_mov_b32 v36, 0\n" \
  "v_mov_b32 v37, 0\n" \
  "v_mov_b32 v38, 0\n" \
  "v_mov_b32 v39, 0\n" \
  "v_mov_b32 v40, 0\n" \
  "v_mov_b32 v41, 0\n" \
  "v_mov_b32 v42, 0\n" \
  "v_mov_b32 v43, 0\n" \
  "v_mov_b32 v44, 0\n" \
  "v_mov_b32 v45, 0\n" \
  "v_mov_b32 v46, 0\n" \
  "v_mov_b32 v47, 0\n" \
  "v_mov_b32 v48, 0\n" \
  "v_mov_b32 v49, 0\n" \
  "v_mov_b32 v50, 0\n" \
  "v_mov_b32 v51, 0\n" \
  "v_mov_b32 v52, 0\n" \
  "v_mov_b32 v53, 0\n" \
  "v_mov_b32 v54, 0\n" \
  "v_mov_b32 v55, 0\n" \
  "v_mov_b32 v56, 0\n" \
  "v_mov_b32 v57, 0\n" \
  "v_mov_b32 v58, 0\n" \
  "v_mov_b32 v59, 0\n" \
  "v_mov_b32 v60, 0\n" \
  "v_mov_b32 v61, 0\n" \
  "v_mov_b32 v62, 0\n" \
  "v_mov_b32 v63, 0\n" \
  "v_mov_b32 v64, 0\n" \
  "v_mov_b32 v65, 0\n" \
  "v_mov_b32 v66, 0\n" \
  "v_mov_b32 v67, 0\n" \
  "v_mov_b32 v68, 0\n" \
  "v_mov_b32 v69, 0\n" \
  "v_mov_b32 v70, 0\n" \
  "v_mov_b32 v71, 0\n" \
  "v_mov_b32 v72, 0\n" \
  "v_mov_b32 v73, 0\n" \
  "v_mov_b32 v74, 0\n" \
  "v_mov_b32 v75, 0\n" \
  "v_mov_b32 v76, 0\n" \
  "v_mov_b32 v77, 0\n" \
  "v_mov_b32 v78, 0\n" \
  "v_mov_b32 v79, 0\n" \
  "v_mov_b32 v80, 0\n" \
  "v_mov_b32 v81, 0\n" \
  "v_mov_b32 v82, 0\n" \
  "v_mov_b32 v83, 0\n" \
  "v_mov_b32 v84, 0\n" \
  "v_mov_b32 v85, 0\n" \
  "v_mov_b32 v86, 0\n" \
  "v_mov_b32 v87, 0\n" \
  "v_mov_b32 v88, 0\n" \
  "v_mov_b32 v89, 0\n" \
  "v_mov_b32 v90, 0\n" \
  "v_mov_b32 v91, 0\n" \
  "v_mov_b32 v92, 0\n" \
  "v_mov_b32 v93, 0\n" \
  "v_mov_b32 v94, 0\n" \
  "v_mov_b32 v95, 0\n" \
  "v_mov_b32 v96, 0\n" \
  "v_mov_b32 v97, 0\n" \
  "v_mov_b32 v98, 0\n" \
  "v_mov_b32 v99, 0\n" \
  "v_mov_b32 v100, 0\n" \
  "v_mov_b32 v101, 0\n" \
  "v_mov_b32 v102, 0\n" \
  "v_mov_b32 v103, 0\n" \
  "v_mov_b32 v104, 0\n" \
  "v_mov_b32 v105, 0\n" \
  "v_mov_b32 v106, 0\n" \
  "v_mov_b32 v107, 0\n" \
  "v_mov_b32 v108, 0\n" \
  "v_mov_b32 v109, 0\n" \
  "v_mov_b32 v110, 0\n" \
  "v_mov_b32 v111, 0\n" \
  "v_mov_b32 v112, 0\n" \
  "v_mov_b32 v113, 0\n" \
  "v_mov_b32 v114, 0\n" \
  "v_mov_b32 v115, 0\n" \
  "v_mov_b32 v116, 0\n" \
  "v_mov_b32 v117, 0\n" \
  "v_mov_b32 v118, 0\n" \
  "v_mov_b32 v119, 0\n" \
  "v_mov_b32 v120, 0\n" \
  "v_mov_b32 v121, 0\n" \
  "v_mov_b32 v122, 0\n" \
  "v_mov_b32 v123, 0\n" \
  "v_mov_b32 v124, 0\n" \
  "v_mov_b32 v125, 0\n" \
  "v_mov_b32 v126, 0\n" \
  "v_mov_b32 v127, 0\n" \
  "v_mov_b32 v128, 0\n" \
  "v_mov_b32 v129, 0\n" \
  "v_mov_b32 v130, 0\n" \
  "v_mov_b32 v131, 0\n" \
  "v_mov_b32 v132, 0\n" \
  "v_mov_b32 v133, 0\n" \
  "v_mov_b32 v134, 0\n" \
  "v_mov_b32 v135, 0\n" \
  "v_mov_b32 v136, 0\n" \
  "v_mov_b32 v137, 0\n" \
  "v_mov_b32 v138, 0\n" \
  "v_mov_b32 v139, 0\n" \
  "v_mov_b32 v140, 0\n" \
  "v_mov_b32 v141, 0\n" \
  "v_mov_b32 v142, 0\n" \
  "v_mov_b32 v143, 0\n" \
  "v_mov_b32 v144, 0\n" \
  "v_mov_b32 v145, 0\n" \
  "v_mov_b32 v146, 0\n" \
  "v_mov_b32 v147, 0\n" \
  "v_mov_b32 v148, 0\n" \
  "v_mov_b32 v149, 0\n" \
  "v_mov_b32 v150, 0\n" \
  "v_mov_b32 v151, 0\n" \
  "v_mov_b32 v152, 0\n" \
  "v_mov_b32 v153, 0\n" \
  "v_mov_b32 v154, 0\n" \
  "v_mov_b32 v155, 0\n" \
  "v_mov_b32 v156, 0\n" \
  "v_mov_b32 v157, 0\n" \
  "v_mov_b32 v158, 0\n" \
  "v_mov_b32 v159, 0\n" \
  "v_mov_b32 v160, 0\n" \
  "v_mov_b32 v161, 0\n" \
  "v_mov_b32 v162, 0\n" \
  "v_mov_b32 v163, 0\n" \
  "v_mov_b32 v164, 0\n" \
  "v_mov_b32 v165, 0\n" \
  "v_mov_b32 v166, 0\n" \
  "v_mov_b32 v167, 0\n" \
  "v_mov_b32 v168, 0\n" \
  "v_mov_b32 v169, 0\n" \
  "v_mov_b32 v170, 0\n" \
  "v_mov_b32 v171, 0\n" \
  "v_mov_b32 v172, 0\n" \
  "v_mov_b32 v173, 0\n" \
  "v_mov_b32 v174, 0\n" \
  "v_mov_b32 v175, 0\n" \
  "v_mov_b32 v176, 0\n" \
  "v_mov_b32 v177, 0\n" \
  "v_mov_b32 v178, 0\n" \
  "v_mov_b32 v179, 0\n" \
  "v_mov_b32 v180, 0\n" \
  "v_mov_b32 v181, 0\n" \
  "global_load_dwordx2 %[ft], %[fa], off sc1\n" \
  "v_mov_b32 v182, 0\n" \
  "v_mov_b32 v183, 0\n" \
  "v_mov_b32 v184, 0\n" \
  "v_mov_b32 v185, 0\n" \
  "v_mov_b32 v186, 0\n" \
  "v_mov_b32 v187, 0\n" \
  "v_mov_b32 v188, 0\n" \
  "v_mov_b32 v189, 0\n" \
  "v_mov_b32 v190, 0\n" \
  "v_mov_b32 v191, 0\n" \
  "v_mov_b32 v192, 0\n" \
  "v_mov_b32 v193, 0\n" \
  "v_mov_b32 v194, 0\n" \
  "v_mov_b32 v195, 0\n" \
  "v_mov_b32 v196, 0\n" \
  "v_mov_b32 v197, 0\n" \
  "v_mov_b32 v198, 0\n" \
  "v_mov_b32 v199, 0\n" \
  "v_mov_b32 v200, 0\n" \
  "v_mov_b32 v201, 0\n" \
  "v_mov_b32 v202, 0\n" \
  "v_mov_b32 v203, 0\n" \
  "v_mov_b32 v204, 0\n" \
  "v_mov_b32 v205, 0\n" \
  "v_mov_b32 v206, 0\n" \
  "v_mov_b32 v207, 0\n" \
  "v_mov_b32 v208, 0\n" \
  "v_mov_b32 v209, 0\n" \
  "v_mov_b32 v210, 0\n" \
  "v_mov_b32 v211, 0\n" \
  "v_mov_b32 v212, 0\n" \
  "v_mov_b32 v213, 0\n" \
  "v_mov_b32 v214, 0\n" \
  "v_mov_b32 v215, 0\n" \
  "v_mov_b32 v216, 0\n" \
  "v_mov_b32 v217, 0\n" \
  "v_mov_b32 v218, 0\n" \
  "v_mov_b32 v219, 0\n" \
  "v_mov_b32 v220, 0\n" \
  "v_mov_b32 v221, 0\n" \
  "v_mov_b32 v222, 0\n" \
  "v_mov_b32 v223, 0\n" \
  "v_mov_b32 v224, 0\n" \
  "v_mov_b32 v225, 0\n" \
  "v_mov_b32 v226, 0\n" \
  "v_mov_b32 v227, 0\n" \
  "v_mov_b32 v228, 0\n" \
  "v_mov_b32 v229, 0\n" \
  "v_mov_b32 v230, 0\n" \
  "v_mov_b32 v231, 0\n" \
  "v_mov_b32 v232, 0\n" \
  "v_mov_b32 v233, 0\n" \
  "v_mov_b32 v234, 0\n" \
  "v_mov_b32 v235, 0\n" \
  "v_mov_b32 v236, 0\n" \
  "v_mov_b32 v237, 0\n" \
  "v_mov_b32 v238, 0\n" \
  "v_mov_b32 v239, 0\n" \
  "v_mov_b32 v240, 0\n" \
  "v_mov_b32 v241, 0\n" \
  "v_mov_b32 v242, 0\n" \
  "v_mov_b32 v243, 0\n" \
  "v_mov_b32 v244, 0\n" \
  "v_mov_b32 v245, 0\n" \
  "v_mov_b32 v246, 0\n" \
  "v_mov_b32 v247, 0\n" \
  "v_mov_b32 v248, 0\n" \
  "v_mov_b32 v249, 0\n" \
  "v_mov_b32 v250, 0\n" \
  "v_mov_b32 v251, 0\n" \
  "v_mov_b32 v252, 0\n" \
  "v_mov_b32 v253, 0\n" \
  "v_mov_b32 v254, 0\n" \
  "v_mov_b32 v255, 0\n" \
  "s_mov_b32 s64, 262144\n" \
  "s_branch Lchk%=\n" \
  "Lpoll%=:\n" \
  "global_load_dwordx2 %[ft], %[fa], off sc1\n" \
  "Lchk%=:\n" \
  "s_waitcnt vmcnt(0)\n" \
  "v_cmp_ne_u64 vcc, %[tag], %[ft]\n" \
  "s_nop 1\n" \
  "s_cbranch_vccz Lgo%=\n" \
  "s_sub_u32 s64, s64, 1\n" \
  "s_cbranch_scc0 Lpoll%=\n" \
  "Lgo%=:\n" \
  "buffer_load_dwordx4 a[0:3], %[voa], s[40:43], s52 offen offset:0 sc1\n" \
  "buffer_load_dwordx4 a[4:7], %[voa], s[40:43], s52 offen offset:1024 sc1\n" \
  "s_add_u32 s52, s52, 2048\n" \
  "buffer_load_dwordx4 a[8:11], %[voa], s[40:43], s53 offen offset:0 sc1\n" \
  "buffer_load_dwordx4 a[12:15], %[voa], s[40:43], s53 offen offset:1024 sc1\n" \
  "s_add_u32 s53, s53, 2048\n" \
  "buffer_load_dwordx4 a[16:19], %[voa], s[40:43], s54 offen offset:0 sc1\n" \
  "buffer_load_dwordx4 a[20:23], %[voa], s[40:43], s54 offen offset:1024 sc1\n" \
  "s_add_u32 s54, s54, 2048\n" \
  "buffer_load_dwordx4 a[24:27], %[voa], s[40:43], s55 offen offset:0 sc1\n" \
  "buffer_load_dwordx4 a[28:31], %[voa], s[40:43], s55 offen offset:1024 sc1\n" \
  "s_add_u32 s55, s55, 2048\n" \
  "s_waitcnt vmcnt(0)\n" \
  "s_barrier\n" \
  "ds_read_b64_tr_b16 a[64:65], v24 offset:0\n" \
  "ds_read_b64_tr_b16 a[66:67], v24 offset:1024\n" \
  "ds_read_b64_tr_b16 a[68:69], v24 offset:8192\n" \
  "ds_read_b64_tr_b16 a[70:71], v24 offset:9216\n" \
  "ds_read_b64_tr_b16 a[72:73], v24 offset:16384\n" \
  "ds_read_b64_tr_b16 a[74:75], v24 offset:17408\n" \
  "ds_read_b64_tr_b16 a[76:77], v24 offset:24576\n" \
  "ds_read_b64_tr_b16 a[78:79], v24 offset:25600\n" \
  "s_mov_b32 s64, 6\n" \
  "Lloop%=:\n" \
  "s_waitcnt lgkmcnt(4)\n" \
  "v_mfma_f32_16x16x32_bf16 v[32:35], a[64:67], a[0:3], v[32:35]\n" \
  "ds_read_b64_tr_b16 a[80:81], v25 offset:0\n" \
  "v_mfma_f32_16x16x32_bf16 v[32:35], a[68:71], a[4:7], v[32:35]\n" \
  "ds_read_b64_tr_b16 a[82:83], v25 offset:1024\n" \
  "v_mfma_f32_16x16x32_bf16 v[88:91], a[68:71], a[12:15], v[88:91]\n" \
  "ds_read_b64_tr_b16 a[84:85], v25 offset:8192\n" \
  "v_mfma_f32_16x16x32_bf16 v[88:91], a[64:67], a[8:11], v[88:91]\n" \
  "ds_read_b64_tr_b16 a[86:87], v25 offset:9216\n" \
  "v_mfma_f32_16x16x32_bf16 v[144:147], a[64:67], a[16:19], v[144:147]\n" \
  "buffer_load_dwordx4 a[32:35], %[voa], s[40:43], s52 offen offset:0 sc1\n" \
  "s_add_u32 s60, s60, 32768\n" \
  "v_mfma_f32_16x16x32_bf16 v[144:147], a[68:71], a[20:23], v[144:147]\n" \
  "s_and_b32 s60, s60, 131071\n" \
  "v_mfma_f32_16x16x32_bf16 v[200:203], a[68:71], a[28:31], v[200:203]\n" \
  "buffer_load_dwordx4 a[36:39], %[voa], s[40:43], s52 offen offset:1024 sc1\n" \
  "v_add_u32 v24, s60, %[vl0]\n" \
  "v_mfma_f32_16x16x32_bf16 v[200:203], a[64:67], a[24:27], v[200:203]\n" \
  "s_add_u32 s52, s52, 2048\n" \
  "s_waitcnt lgkmcnt(4)\n" \
  "v_mfma_f32_16x16x32_bf16 v[204:207], a[72:75], a[24:27], v[204:207]\n" \
  "ds_read_b64_tr_b16 a[88:89], v25 offset:16384\n" \
  "v_mfma_f32_16x16x32_bf16 v[204:207], a[76:79], a[28:31], v[204:207]\n" \
  "ds_read_b64_tr_b16 a[90:91], v25 offset:17408\n" \
  "v_mfma_f32_16x16x32_bf16 v[148:151], a[76:79], a[20:23], v[148:151]\n" \
  "ds_read_b64_tr_b16 a[92:93], v25 offset:24576\n" \
  "v_mfma_f32_16x16x32_bf16 v[148:151], a[72:75], a[16:19], v[148:151]\n" \
  "ds_read_b64_tr_b16 a[94:95], v25 offset:25600\n" \
  "v_mfma_f32_16x16x32_bf16 v[92:95], a[72:75], a[8:11], v[92:95]\n" \
  "buffer_load_dwordx4 a[40:43], %[voa], s[40:43], s53 offen offset:0 sc1\n" \
  "v_add_u32 v25, s60, %[vl1]\n" \
  "v_mfma_f32_16x16x32_bf16 v[92:95], a[76:79], a[12:15], v[92:95]\n" \
  "v_mfma_f32_16x16x32_bf16 v[36:39], a[76:79], a[4:7], v[36:39]\n" \
  "v_mfma_f32_16x16x32_bf16 v[36:39], a[72:75], a[0:3], v[36:39]\n" \
  "s_waitcnt lgkmcnt(4)\n" \
  "v_mfma_f32_16x16x32_bf16 v[40:43], a[80:83], a[0:3], v[40:43]\n" \
  "ds_read_b64_tr_b16 a[64:65], v26 offset:0\n" \
  "v_mfma_f32_16x16x32_bf16 v[40:43], a[84:87], a[4:7], v[40:43]\n" \
  "ds_read_b64_tr_b16 a[66:67], v26 offset:1024\n" \
  "v_mfma_f32_16x16x32_bf16 v[96:99], a[84:87], a[12:15], v[96:99]\n" \
  "ds_read_b64_tr_b16 a[68:69], v26 offset:8192\n" \
  "v_mfma_f32_16x16x32_bf16 v[96:99], a[80:83], a[8:11], v[96:99]\n" \
  "ds_read_b64_tr_b16 a[70:71], v26 offset:9216\n" \
  "v_mfma_f32_16x16x32_bf16 v[152:155], a[80:83], a[16:19], v[152:155]\n" \
  "buffer_load_dwordx4 a[44:47], %[voa], s[40:43], s53 offen offset:1024 sc1\n" \
  "v_mfma_f32_16x16x32_bf16 v[152:155], a[84:87], a[20:23], v[152:155]\n" \
  "s_add_u32 s53, s53, 2048\n" \
  "v_mfma_f32_16x16x32_bf16 v[208:211], a[84:87], a[28:31], v[208:211]\n" \
  "buffer_load_dwordx4 a[48:51], %[voa], s[40:43], s54 offen offset:0 sc1\n" \
  "v_mfma_f32_16x16x32_bf16 v[208:211], a[80:83], a[24:27], v[208:211]\n" \
  "s_waitcnt lgkmcnt(4)\n" \
  "v_mfma_f32_16x16x32_bf16 v[212:215], a[88:91], a[24:27], v[212:215]\n" \
  "ds_read_b64_tr_b16 a[72:73], v26 offset:16384\n" \
  "v_mfma_f32_16x16x32_bf16 v[212:215], a[92:95], a[28:31], v[212:215]\n" \
  "ds_read_b64_tr_b16 a[74:75], v26 offset:17408\n" \
  "v_mfma_f32_16x16x32_bf16 v[156:159], a[92:95], a[20:23], v[156:159]\n" \
  "ds_read_b64_tr_b16 a[76:77], v26 offset:24576\n" \
  "v_mfma_f32_16x16x32_bf16 v[156:159], a[88:91], a[16:19], v[156:159]\n" \
  "ds_read_b64_tr_b16 a[78:79], v26 offset:25600\n" \
  "v_mfma_f32_16x16x32_bf16 v[100:103], a[88:91], a[8:11], v[100:103]\n" \
  "v_add_u32 v26, s60, %[vl2]\n" \
  "v_mfma_f32_16x16x32_bf16 v[100:103], a[92:95], a[12:15], v[100:103]\n" \
  "v_mfma_f32_16x16x32_bf16 v[44:47], a[92:95], a[4:7], v[44:47]\n" \
  "buffer_load_dwordx4 a[52:55], %[voa], s[40:43], s54 offen offset:1024 sc1\n" \
  "v_mfma_f32_16x16x32_bf16 v[44:47], a[88:91], a[0:3], v[44:47]\n" \
  "s_add_u32 s54, s54, 2048\n" \
  "s_waitcnt lgkmcnt(4)\n" \
  "v_mfma_f32_16x16x32_bf16 v[48:51], a[64:67], a[0:3], v[48:51]\n" \
  "ds_read_b64_tr_b16 a[80:81], v27 offset:0\n" \
  "v_mfma_f32_16x16x32_bf16 v[48:51], a[68:71], a[4:7], v[48:51]\n" \
  "ds_read_b64_tr_b16 a[82:83], v27 offset:1024\n" \
  "v_mfma_f32_16x16x32_bf16 v[104:107], a[68:71], a[12:15], v[104:107]\n" \
  "ds_read_b64_tr_b16 a[84:85], v27 offset:8192\n" \
  "v_mfma_f32_16x16x32_bf16 v[104:107], a[64:67], a[8:11], v[104:107]\n" \
  "ds_read_b64_tr_b16 a[86:87], v27 offset:9216\n" \
  "v_mfma_f32_16x16x32_bf16 v[160:163], a[64:67], a[16:19], v[160:163]\n" \
  "buffer_load_dwordx4 a[56:59], %[voa], s[40:43], s55 offen offset:0 sc1\n" \
  "v_mfma_f32_16x16x32_bf16 v[160:163], a[68:71], a[20:23], v[160:163]\n" \
  "v_mfma_f32_16x16x32_bf16 v[216:219], a[68:71], a[28:31], v[216:219]\n" \
  "buffer_load_dwordx4 a[60:63], %[voa], s[40:43], s55 offen offset:1024 sc1\n" \
  "v_mfma_f32_16x16x32_bf16 v[216:219], a[64:67], a[24:27], v[216:219]\n" \
  "s_add_u32 s55, s55, 2048\n" \
  "s_waitcnt lgkmcnt(4)\n" \
  "v_mfma_f32_16x16x32_bf16 v[220:223], a[72:75], a[24:27], v[220:223]\n" \
  "ds_read_b64_tr_b16 a[88:89], v27 offset:16384\n" \
  "v_mfma_f32_16x16x32_bf16 v[220:223], a[76:79], a[28:31], v[220:223]\n" \
  "ds_read_b64_tr_b16 a[90:91], v27 offset:17408\n" \
  "v_mfma_f32_16x16x32_bf16 v[164:167], a[76:79], a[20:23], v[164:167]\n" \
  "ds_read_b64_tr_b16 a[92:93], v27 offset:24576\n" \
  "v_mfma_f32_16x16x32_bf16 v[164:167], a[72:75], a[16:19], v[164:167]\n" \
  "ds_read_b64_tr_b16 a[94:95], v27 offset:25600\n" \
  "v_mfma_f32_16x16x32_bf16 v[108:111], a[72:75], a[8:11], v[108:111]\n" \
  "v_add_u32 v27, s60, %[vl3]\n" \
  "v_mfma_f32_16x16x32_bf16 v[108:111], a[76:79], a[12:15], v[108:111]\n" \
  "s_add_u32 m0, s61, 0\n" \
  "v_mfma_f32_16x16x32_bf16 v[52:55], a[76:79], a[4:7], v[52:55]\n" \
  "buffer_load_dwordx4 %[vow0], s[44:47], s56 offen lds\n" \
  "v_mfma_f32_16x16x32_bf16 v[52:55], a[72:75], a[0:3], v[52:55]\n" \
  "s_waitcnt lgkmcnt(4)\n" \
  "v_mfma_f32_16x16x32_bf16 v[56:59], a[80:83], a[0:3], v[56:59]\n" \
  "ds_read_b64_tr_b16 a[64:65], v28 offset:0\n" \
  "v_mfma_f32_16x16x32_bf16 v[56:59], a[84:87], a[4:7], v[56:59]\n" \
  "ds_read_b64_tr_b16 a[66:67], v28 offset:1024\n" \
  "v_mfma_f32_16x16x32_bf16 v[112:115], a[84:87], a[12:15], v[112:115]\n" \
  "ds_read_b64_tr_b16 a[68:69], v28 offset:8192\n" \
  "v_mfma_f32_16x16x32_bf16 v[112:115], a[80:83], a[8:11], v[112:115]\n" \
  "ds_read_b64_tr_b16 a[70:71], v28 offset:9216\n" \
  "s_add_u32 m0, s61, 1024\n" \
  "v_mfma_f32_16x16x32_bf16 v[168:171], a[80:83], a[16:19], v[168:171]\n" \
  "buffer_load_dwordx4 %[vow0], s[44:47], s57 offen lds\n" \
  "v_mfma_f32_16x16x32_bf16 v[168:171], a[84:87], a[20:23], v[168:171]\n" \
  "v_mfma_f32_16x16x32_bf16 v[224:227], a[84:87], a[28:31], v[224:227]\n" \
  "v_mfma_f32_16x16x32_bf16 v[224:227], a[80:83], a[24:27], v[224:227]\n" \
  "s_waitcnt lgkmcnt(4)\n" \
  "v_mfma_f32_16x16x32_bf16 v[228:231], a[88:91], a[24:27], v[228:231]\n" \
  "ds_read_b64_tr_b16 a[72:73], v28 offset:16384\n" \
  "v_mfma_f32_16x16x32_bf16 v[228:231], a[92:95], a[28:31], v[228:231]\n" \
  "ds_read_b64_tr_b16 a[74:75], v28 offset:17408\n" \
  "v_mfma_f32_16x16x32_bf16 v[172:175], a[92:95], a[20:23], v[172:175]\n" \
  "ds_read_b64_tr_b16 a[76:77], v28 offset:24576\n" \
  "v_mfma_f32_16x16x32_bf16 v[172:175], a[88:91], a[16:19], v[172:175]\n" \
  "ds_read_b64_tr_b16 a[78:79], v28 offset:25600\n" \
  "s_add_u32 m0, s61, 2048\n" \
  "v_mfma_f32_16x16x32_bf16 v[116:119], a[88:91], a[8:11], v[116:119]\n" \
  "buffer_load_dwordx4 %[vow1], s[44:47], s58 offen lds\n" \
  "v_add_u32 v28, s60, %[vl4]\n" \
  "v_mfma_f32_16x16x32_bf16 v[116:119], a[92:95], a[12:15], v[116:119]\n" \
  "s_add_u32 m0, s61, 3072\n" \
  "v_mfma_f32_16x16x32_bf16 v[60:63], a[92:95], a[4:7], v[60:63]\n" \
  "buffer_load_dwordx4 %[vow1], s[44:47], s59 offen lds\n" \
  "v_mfma_f32_16x16x32_bf16 v[60:63], a[88:91], a[0:3], v[60:63]\n" \
  "s_waitcnt lgkmcnt(4)\n" \
  "v_mfma_f32_16x16x32_bf16 v[64:67], a[64:67], a[0:3], v[64:67]\n" \
  "ds_read_b64_tr_b16 a[80:81], v29 offset:0\n" \
  "v_mfma_f32_16x16x32_bf16 v[64:67], a[68:71], a[4:7], v[64:67]\n" \
  "ds_read_b64_tr_b16 a[82:83], v29 offset:1024\n" \
  "v_mfma_f32_16x16x32_bf16 v[120:123], a[68:71], a[12:15], v[120:123]\n" \
  "ds_read_b64_tr_b16 a[84:85], v29 offset:8192\n" \
  "v_mfma_f32_16x16x32_bf16 v[120:123], a[64:67], a[8:11], v[120:123]\n" \
  "ds_read_b64_tr_b16 a[86:87], v29 offset:9216\n" \
  "s_add_u32 m0, s61, 16384\n" \
  "v_mfma_f32_16x16x32_bf16 v[176:179], a[64:67], a[16:19], v[176:179]\n" \
  "buffer_load_dwordx4 %[vow0], s[48:51], s56 offen lds\n" \
  "v_mfma_f32_16x16x32_bf16 v[176:179], a[68:71], a[20:23], v[176:179]\n" \
  "s_add_u32 s56, s56, 458752\n" \
  "v_mfma_f32_16x16x32_bf16 v[232:235], a[68:71], a[28:31], v[232:235]\n" \
  "v_mfma_f32_16x16x32_bf16 v[232:235], a[64:67], a[24:27], v[232:235]\n" \
  "s_waitcnt lgkmcnt(4)\n" \
  "v_mfma_f32_16x16x32_bf16 v[236:239], a[72:75], a[24:27], v[236:239]\n" \
  "ds_read_b64_tr_b16 a[88:89], v29 offset:16384\n" \
  "v_mfma_f32_16x16x32_bf16 v[236:239], a[76:79], a[28:31], v[236:239]\n" \
  "ds_read_b64_tr_b16 a[90:91], v29 offset:17408\n" \
  "v_mfma_f32_16x16x32_bf16 v[180:183], a[76:79], a[20:23], v[180:183]\n" \
  "ds_read_b64_tr_b16 a[92:93], v29 offset:24576\n" \
  "v_mfma_f32_16x16x32_bf16 v[180:183], a[72:75], a[16:19], v[180:183]\n" \
  "ds_read_b64_tr_b16 a[94:95], v29 offset:25600\n" \
  "s_add_u32 m0, s61, 17408\n" \
  "v_mfma_f32_16x16x32_bf16 v[124:127], a[72:75], a[8:11], v[124:127]\n" \
  "buffer_load_dwordx4 %[vow0], s[48:51], s57 offen lds\n" \
  "v_add_u32 v29, s60, %[vl5]\n" \
  "v_mfma_f32_16x16x32_bf16 v[124:127], a[76:79], a[12:15], v[124:127]\n" \
  "s_add_u32 m0, s61, 18432\n" \
  "s_add_u32 s57, s57, 458752\n" \
  "v_mfma_f32_16x16x32_bf16 v[68:71], a[76:79], a[4:7], v[68:71]\n" \
  "buffer_load_dwordx4 %[vow1], s[48:51], s58 offen lds\n" \
  "v_mfma_f32_16x16x32_bf16 v[68:71], a[72:75], a[0:3], v[68:71]\n" \
  "s_add_u32 s58, s58, 458752\n" \
  "s_waitcnt lgkmcnt(4)\n" \
  "v_mfma_f32_16x16x32_bf16 v[72:75], a[80:83], a[0:3], v[72:75]\n" \
  "ds_read_b64_tr_b16 a[64:65], v30 offset:0\n" \
  "v_mfma_f32_16x16x32_bf16 v[72:75], a[84:87], a[4:7], v[72:75]\n" \
  "ds_read_b64_tr_b16 a[66:67], v30 offset:1024\n" \
  "v_mfma_f32_16x16x32_bf16 v[128:131], a[84:87], a[12:15], v[128:131]\n" \
  "ds_read_b64_tr_b16 a[68:69], v30 offset:8192\n" \
  "v_mfma_f32_16x16x32_bf16 v[128:131], a[80:83], a[8:11], v[128:131]\n" \
  "ds_read_b64_tr_b16 a[70:71], v30 offset:9216\n" \
  "v_mfma_f32_16x16x32_bf16 v[184:187], a[80:83], a[16:19], v[184:187]\n" \
  "v_mfma_f32_16x16x32_bf16 v[184:187], a[84:87], a[20:23], v[184:187]\n" \
  "s_add_u32 m0, s61, 19456\n" \
  "v_mfma_f32_16x16x32_bf16 v[240:243], a[84:87], a[28:31], v[240:243]\n" \
  "buffer_load_dwordx4 %[vow1], s[48:51], s59 offen lds\n" \
  "v_mfma_f32_16x16x32_bf16 v[240:243], a[80:83], a[24:27], v[240:243]\n" \
  "s_add_u32 s59, s59, 458752\n" \
  "s_waitcnt lgkmcnt(4)\n" \
  "v_mfma_f32_16x16x32_bf16 v[244:247], a[88:91], a[24:27], v[244:247]\n" \
  "ds_read_b64_tr_b16 a[72:73], v30 offset:16384\n" \
  "s_add_u32 s61, s61, 32768\n" \
  "v_mfma_f32_16x16x32_bf16 v[244:247], a[92:95], a[28:31], v[244:247]\n" \
  "ds_read_b64_tr_b16 a[74:75], v30 offset:17408\n" \
  "s_and_b32 s61, s61, 131071\n" \
  "v_mfma_f32_16x16x32_bf16 v[188:191], a[92:95], a[20:23], v[188:191]\n" \
  "ds_read_b64_tr_b16 a[76:77], v30 offset:24576\n" \
  "v_mfma_f32_16x16x32_bf16 v[188:191], a[88:91], a[16:19], v[188:191]\n" \
  "ds_read_b64_tr_b16 a[78:79], v30 offset:25600\n" \
  "s_mov_b32 m0, 163584\n" \
  "v_mfma_f32_16x16x32_bf16 v[132:135], a[88:91], a[8:11], v[132:135]\n" \
  "buffer_load_dword %[vt], %[srdt], 0 offen lds\n" \
  "v_add_u32 v30, s60, %[vl6]\n" \
  "v_mfma_f32_16x16x32_bf16 v[132:135], a[92:95], a[12:15], v[132:135]\n" \
  "v_add_u32 %[vt], %[st], %[vt]\n" \
  "v_mfma_f32_16x16x32_bf16 v[76:79], a[92:95], a[4:7], v[76:79]\n" \
  "v_mfma_f32_16x16x32_bf16 v[76:79], a[88:91], a[0:3], v[76:79]\n" \
  "s_waitcnt vmcnt(9) lgkmcnt(4)\n" \
  "s_barrier\n" \
  "v_mfma_f32_16x16x32_bf16 v[80:83], a[64:67], a[0:3], v[80:83]\n" \
  "ds_read_b64_tr_b16 a[80:81], v24 offset:0\n" \
  "v_mfma_f32_16x16x32_bf16 v[80:83], a[68:71], a[4:7], v[80:83]\n" \
  "ds_read_b64_tr_b16 a[82:83], v24 offset:1024\n" \
  "v_mfma_f32_16x16x32_bf16 v[136:139], a[68:71], a[12:15], v[136:139]\n" \
  "ds_read_b64_tr_b16 a[84:85], v24 offset:8192\n" \
  "v_mfma_f32_16x16x32_bf16 v[136:139], a[64:67], a[8:11], v[136:139]\n" \
  "ds_read_b64_tr_b16 a[86:87], v24 offset:9216\n" \
  "v_mfma_f32_16x16x32_bf16 v[192:195], a[64:67], a[16:19], v[192:195]\n" \
  "v_mfma_f32_16x16x32_bf16 v[192:195], a[68:71], a[20:23], v[192:195]\n" \
  "v_mfma_f32_16x16x32_bf16 v[248:251], a[68:71], a[28:31], v[248:251]\n" \
  "v_mfma_f32_16x16x32_bf16 v[248:251], a[64:67], a[24:27], v[248:251]\n" \
  "s_waitcnt lgkmcnt(4)\n" \
  "v_mfma_f32_16x16x32_bf16 v[252:255], a[72:75], a[24:27], v[252:255]\n" \
  "ds_read_b64_tr_b16 a[88:89], v24 offset:16384\n" \
  "v_mfma_f32_16x16x32_bf16 v[252:255], a[76:79], a[28:31], v[252:255]\n" \
  "ds_read_b64_tr_b16 a[90:91], v24 offset:17408\n" \
  "v_mfma_f32_16x16x32_bf16 v[196:199], a[76:79], a[20:23], v[196:199]\n" \
  "ds_read_b64_tr_b16 a[92:93], v24 offset:24576\n" \
  "v_mfma_f32_16x16x32_bf16 v[196:199], a[72:75], a[16:19], v[196:199]\n" \
  "ds_read_b64_tr_b16 a[94:95], v24 offset:25600\n" \
  "v_mfma_f32_16x16x32_bf16 v[140:143], a[72:75], a[8:11], v[140:143]\n" \
  "v_mfma_f32_16x16x32_bf16 v[140:143], a[76:79], a[12:15], v[140:143]\n" \
  "v_mfma_f32_16x16x32_bf16 v[84:87], a[76:79], a[4:7], v[84:87]\n" \
  "v_mfma_f32_16x16x32_bf16 v[84:87], a[72:75], a[0:3], v[84:87]\n" \
  "s_waitcnt lgkmcnt(4)\n" \
  "v_mfma_f32_16x16x32_bf16 v[32:35], a[80:83], a[32:35], v[32:35]\n" \
  "ds_read_b64_tr_b16 a[64:65], v25 offset:0\n" \
  "v_mfma_f32_16x16x32_bf16 v[32:35], a[84:87], a[36:39], v[32:35]\n" \
  "ds_read_b64_tr_b16 a[66:67], v25 offset:1024\n" \
  "v_mfma_f32_16x16x32_bf16 v[88:91], a[84:87], a[44:47], v[88:91]\n" \
  "ds_read_b64_tr_b16 a[68:69], v25 offset:8192\n" \
  "v_mfma_f32_16x16x32_bf16 v[88:91], a[80:83], a[40:43], v[88:91]\n" \
  "ds_read_b64_tr_b16 a[70:71], v25 offset:9216\n" \
  "v_mfma_f32_16x16x32_bf16 v[144:147], a[80:83], a[48:51], v[144:147]\n" \
  "buffer_load_dwordx4 a[0:3], %[voa], s[40:43], s52 offen offset:0 sc1\n" \
  "s_add_u32 s60, s60, 32768\n" \
  "v_mfma_f32_16x16x32_bf16 v[144:147], a[84:87], a[52:55], v[144:147]\n" \
  "s_and_b32 s60, s60, 131071\n" \
  "v_mfma_f32_16x16x32_bf16 v[200:203], a[84:87], a[60:63], v[200:203]\n" \
  "buffer_load_dwordx4 a[4:7], %[voa], s[40:43], s52 offen offset:1024 sc1\n" \
  "v_add_u32 v24, s60, %[vl0]\n" \
  "v_mfma_f32_16x16x32_bf16 v[200:203], a[80:83], a[56:59], v[200:203]\n" \
  "s_add_u32 s52, s52, 2048\n" \
  "s_waitcnt lgkmcnt(4)\n" \
  "v_mfma_f32_16x16x32_bf16 v[204:207], a[88:91], a[56:59], v[204:207]\n" \
  "ds_read_b64_tr_b16 a[72:73], v25 offset:16384\n" \
  "v_mfma_f32_16x16x32_bf16 v[204:207], a[92:95], a[60:63], v[204:207]\n" \
  "ds_read_b64_tr_b16 a[74:75], v25 offset:17408\n" \
  "v_mfma_f32_16x16x32_bf16 v[148:151], a[92:95], a[52:55], v[148:151]\n" \
  "ds_read_b64_tr_b16 a[76:77], v25 offset:24576\n" \
  "v_mfma_f32_16x16x32_bf16 v[148:151], a[88:91], a[48:51], v[148:151]\n" \
  "ds_read_b64_tr_b16 a[78:79], v25 offset:25600\n" \
  "v_mfma_f32_16x16x32_bf16 v[92:95], a[88:91], a[40:43], v[92:95]\n" \
  "buffer_load_dwordx4 a[8:11], %[voa], s[40:43], s53 offen offset:0 sc1\n" \
  "v_add_u32 v25, s60, %[vl1]\n" \
  "v_mfma_f32_16x16x32_bf16 v[92:95], a[92:95], a[44:47], v[92:95]\n" \
  "v_mfma_f32_16x16x32_bf16 v[36:39], a[92:95], a[36:39], v[36:39]\n" \
  "v_mfma_f32_16x16x32_bf16 v[36:39], a[88:91], a[32:35], v[36:39]\n" \
  "s_waitcnt lgkmcnt(4)\n" \
  "v_mfma_f32_16x16x32_bf16 v[40:43], a[64:67], a[32:35], v[40:43]\n" \
  "ds_read_b64_tr_b16 a[80:81], v26 offset:0\n" \
  "v_mfma_f32_16x16x32_bf16 v[40:43], a[68:71], a[36:39], v[40:43]\n" \
  "ds_read_b64_tr_b16 a[82:83], v26 offset:1024\n" \
  "v_mfma_f32_16x16x32_bf16 v[96:99], a[68:71], a[44:47], v[96:99]\n" \
  "ds_read_b64_tr_b16 a[84:85], v26 offset:8192\n" \
  "v_mfma_f32_16x16x32_bf16 v[96:99], a[64:67], a[40:43], v[96:99]\n" \
  "ds_read_b64_tr_b16 a[86:87], v26 offset:9216\n" \
  "v_mfma_f32_16x16x32_bf16 v[152:155], a[64:67], a[48:51], v[152:155]\n" \
  "buffer_load_dwordx4 a[12:15], %[voa], s[40:43], s53 offen offset:1024 sc1\n" \
  "v_mfma_f32_16x16x32_bf16 v[152:155], a[68:71], a[52:55], v[152:155]\n" \
  "s_add_u32 s53, s53, 2048\n" \
  "v_mfma_f32_16x16x32_bf16 v[208:211], a[68:71], a[60:63], v[208:211]\n" \
  "buffer_load_dwordx4 a[16:19], %[voa], s[40:43], s54 offen offset:0 sc1\n" \
  "v_mfma_f32_16x16x32_bf16 v[208:211], a[64:67], a[56:59], v[208:211]\n" \
  "s_waitcnt lgkmcnt(4)\n" \
  "v_mfma_f32_16x16x32_bf16 v[212:215], a[72:75], a[56:59], v[212:215]\n" \
  "ds_read_b64_tr_b16 a[88:89], v26 offset:16384\n" \
  "v_mfma_f32_16x16x32_bf16 v[212:215], a[76:79], a[60:63], v[212:215]\n" \
  "ds_read_b64_tr_b16 a[90:91], v26 offset:17408\n" \
  "v_mfma_f32_16x16x32_bf16 v[156:159], a[76:79], a[52:55], v[156:159]\n" \
  "ds_read_b64_tr_b16 a[92:93], v26 offset:24576\n" \
  "v_mfma_f32_16x16x32_bf16 v[156:159], a[72:75], a[48:51], v[156:159]\n" \
  "ds_read_b64_tr_b16 a[94:95], v26 offset:25600\n" \
  "v_mfma_f32_16x16x32_bf16 v[100:103], a[72:75], a[40:43], v[100:103]\n" \
  "v_add_u32 v26, s60, %[vl2]\n" \
  "v_mfma_f32_16x16x32_bf16 v[100:103], a[76:79], a[44:47], v[100:103]\n" \
  "v_mfma_f32_16x16x32_bf16 v[44:47], a[76:79], a[36:39], v[44:47]\n" \
  "buffer_load_dwordx4 a[20:23], %[voa], s[40:43], s54 offen offset:1024 sc1\n" \
  "v_mfma_f32_16x16x32_bf16 v[44:47], a[72:75], a[32:35], v[44:47]\n" \
  "s_add_u32 s54, s54, 2048\n" \
  "s_waitcnt lgkmcnt(4)\n" \
  "v_mfma_f32_16x16x32_bf16 v[48:51], a[80:83], a[32:35], v[48:51]\n" \
  "ds_read_b64_tr_b16 a[64:65], v27 offset:0\n" \
  "v_mfma_f32_16x16x32_bf16 v[48:51], a[84:87], a[36:39], v[48:51]\n" \
  "ds_read_b64_tr_b16 a[66:67], v27 offset:1024\n" \
  "v_mfma_f32_16x16x32_bf16 v[104:107], a[84:87], a[44:47], v[104:107]\n" \
  "ds_read_b64_tr_b16 a[68:69], v27 offset:8192\n" \
  "v_mfma_f32_16x16x32_bf16 v[104:107], a[80:83], a[40:43], v[104:107]\n" \
  "ds_read_b64_tr_b16 a[70:71], v27 offset:9216\n" \
  "v_mfma_f32_16x16x32_bf16 v[160:163], a[80:83], a[48:51], v[160:163]\n" \
  "buffer_load_dwordx4 a[24:27], %[voa], s[40:43], s55 offen offset:0 sc1\n" \
  "v_mfma_f32_16x16x32_bf16 v[160:163], a[84:87], a[52:55], v[160:163]\n" \
  "v_mfma_f32_16x16x32_bf16 v[216:219], a[84:87], a[60:63], v[216:219]\n" \
  "buffer_load_dwordx4 a[28:31], %[voa], s[40:43], s55 offen offset:1024 sc1\n" \
  "v_mfma_f32_16x16x32_bf16 v[216:219], a[80:83], a[56:59], v[216:219]\n" \
  "s_add_u32 s55, s55, 2048\n" \
  "s_waitcnt lgkmcnt(4)\n" \
  "v_mfma_f32_16x16x32_bf16 v[220:223], a[88:91], a[56:59], v[220:223]\n" \
  "ds_read_b64_tr_b16 a[72:73], v27 offset:16384\n" \
  "v_mfma_f32_16x16x32_bf16 v[220:223], a[92:95], a[60:63], v[220:223]\n" \
  "ds_read_b64_tr_b16 a[74:75], v27 offset:17408\n" \
  "v_mfma_f32_16x16x32_bf16 v[164:167], a[92:95], a[52:55], v[164:167]\n" \
  "ds_read_b64_tr_b16 a[76:77], v27 offset:24576\n" \
  "v_mfma_f32_16x16x32_bf16 v[164:167], a[88:91], a[48:51], v[164:167]\n" \
  "ds_read_b64_tr_b16 a[78:79], v27 offset:25600\n" \
  "v_mfma_f32_16x16x32_bf16 v[108:111], a[88:91], a[40:43], v[108:111]\n" \
  "v_add_u32 v27, s60, %[vl3]\n" \
  "v_mfma_f32_16x16x32_bf16 v[108:111], a[92:95], a[44:47], v[108:111]\n" \
  "s_add_u32 m0, s61, 0\n" \
  "v_mfma_f32_16x16x32_bf16 v[52:55], a[92:95], a[36:39], v[52:55]\n" \
  "buffer_load_dwordx4 %[vow0], s[44:47], s56 offen lds\n" \
  "v_mfma_f32_16x16x32_bf16 v[52:55], a[88:91], a[32:35], v[52:55]\n" \
  "s_waitcnt lgkmcnt(4)\n" \
  "v_mfma_f32_16x16x32_bf16 v[56:59], a[64:67], a[32:35], v[56:59]\n" \
  "ds_read_b64_tr_b16 a[80:81], v28 offset:0\n" \
  "v_mfma_f32_16x16x32_bf16 v[56:59], a[68:71], a[36:39], v[56:59]\n" \
  "ds_read_b64_tr_b16 a[82:83], v28 offset:1024\n" \
  "v_mfma_f32_16x16x32_bf16 v[112:115], a[68:71], a[44:47], v[112:115]\n" \
  "ds_read_b64_tr_b16 a[84:85], v28 offset:8192\n" \
  "v_mfma_f32_16x16x32_bf16 v[112:115], a[64:67], a[40:43], v[112:115]\n" \
  "ds_read_b64_tr_b16 a[86:87], v28 offset:9216\n" \
  "s_add_u32 m0, s61, 1024\n" \
  "v_mfma_f32_16x16x32_bf16 v[168:171], a[64:67], a[48:51], v[168:171]\n" \
  "buffer_load_dwordx4 %[vow0], s[44:47], s57 offen lds\n" \
  "v_mfma_f32_16x16x32_bf16 v[168:171], a[68:71], a[52:55], v[168:171]\n" \
  "v_mfma_f32_16x16x32_bf16 v[224:227], a[68:71], a[60:63], v[224:227]\n" \
  "v_mfma_f32_16x16x32_bf16 v[224:227], a[64:67], a[56:59], v[224:227]\n" \
  "s_waitcnt lgkmcnt(4)\n" \
  "v_mfma_f32_16x16x32_bf16 v[228:231], a[72:75], a[56:59], v[228:231]\n" \
  "ds_read_b64_tr_b16 a[88:89], v28 offset:16384\n" \
  "v_mfma_f32_16x16x32_bf16 v[228:231], a[76:79], a[60:63], v[228:231]\n" \
  "ds_read_b64_tr_b16 a[90:91], v28 offset:17408\n" \
  "v_mfma_f32_16x16x32_bf16 v[172:175], a[76:79], a[52:55], v[172:175]\n" \
  "ds_read_b64_tr_b16 a[92:93], v28 offset:24576\n" \
  "v_mfma_f32_16x16x32_bf16 v[172:175], a[72:75], a[48:51], v[172:175]\n" \
  "ds_read_b64_tr_b16 a[94:95], v28 offset:25600\n" \
  "s_add_u32 m0, s61, 2048\n" \
  "v_mfma_f32_16x16x32_bf16 v[116:119], a[72:75], a[40:43], v[116:119]\n" \
  "buffer_load_dwordx4 %[vow1], s[44:47], s58 offen lds\n" \
  "v_add_u32 v28, s60, %[vl4]\n" \
  "v_mfma_f32_16x16x32_bf16 v[116:119], a[76:79], a[44:47], v[116:119]\n" \
  "s_add_u32 m0, s61, 3072\n" \
  "v_mfma_f32_16x16x32_bf16 v[60:63], a[76:79], a[36:39], v[60:63]\n" \
  "buffer_load_dwordx4 %[vow1], s[44:47], s59 offen lds\n" \
  "v_mfma_f32_16x16x32_bf16 v[60:63], a[72:75], a[32:35], v[60:63]\n" \
  "s_waitcnt lgkmcnt(4)\n" \
  "v_mfma_f32_16x16x32_bf16 v[64:67], a[80:83], a[32:35], v[64:67]\n" \
  "ds_read_b64_tr_b16 a[64:65], v29 offset:0\n" \
  "v_mfma_f32_16x16x32_bf16 v[64:67], a[84:87], a[36:39], v[64:67]\n" \
  "ds_read_b64_tr_b16 a[66:67], v29 offset:1024\n" \
  "v_mfma_f32_16x16x32_bf16 v[120:123], a[84:87], a[44:47], v[120:123]\n" \
  "ds_read_b64_tr_b16 a[68:69], v29 offset:8192\n" \
  "v_mfma_f32_16x16x32_bf16 v[120:123], a[80:83], a[40:43], v[120:123]\n" \
  "ds_read_b64_tr_b16 a[70:71], v29 offset:9216\n" \
  "s_add_u32 m0, s61, 16384\n" \
  "v_mfma_f32_16x16x32_bf16 v[176:179], a[80:83], a[48:51], v[176:179]\n" \
  "buffer_load_dwordx4 %[vow0], s[48:51], s56 offen lds\n" \
  "v_mfma_f32_16x16x32_bf16 v[176:179], a[84:87], a[52:55], v[176:179]\n" \
  "s_add_u32 s56, s56, 458752\n" \
  "v_mfma_f32_16x16x32_bf16 v[232:235], a[84:87], a[60:63], v[232:235]\n" \
  "v_mfma_f32_16x16x32_bf16 v[232:235], a[80:83], a[56:59], v[232:235]\n" \
  "s_waitcnt lgkmcnt(4)\n" \
  "v_mfma_f32_16x16x32_bf16 v[236:239], a[88:91], a[56:59], v[236:239]\n" \
  "ds_read_b64_tr_b16 a[72:73], v29 offset:16384\n" \
  "v_mfma_f32_16x16x32_bf16 v[236:239], a[92:95], a[60:63], v[236:239]\n" \
  "ds_read_b64_tr_b16 a[74:75], v29 offset:17408\n" \
  "v_mfma_f32_16x16x32_bf16 v[180:183], a[92:95], a[52:55], v[180:183]\n" \
  "ds_read_b64_tr_b16 a[76:77], v29 offset:24576\n" \
  "v_mfma_f32_16x16x32_bf16 v[180:183], a[88:91], a[48:51], v[180:183]\n" \
  "ds_read_b64_tr_b16 a[78:79], v29 offset:25600\n" \
  "s_add_u32 m0, s61, 17408\n" \
  "v_mfma_f32_16x16x32_bf16 v[124:127], a[88:91], a[40:43], v[124:127]\n" \
  "buffer_load_dwordx4 %[vow0], s[48:51], s57 offen lds\n" \
  "v_add_u32 v29, s60, %[vl5]\n" \
  "v_mfma_f32_16x16x32_bf16 v[124:127], a[92:95], a[44:47], v[124:127]\n" \
  "s_add_u32 m0, s61, 18432\n" \
  "s_add_u32 s57, s57, 458752\n" \
  "v_mfma_f32_16x16x32_bf16 v[68:71], a[92:95], a[36:39], v[68:71]\n" \
  "buffer_load_dwordx4 %[vow1], s[48:51], s58 offen lds\n" \
  "v_mfma_f32_16x16x32_bf16 v[68:71], a[88:91], a[32:35], v[68:71]\n" \
  "s_add_u32 s58, s58, 458752\n" \
  "s_waitcnt lgkmcnt(4)\n" \
  "v_mfma_f32_16x16x32_bf16 v[72:75], a[64:67], a[32:35], v[72:75]\n" \
  "ds_read_b64_tr_b16 a[80:81], v30 offset:0\n" \
  "v_mfma_f32_16x16x32_bf16 v[72:75], a[68:71], a[36:39], v[72:75]\n" \
  "ds_read_b64_tr_b16 a[82:83], v30 offset:1024\n" \
  "v_mfma_f32_16x16x32_bf16 v[128:131], a[68:71], a[44:47], v[128:131]\n" \
  "ds_read_b64_tr_b16 a[84:85], v30 offset:8192\n" \
  "v_mfma_f32_16x16x32_bf16 v[128:131], a[64:67], a[40:43], v[128:131]\n" \
  "ds_read_b64_tr_b16 a[86:87], v30 offset:9216\n" \
  "v_mfma_f32_16x16x32_bf16 v[184:187], a[64:67], a[48:51], v[184:187]\n" \
  "v_mfma_f32_16x16x32_bf16 v[184:187], a[68:71], a[52:55], v[184:187]\n" \
  "s_add_u32 m0, s61, 19456\n" \
  "v_mfma_f32_16x16x32_bf16 v[240:243], a[68:71], a[60:63], v[240:243]\n" \
  "buffer_load_dwordx4 %[vow1], s[48:51], s59 offen lds\n" \
  "v_mfma_f32_16x16x32_bf16 v[240:243], a[64:67], a[56:59], v[240:243]\n" \
  "s_add_u32 s59, s59, 458752\n" \
  "s_waitcnt lgkmcnt(4)\n" \
  "v_mfma_f32_16x16x32_bf16 v[244:247], a[72:75], a[56:59], v[244:247]\n" \
  "ds_read_b64_tr_b16 a[88:89], v30 offset:16384\n" \
  "s_add_u32 s61, s61, 32768\n" \
  "v_mfma_f32_16x16x32_bf16 v[244:247], a[76:79], a[60:63], v[244:247]\n" \
  "ds_read_b64_tr_b16 a[90:91], v30 offset:17408\n" \
  "s_and_b32 s61, s61, 131071\n" \
  "v_mfma_f32_16x16x32_bf16 v[188:191], a[76:79], a[52:55], v[188:191]\n" \
  "ds_read_b64_tr_b16 a[92:93], v30 offset:24576\n" \
  "v_mfma_f32_16x16x32_bf16 v[188:191], a[72:75], a[48:51], v[188:191]\n" \
  "ds_read_b64_tr_b16 a[94:95], v30 offset:25600\n" \
  "s_mov_b32 m0, 163584\n" \
  "v_mfma_f32_16x16x32_bf16 v[132:135], a[72:75], a[40:43], v[132:135]\n" \
  "buffer_load_dword %[vt], %[srdt], 0 offen lds\n" \
  "v_add_u32 v30, s60, %[vl6]\n" \
  "v_mfma_f32_16x16x32_bf16 v[132:135], a[76:79], a[44:47], v[132:135]\n" \
  "v_add_u32 %[vt], %[st], %[vt]\n" \
  "v_mfma_f32_16x16x32_bf16 v[76:79], a[76:79], a[36:39], v[76:79]\n" \
  "v_mfma_f32_16x16x32_bf16 v[76:79], a[72:75], a[32:35], v[76:79]\n" \
  "s_waitcnt vmcnt(9) lgkmcnt(4)\n" \
  "s_barrier\n" \
  "v_mfma_f32_16x16x32_bf16 v[80:83], a[80:83], a[32:35], v[80:83]\n" \
  "ds_read_b64_tr_b16 a[64:65], v24 offset:0\n" \
  "v_mfma_f32_16x16x32_bf16 v[80:83], a[84:87], a[36:39], v[80:83]\n" \
  "ds_read_b64_tr_b16 a[66:67], v24 offset:1024\n" \
  "v_mfma_f32_16x16x32_bf16 v[136:139], a[84:87], a[44:47], v[136:139]\n" \
  "ds_read_b64_tr_b16 a[68:69], v24 offset:8192\n" \
  "v_mfma_f32_16x16x32_bf16 v[136:139], a[80:83], a[40:43], v[136:139]\n" \
  "ds_read_b64_tr_b16 a[70:71], v24 offset:9216\n" \
  "v_mfma_f32_16x16x32_bf16 v[192:195], a[80:83], a[48:51], v[192:195]\n" \
  "v_mfma_f32_16x16x32_bf16 v[192:195], a[84:87], a[52:55], v[192:195]\n" \
  "v_mfma_f32_16x16x32_bf16 v[248:251], a[84:87], a[60:63], v[248:251]\n" \
  "v_mfma_f32_16x16x32_bf16 v[248:251], a[80:83], a[56:59], v[248:251]\n" \
  "s_waitcnt lgkmcnt(4)\n" \
  "v_mfma_f32_16x16x32_bf16 v[252:255], a[88:91], a[56:59], v[252:255]\n" \
  "ds_read_b64_tr_b16 a[72:73], v24 offset:16384\n" \
  "v_mfma_f32_16x16x32_bf16 v[252:255], a[92:95], a[60:63], v[252:255]\n" \
  "ds_read_b64_tr_b16 a[74:75], v24 offset:17408\n" \
  "v_mfma_f32_16x16x32_bf16 v[196:199], a[92:95], a[52:55], v[196:199]\n" \
  "ds_read_b64_tr_b16 a[76:77], v24 offset:24576\n" \
  "v_mfma_f32_16x16x32_bf16 v[196:199], a[88:91], a[48:51], v[196:199]\n" \
  "ds_read_b64_tr_b16 a[78:79], v24 offset:25600\n" \
  "v_mfma_f32_16x16x32_bf16 v[140:143], a[88:91], a[40:43], v[140:143]\n" \
  "v_mfma_f32_16x16x32_bf16 v[140:143], a[92:95], a[44:47], v[140:143]\n" \
  "v_mfma_f32_16x16x32_bf16 v[84:87], a[92:95], a[36:39], v[84:87]\n" \
  "v_mfma_f32_16x16x32_bf16 v[84:87], a[88:91], a[32:35], v[84:87]\n" \
  "s_sub_u32 s64, s64, 1\n" \
  "s_cbranch_scc0 Lloop%=\n" \
  "s_waitcnt lgkmcnt(4)\n" \
  "v_mfma_f32_16x16x32_bf16 v[32:35], a[64:67], a[0:3], v[32:35]\n" \
  "ds_read_b64_tr_b16 a[80:81], v25 offset:0\n" \
  "v_mfma_f32_16x16x32_bf16 v[32:35], a[68:71], a[4:7], v[32:35]\n" \
  "ds_read_b64_tr_b16 a[82:83], v25 offset:1024\n" \
  "v_mfma_f32_16x16x32_bf16 v[88:91], a[68:71], a[12:15], v[88:91]\n" \
  "ds_read_b64_tr_b16 a[84:85], v25 offset:8192\n" \
  "v_mfma_f32_16x16x32_bf16 v[88:91], a[64:67], a[8:11], v[88:91]\n" \
  "ds_read_b64_tr_b16 a[86:87], v25 offset:9216\n" \
  "v_mfma_f32_16x16x32_bf16 v[144:147], a[64:67], a[16:19], v[144:147]\n" \
  "buffer_load_dwordx4 a[32:35], %[voa], s[40:43], s52 offen offset:0 sc1\n" \
  "s_add_u32 s60, s60, 32768\n" \
  "v_mfma_f32_16x16x32_bf16 v[144:147], a[68:71], a[20:23], v[144:147]\n" \
  "s_and_b32 s60, s60, 131071\n" \
  "v_mfma_f32_16x16x32_bf16 v[200:203], a[68:71], a[28:31], v[200:203]\n" \
  "v_add_u32 v24, s60, %[vl0]\n" \
  "v_mfma_f32_16x16x32_bf16 v[200:203], a[64:67], a[24:27], v[200:203]\n" \
  "s_waitcnt lgkmcnt(4)\n" \
  "v_mfma_f32_16x16x32_bf16 v[204:207], a[72:75], a[24:27], v[204:207]\n" \
  "ds_read_b64_tr_b16 a[88:89], v25 offset:16384\n" \
  "v_mfma_f32_16x16x32_bf16 v[204:207], a[76:79], a[28:31], v[204:207]\n" \
  "ds_read_b64_tr_b16 a[90:91], v25 offset:17408\n" \
  "v_mfma_f32_16x16x32_bf16 v[148:151], a[76:79], a[20:23], v[148:151]\n" \
  "ds_read_b64_tr_b16 a[92:93], v25 offset:24576\n" \
  "v_mfma_f32_16x16x32_bf16 v[148:151], a[72:75], a[16:19], v[148:151]\n" \
  "ds_read_b64_tr_b16 a[94:95], v25 offset:25600\n" \
  "v_mfma_f32_16x16x32_bf16 v[92:95], a[72:75], a[8:11], v[92:95]\n" \
  "buffer_load_dwordx4 a[36:39], %[voa], s[40:43], s52 offen offset:1024 sc1\n" \
  "v_add_u32 v25, s60, %[vl1]\n" \
  "v_mfma_f32_16x16x32_bf16 v[92:95], a[76:79], a[12:15], v[92:95]\n" \
  "s_add_u32 s52, s52, 2048\n" \
  "v_mfma_f32_16x16x32_bf16 v[36:39], a[76:79], a[4:7], v[36:39]\n" \
  "v_mfma_f32_16x16x32_bf16 v[36:39], a[72:75], a[0:3], v[36:39]\n" \
  "s_waitcnt lgkmcnt(4)\n" \
  "v_mfma_f32_16x16x32_bf16 v[40:43], a[80:83], a[0:3], v[40:43]\n" \
  "ds_read_b64_tr_b16 a[64:65], v26 offset:0\n" \
  "v_mfma_f32_16x16x32_bf16 v[40:43], a[84:87], a[4:7], v[40:43]\n" \
  "ds_read_b64_tr_b16 a[66:67], v26 offset:1024\n" \
  "v_mfma_f32_16x16x32_bf16 v[96:99], a[84:87], a[12:15], v[96:99]\n" \
  "ds_read_b64_tr_b16 a[68:69], v26 offset:8192\n" \
  "v_mfma_f32_16x16x32_bf16 v[96:99], a[80:83], a[8:11], v[96:99]\n" \
  "ds_read_b64_tr_b16 a[70:71], v26 offset:9216\n" \
  "v_mfma_f32_16x16x32_bf16 v[152:155], a[80:83], a[16:19], v[152:155]\n" \
  "v_mfma_f32_16x16x32_bf16 v[152:155], a[84:87], a[20:23], v[152:155]\n" \
  "v_mfma_f32_16x16x32_bf16 v[208:211], a[84:87], a[28:31], v[208:211]\n" \
  "buffer_load_dwordx4 a[40:43], %[voa], s[40:43], s53 offen offset:0 sc1\n" \
  "v_mfma_f32_16x16x32_bf16 v[208:211], a[80:83], a[24:27], v[208:211]\n" \
  "s_waitcnt lgkmcnt(4)\n" \
  "v_mfma_f32_16x16x32_bf16 v[212:215], a[88:91], a[24:27], v[212:215]\n" \
  "ds_read_b64_tr_b16 a[72:73], v26 offset:16384\n" \
  "v_mfma_f32_16x16x32_bf16 v[212:215], a[92:95], a[28:31], v[212:215]\n" \
  "ds_read_b64_tr_b16 a[74:75], v26 offset:17408\n" \
  "v_mfma_f32_16x16x32_bf16 v[156:159], a[92:95], a[20:23], v[156:159]\n" \
  "ds_read_b64_tr_b16 a[76:77], v26 offset:24576\n" \
  "v_mfma_f32_16x16x32_bf16 v[156:159], a[88:91], a[16:19], v[156:159]\n" \
  "ds_read_b64_tr_b16 a[78:79], v26 offset:25600\n" \
  "v_mfma_f32_16x16x32_bf16 v[100:103], a[88:91], a[8:11], v[100:103]\n" \
  "v_add_u32 v26, s60, %[vl2]\n" \
  "v_mfma_f32_16x16x32_bf16 v[100:103], a[92:95], a[12:15], v[100:103]\n" \
  "v_mfma_f32_16x16x32_bf16 v[44:47], a[92:95], a[4:7], v[44:47]\n" \
  "v_mfma_f32_16x16x32_bf16 v[44:47], a[88:91], a[0:3], v[44:47]\n" \
  "s_waitcnt lgkmcnt(4)\n" \
  "v_mfma_f32_16x16x32_bf16 v[48:51], a[64:67], a[0:3], v[48:51]\n" \
  "ds_read_b64_tr_b16 a[80:81], v27 offset:0\n" \
  "v_mfma_f32_16x16x32_bf16 v[48:51], a[68:71], a[4:7], v[48:51]\n" \
  "ds_read_b64_tr_b16 a[82:83], v27 offset:1024\n" \
  "v_mfma_f32_16x16x32_bf16 v[104:107], a[68:71], a[12:15], v[104:107]\n" \
  "ds_read_b64_tr_b16 a[84:85], v27 offset:8192\n" \
  "v_mfma_f32_16x16x32_bf16 v[104:107], a[64:67], a[8:11], v[104:107]\n" \
  "ds_read_b64_tr_b16 a[86:87], v27 offset:9216\n" \
  "v_mfma_f32_16x16x32_bf16 v[160:163], a[64:67], a[16:19], v[160:163]\n" \
  "buffer_load_dwordx4 a[44:47], %[voa], s[40:43], s53 offen offset:1024 sc1\n" \
  "v_mfma_f32_16x16x32_bf16 v[160:163], a[68:71], a[20:23], v[160:163]\n" \
  "s_add_u32 s53, s53, 2048\n" \
  "v_mfma_f32_16x16x32_bf16 v[216:219], a[68:71], a[28:31], v[216:219]\n" \
  "v_mfma_f32_16x16x32_bf16 v[216:219], a[64:67], a[24:27], v[216:219]\n" \
  "s_waitcnt lgkmcnt(4)\n" \
  "v_mfma_f32_16x16x32_bf16 v[220:223], a[72:75], a[24:27], v[220:223]\n" \
  "ds_read_b64_tr_b16 a[88:89], v27 offset:16384\n" \
  "v_mfma_f32_16x16x32_bf16 v[220:223], a[76:79], a[28:31], v[220:223]\n" \
  "ds_read_b64_tr_b16 a[90:91], v27 offset:17408\n" \
  "v_mfma_f32_16x16x32_bf16 v[164:167], a[76:79], a[20:23], v[164:167]\n" \
  "ds_read_b64_tr_b16 a[92:93], v27 offset:24576\n" \
  "v_mfma_f32_16x16x32_bf16 v[164:167], a[72:75], a[16:19], v[164:167]\n" \
  "ds_read_b64_tr_b16 a[94:95], v27 offset:25600\n" \
  "v_mfma_f32_16x16x32_bf16 v[108:111], a[72:75], a[8:11], v[108:111]\n" \
  "buffer_load_dwordx4 a[48:51], %[voa], s[40:43], s54 offen offset:0 sc1\n" \
  "v_add_u32 v27, s60, %[vl3]\n" \
  "v_mfma_f32_16x16x32_bf16 v[108:111], a[76:79], a[12:15], v[108:111]\n" \
  "v_mfma_f32_16x16x32_bf16 v[52:55], a[76:79], a[4:7], v[52:55]\n" \
  "v_mfma_f32_16x16x32_bf16 v[52:55], a[72:75], a[0:3], v[52:55]\n" \
  "s_waitcnt lgkmcnt(4)\n" \
  "v_mfma_f32_16x16x32_bf16 v[56:59], a[80:83], a[0:3], v[56:59]\n" \
  "ds_read_b64_tr_b16 a[64:65], v28 offset:0\n" \
  "v_mfma_f32_16x16x32_bf16 v[56:59], a[84:87], a[4:7], v[56:59]\n" \
  "ds_read_b64_tr_b16 a[66:67], v28 offset:1024\n" \
  "v_mfma_f32_16x16x32_bf16 v[112:115], a[84:87], a[12:15], v[112:115]\n" \
  "ds_read_b64_tr_b16 a[68:69], v28 offset:8192\n" \
  "v_mfma_f32_16x16x32_bf16 v[112:115], a[80:83], a[8:11], v[112:115]\n" \
  "ds_read_b64_tr_b16 a[70:71], v28 offset:9216\n" \
  "v_mfma_f32_16x16x32_bf16 v[168:171], a[80:83], a[16:19], v[168:171]\n" \
  "v_mfma_f32_16x16x32_bf16 v[168:171], a[84:87], a[20:23], v[168:171]\n" \
  "v_mfma_f32_16x16x32_bf16 v[224:227], a[84:87], a[28:31], v[224:227]\n" \
  "buffer_load_dwordx4 a[52:55], %[voa], s[40:43], s54 offen offset:1024 sc1\n" \
  "v_mfma_f32_16x16x32_bf16 v[224:227], a[80:83], a[24:27], v[224:227]\n" \
  "s_add_u32 s54, s54, 2048\n" \
  "s_waitcnt lgkmcnt(4)\n" \
  "v_mfma_f32_16x16x32_bf16 v[228:231], a[88:91], a[24:27], v[228:231]\n" \
  "ds_read_b64_tr_b16 a[72:73], v28 offset:16384\n" \
  "v_mfma_f32_16x16x32_bf16 v[228:231], a[92:95], a[28:31], v[228:231]\n" \
  "ds_read_b64_tr_b16 a[74:75], v28 offset:17408\n" \
  "v_mfma_f32_16x16x32_bf16 v[172:175], a[92:95], a[20:23], v[172:175]\n" \
  "ds_read_b64_tr_b16 a[76:77], v28 offset:24576\n" \
  "v_mfma_f32_16x16x32_bf16 v[172:175], a[88:91], a[16:19], v[172:175]\n" \
  "ds_read_b64_tr_b16 a[78:79], v28 offset:25600\n" \
  "v_mfma_f32_16x16x32_bf16 v[116:119], a[88:91], a[8:11], v[116:119]\n" \
  "v_add_u32 v28, s60, %[vl4]\n" \
  "v_mfma_f32_16x16x32_bf16 v[116:119], a[92:95], a[12:15], v[116:119]\n" \
  "v_mfma_f32_16x16x32_bf16 v[60:63], a[92:95], a[4:7], v[60:63]\n" \
  "v_mfma_f32_16x16x32_bf16 v[60:63], a[88:91], a[0:3], v[60:63]\n" \
  "s_waitcnt lgkmcnt(4)\n" \
  "v_mfma_f32_16x16x32_bf16 v[64:67], a[64:67], a[0:3], v[64:67]\n" \
  "ds_read_b64_tr_b16 a[80:81], v29 offset:0\n" \
  "v_mfma_f32_16x16x32_bf16 v[64:67], a[68:71], a[4:7], v[64:67]\n" \
  "ds_read_b64_tr_b16 a[82:83], v29 offset:1024\n" \
  "v_mfma_f32_16x16x32_bf16 v[120:123], a[68:71], a[12:15], v[120:123]\n" \
  "ds_read_b64_tr_b16 a[84:85], v29 offset:8192\n" \
  "v_mfma_f32_16x16x32_bf16 v[120:123], a[64:67], a[8:11], v[120:123]\n" \
  "ds_read_b64_tr_b16 a[86:87], v29 offset:9216\n" \
  "v_mfma_f32_16x16x32_bf16 v[176:179], a[64:67], a[16:19], v[176:179]\n" \
  "buffer_load_dwordx4 a[56:59], %[voa], s[40:43], s55 offen offset:0 sc1\n" \
  "v_mfma_f32_16x16x32_bf16 v[176:179], a[68:71], a[20:23], v[176:179]\n" \
  "v_mfma_f32_16x16x32_bf16 v[232:235], a[68:71], a[28:31], v[232:235]\n" \
  "v_mfma_f32_16x16x32_bf16 v[232:235], a[64:67], a[24:27], v[232:235]\n" \
  "s_waitcnt lgkmcnt(4)\n" \
  "v_mfma_f32_16x16x32_bf16 v[236:239], a[72:75], a[24:27], v[236:239]\n" \
  "ds_read_b64_tr_b16 a[88:89], v29 offset:16384\n" \
  "v_mfma_f32_16x16x32_bf16 v[236:239], a[76:79], a[28:31], v[236:239]\n" \
  "ds_read_b64_tr_b16 a[90:91], v29 offset:17408\n" \
  "v_mfma_f32_16x16x32_bf16 v[180:183], a[76:79], a[20:23], v[180:183]\n" \
  "ds_read_b64_tr_b16 a[92:93], v29 offset:24576\n" \
  "v_mfma_f32_16x16x32_bf16 v[180:183], a[72:75], a[16:19], v[180:183]\n" \
  "ds_read_b64_tr_b16 a[94:95], v29 offset:25600\n" \
  "v_mfma_f32_16x16x32_bf16 v[124:127], a[72:75], a[8:11], v[124:127]\n" \
  "buffer_load_dwordx4 a[60:63], %[voa], s[40:43], s55 offen offset:1024 sc1\n" \
  "v_add_u32 v29, s60, %[vl5]\n" \
  "v_mfma_f32_16x16x32_bf16 v[124:127], a[76:79], a[12:15], v[124:127]\n" \
  "s_add_u32 s55, s55, 2048\n" \
  "v_mfma_f32_16x16x32_bf16 v[68:71], a[76:79], a[4:7], v[68:71]\n" \
  "v_mfma_f32_16x16x32_bf16 v[68:71], a[72:75], a[0:3], v[68:71]\n" \
  "s_waitcnt lgkmcnt(4)\n" \
  "v_mfma_f32_16x16x32_bf16 v[72:75], a[80:83], a[0:3], v[72:75]\n" \
  "ds_read_b64_tr_b16 a[64:65], v30 offset:0\n" \
  "v_mfma_f32_16x16x32_bf16 v[72:75], a[84:87], a[4:7], v[72:75]\n" \
  "ds_read_b64_tr_b16 a[66:67], v30 offset:1024\n" \
  "v_mfma_f32_16x16x32_bf16 v[128:131], a[84:87], a[12:15], v[128:131]\n" \
  "ds_read_b64_tr_b16 a[68:69], v30 offset:8192\n" \
  "v_mfma_f32_16x16x32_bf16 v[128:131], a[80:83], a[8:11], v[128:131]\n" \
  "ds_read_b64_tr_b16 a[70:71], v30 offset:9216\n" \
  "v_mfma_f32_16x16x32_bf16 v[184:187], a[80:83], a[16:19], v[184:187]\n" \
  "v_mfma_f32_16x16x32_bf16 v[184:187], a[84:87], a[20:23], v[184:187]\n" \
  "s_mov_b32 m0, 163584\n" \
  "v_mfma_f32_16x16x32_bf16 v[240:243], a[84:87], a[28:31], v[240:243]\n" \
  "buffer_load_dword %[vo3], %[srdo], 0 offen lds\n" \
  "v_mfma_f32_16x16x32_bf16 v[240:243], a[80:83], a[24:27], v[240:243]\n" \
  "s_waitcnt lgkmcnt(4)\n" \
  "v_mfma_f32_16x16x32_bf16 v[244:247], a[88:91], a[24:27], v[244:247]\n" \
  "ds_read_b64_tr_b16 a[72:73], v30 offset:16384\n" \
  "v_mfma_f32_16x16x32_bf16 v[244:247], a[92:95], a[28:31], v[244:247]\n" \
  "ds_read_b64_tr_b16 a[74:75], v30 offset:17408\n" \
  "v_mfma_f32_16x16x32_bf16 v[188:191], a[92:95], a[20:23], v[188:191]\n" \
  "ds_read_b64_tr_b16 a[76:77], v30 offset:24576\n" \
  "v_mfma_f32_16x16x32_bf16 v[188:191], a[88:91], a[16:19], v[188:191]\n" \
  "ds_read_b64_tr_b16 a[78:79], v30 offset:25600\n" \
  "v_mfma_f32_16x16x32_bf16 v[132:135], a[88:91], a[8:11], v[132:135]\n" \
  "v_add_u32 v30, s60, %[vl6]\n" \
  "v_mfma_f32_16x16x32_bf16 v[132:135], a[92:95], a[12:15], v[132:135]\n" \
  "v_mfma_f32_16x16x32_bf16 v[76:79], a[92:95], a[4:7], v[76:79]\n" \
  "v_mfma_f32_16x16x32_bf16 v[76:79], a[88:91], a[0:3], v[76:79]\n" \
  "s_waitcnt vmcnt(1) lgkmcnt(4)\n" \
  "s_barrier\n" \
  "v_mfma_f32_16x16x32_bf16 v[80:83], a[64:67], a[0:3], v[80:83]\n" \
  "ds_read_b64_tr_b16 a[80:81], v24 offset:0\n" \
  "v_mfma_f32_16x16x32_bf16 v[80:83], a[68:71], a[4:7], v[80:83]\n" \
  "ds_read_b64_tr_b16 a[82:83], v24 offset:1024\n" \
  "v_mfma_f32_16x16x32_bf16 v[136:139], a[68:71], a[12:15], v[136:139]\n" \
  "ds_read_b64_tr_b16 a[84:85], v24 offset:8192\n" \
  "v_mfma_f32_16x16x32_bf16 v[136:139], a[64:67], a[8:11], v[136:139]\n" \
  "ds_read_b64_tr_b16 a[86:87], v24 offset:9216\n" \
  "v_mfma_f32_16x16x32_bf16 v[192:195], a[64:67], a[16:19], v[192:195]\n" \
  "v_mfma_f32_16x16x32_bf16 v[192:195], a[68:71], a[20:23], v[192:195]\n" \
  "v_mfma_f32_16x16x32_bf16 v[248:251], a[68:71], a[28:31], v[248:251]\n" \
  "v_mfma_f32_16x16x32_bf16 v[248:251], a[64:67], a[24:27], v[248:251]\n" \
  "s_waitcnt lgkmcnt(4)\n" \
  "v_mfma_f32_16x16x32_bf16 v[252:255], a[72:75], a[24:27], v[252:255]\n" \
  "ds_read_b64_tr_b16 a[88:89], v24 offset:16384\n" \
  "v_mfma_f32_16x16x32_bf16 v[252:255], a[76:79], a[28:31], v[252:255]\n" \
  "ds_read_b64_tr_b16 a[90:91], v24 offset:17408\n" \
  "v_mfma_f32_16x16x32_bf16 v[196:199], a[76:79], a[20:23], v[196:199]\n" \
  "ds_read_b64_tr_b16 a[92:93], v24 offset:24576\n" \
  "v_mfma_f32_16x16x32_bf16 v[196:199], a[72:75], a[16:19], v[196:199]\n" \
  "ds_read_b64_tr_b16 a[94:95], v24 offset:25600\n" \
  "v_mfma_f32_16x16x32_bf16 v[140:143], a[72:75], a[8:11], v[140:143]\n" \
  "v_mfma_f32_16x16x32_bf16 v[140:143], a[76:79], a[12:15], v[140:143]\n" \
  "v_mfma_f32_16x16x32_bf16 v[84:87], a[76:79], a[4:7], v[84:87]\n" \
  "v_mfma_f32_16x16x32_bf16 v[84:87], a[72:75], a[0:3], v[84:87]\n" \
  "s_waitcnt lgkmcnt(4)\n" \
  "v_mfma_f32_16x16x32_bf16 v[32:35], a[80:83], a[32:35], v[32:35]\n" \
  "ds_read_b64_tr_b16 a[64:65], v25 offset:0\n" \
  "v_mfma_f32_16x16x32_bf16 v[32:35], a[84:87], a[36:39], v[32:35]\n" \
  "ds_read_b64_tr_b16 a[66:67], v25 offset:1024\n" \
  "v_mfma_f32_16x16x32_bf16 v[88:91], a[84:87], a[44:47], v[88:91]\n" \
  "ds_read_b64_tr_b16 a[68:69], v25 offset:8192\n" \
  "v_mfma_f32_16x16x32_bf16 v[88:91], a[80:83], a[40:43], v[88:91]\n" \
  "ds_read_b64_tr_b16 a[70:71], v25 offset:9216\n" \
  "v_mfma_f32_16x16x32_bf16 v[144:147], a[80:83], a[48:51], v[144:147]\n" \
  "v_mfma_f32_16x16x32_bf16 v[144:147], a[84:87], a[52:55], v[144:147]\n" \
  "v_mfma_f32_16x16x32_bf16 v[200:203], a[84:87], a[60:63], v[200:203]\n" \
  "v_mfma_f32_16x16x32_bf16 v[200:203], a[80:83], a[56:59], v[200:203]\n" \
  "s_waitcnt lgkmcnt(4)\n" \
  "v_mfma_f32_16x16x32_bf16 v[204:207], a[88:91], a[56:59], v[204:207]\n" \
  "ds_read_b64_tr_b16 a[72:73], v25 offset:16384\n" \
  "v_mfma_f32_16x16x32_bf16 v[204:207], a[92:95], a[60:63], v[204:207]\n" \
  "ds_read_b64_tr_b16 a[74:75], v25 offset:17408\n" \
  "v_mfma_f32_16x16x32_bf16 v[148:151], a[92:95], a[52:55], v[148:151]\n" \
  "ds_read_b64_tr_b16 a[76:77], v25 offset:24576\n" \
  "v_mfma_f32_16x16x32_bf16 v[148:151], a[88:91], a[48:51], v[148:151]\n" \
  "ds_read_b64_tr_b16 a[78:79], v25 offset:25600\n" \
  "v_mfma_f32_16x16x32_bf16 v[92:95], a[88:91], a[40:43], v[92:95]\n" \
  "v_mfma_f32_16x16x32_bf16 v[92:95], a[92:95], a[44:47], v[92:95]\n" \
  "v_mfma_f32_16x16x32_bf16 v[36:39], a[92:95], a[36:39], v[36:39]\n" \
  "v_mfma_f32_16x16x32_bf16 v[36:39], a[88:91], a[32:35], v[36:39]\n" \
  "s_waitcnt lgkmcnt(4)\n" \
  "v_mfma_f32_16x16x32_bf16 v[40:43], a[64:67], a[32:35], v[40:43]\n" \
  "ds_read_b64_tr_b16 a[80:81], v26 offset:0\n" \
  "v_mfma_f32_16x16x32_bf16 v[40:43], a[68:71], a[36:39], v[40:43]\n" \
  "ds_read_b64_tr_b16 a[82:83], v26 offset:1024\n" \
  "v_mfma_f32_16x16x32_bf16 v[96:99], a[68:71], a[44:47], v[96:99]\n" \
  "ds_read_b64_tr_b16 a[84:85], v26 offset:8192\n" \
  "v_mfma_f32_16x16x32_bf16 v[96:99], a[64:67], a[40:43], v[96:99]\n" \
  "ds_read_b64_tr_b16 a[86:87], v26 offset:9216\n" \
  "v_mfma_f32_16x16x32_bf16 v[152:155], a[64:67], a[48:51], v[152:155]\n" \
  "v_mfma_f32_16x16x32_bf16 v[152:155], a[68:71], a[52:55], v[152:155]\n" \
  "v_mfma_f32_16x16x32_bf16 v[208:211], a[68:71], a[60:63], v[208:211]\n" \
  "v_mfma_f32_16x16x32_bf16 v[208:211], a[64:67], a[56:59], v[208:211]\n" \
  "s_waitcnt lgkmcnt(4)\n" \
  "v_mfma_f32_16x16x32_bf16 v[212:215], a[72:75], a[56:59], v[212:215]\n" \
  "ds_read_b64_tr_b16 a[88:89], v26 offset:16384\n" \
  "v_mfma_f32_16x16x32_bf16 v[212:215], a[76:79], a[60:63], v[212:215]\n" \
  "ds_read_b64_tr_b16 a[90:91], v26 offset:17408\n" \
  "v_mfma_f32_16x16x32_bf16 v[156:159], a[76:79], a[52:55], v[156:159]\n" \
  "ds_read_b64_tr_b16 a[92:93], v26 offset:24576\n" \
  "v_mfma_f32_16x16x32_bf16 v[156:159], a[72:75], a[48:51], v[156:159]\n" \
  "ds_read_b64_tr_b16 a[94:95], v26 offset:25600\n" \
  "v_mfma_f32_16x16x32_bf16 v[100:103], a[72:75], a[40:43], v[100:103]\n" \
  "v_mfma_f32_16x16x32_bf16 v[100:103], a[76:79], a[44:47], v[100:103]\n" \
  "v_mfma_f32_16x16x32_bf16 v[44:47], a[76:79], a[36:39], v[44:47]\n" \
  "v_mfma_f32_16x16x32_bf16 v[44:47], a[72:75], a[32:35], v[44:47]\n" \
  "s_waitcnt lgkmcnt(4)\n" \
  "v_mfma_f32_16x16x32_bf16 v[48:51], a[80:83], a[32:35], v[48:51]\n" \
  "ds_read_b64_tr_b16 a[64:65], v27 offset:0\n" \
  "v_mfma_f32_16x16x32_bf16 v[48:51], a[84:87], a[36:39], v[48:51]\n" \
  "ds_read_b64_tr_b16 a[66:67], v27 offset:1024\n" \
  "v_mfma_f32_16x16x32_bf16 v[104:107], a[84:87], a[44:47], v[104:107]\n" \
  "ds_read_b64_tr_b16 a[68:69], v27 offset:8192\n" \
  "v_mfma_f32_16x16x32_bf16 v[104:107], a[80:83], a[40:43], v[104:107]\n" \
  "ds_read_b64_tr_b16 a[70:71], v27 offset:9216\n" \
  "v_mfma_f32_16x16x32_bf16 v[160:163], a[80:83], a[48:51], v[160:163]\n" \
  "v_mfma_f32_16x16x32_bf16 v[160:163], a[84:87], a[52:55], v[160:163]\n" \
  "v_mfma_f32_16x16x32_bf16 v[216:219], a[84:87], a[60:63], v[216:219]\n" \
  "v_mfma_f32_16x16x32_bf16 v[216:219], a[80:83], a[56:59], v[216:219]\n" \
  "s_waitcnt lgkmcnt(4)\n" \
  "v_mfma_f32_16x16x32_bf16 v[220:223], a[88:91], a[56:59], v[220:223]\n" \
  "ds_read_b64_tr_b16 a[72:73], v27 offset:16384\n" \
  "v_mfma_f32_16x16x32_bf16 v[220:223], a[92:95], a[60:63], v[220:223]\n" \
  "ds_read_b64_tr_b16 a[74:75], v27 offset:17408\n" \
  "v_mfma_f32_16x16x32_bf16 v[164:167], a[92:95], a[52:55], v[164:167]\n" \
  "ds_read_b64_tr_b16 a[76:77], v27 offset:24576\n" \
  "v_mfma_f32_16x16x32_bf16 v[164:167], a[88:91], a[48:51], v[164:167]\n" \
  "ds_read_b64_tr_b16 a[78:79], v27 offset:25600\n" \
  "v_mfma_f32_16x16x32_bf16 v[108:111], a[88:91], a[40:43], v[108:111]\n" \
  "v_mfma_f32_16x16x32_bf16 v[108:111], a[92:95], a[44:47], v[108:111]\n" \
  "v_mfma_f32_16x16x32_bf16 v[52:55], a[92:95], a[36:39], v[52:55]\n" \
  "v_mfma_f32_16x16x32_bf16 v[52:55], a[88:91], a[32:35], v[52:55]\n" \
  "s_waitcnt lgkmcnt(4)\n" \
  "v_mfma_f32_16x16x32_bf16 v[56:59], a[64:67], a[32:35], v[56:59]\n" \
  "ds_read_b64_tr_b16 a[80:81], v28 offset:0\n" \
  "v_mfma_f32_16x16x32_bf16 v[56:59], a[68:71], a[36:39], v[56:59]\n" \
  "ds_read_b64_tr_b16 a[82:83], v28 offset:1024\n" \
  "v_mfma_f32_16x16x32_bf16 v[112:115], a[68:71], a[44:47], v[112:115]\n" \
  "ds_read_b64_tr_b16 a[84:85], v28 offset:8192\n" \
  "v_mfma_f32_16x16x32_bf16 v[112:115], a[64:67], a[40:43], v[112:115]\n" \
  "ds_read_b64_tr_b16 a[86:87], v28 offset:9216\n" \
  "v_mfma_f32_16x16x32_bf16 v[168:171], a[64:67], a[48:51], v[168:171]\n" \
  "v_mfma_f32_16x16x32_bf16 v[168:171], a[68:71], a[52:55], v[168:171]\n" \
  "v_mfma_f32_16x16x32_bf16 v[224:227], a[68:71], a[60:63], v[224:227]\n" \
  "v_mfma_f32_16x16x32_bf16 v[224:227], a[64:67], a[56:59], v[224:227]\n" \
  "s_waitcnt lgkmcnt(4)\n" \
  "v_mfma_f32_16x16x32_bf16 v[228:231], a[72:75], a[56:59], v[228:231]\n" \
  "ds_read_b64_tr_b16 a[88:89], v28 offset:16384\n" \
  "v_mfma_f32_16x16x32_bf16 v[228:231], a[76:79], a[60:63], v[228:231]\n" \
  "ds_read_b64_tr_b16 a[90:91], v28 offset:17408\n" \
  "v_mfma_f32_16x16x32_bf16 v[172:175], a[76:79], a[52:55], v[172:175]\n" \
  "ds_read_b64_tr_b16 a[92:93], v28 offset:24576\n" \
  "v_mfma_f32_16x16x32_bf16 v[172:175], a[72:75], a[48:51], v[172:175]\n" \
  "ds_read_b64_tr_b16 a[94:95], v28 offset:25600\n" \
  "v_mfma_f32_16x16x32_bf16 v[116:119], a[72:75], a[40:43], v[116:119]\n" \
  "v_mfma_f32_16x16x32_bf16 v[116:119], a[76:79], a[44:47], v[116:119]\n" \
  "v_mfma_f32_16x16x32_bf16 v[60:63], a[76:79], a[36:39], v[60:63]\n" \
  "v_mfma_f32_16x16x32_bf16 v[60:63], a[72:75], a[32:35], v[60:63]\n" \
  "s_waitcnt lgkmcnt(4)\n" \
  "v_mfma_f32_16x16x32_bf16 v[64:67], a[80:83], a[32:35], v[64:67]\n" \
  "ds_read_b64_tr_b16 a[64:65], v29 offset:0\n" \
  "v_mfma_f32_16x16x32_bf16 v[64:67], a[84:87], a[36:39], v[64:67]\n" \
  "ds_read_b64_tr_b16 a[66:67], v29 offset:1024\n" \
  "v_mfma_f32_16x16x32_bf16 v[120:123], a[84:87], a[44:47], v[120:123]\n" \
  "ds_read_b64_tr_b16 a[68:69], v29 offset:8192\n" \
  "v_mfma_f32_16x16x32_bf16 v[120:123], a[80:83], a[40:43], v[120:123]\n" \
  "ds_read_b64_tr_b16 a[70:71], v29 offset:9216\n" \
  "v_mfma_f32_16x16x32_bf16 v[176:179], a[80:83], a[48:51], v[176:179]\n" \
  "v_mfma_f32_16x16x32_bf16 v[176:179], a[84:87], a[52:55], v[176:179]\n" \
  "v_mfma_f32_16x16x32_bf16 v[232:235], a[84:87], a[60:63], v[232:235]\n" \
  "v_mfma_f32_16x16x32_bf16 v[232:235], a[80:83], a[56:59], v[232:235]\n" \
  "s_waitcnt lgkmcnt(4)\n" \
  "v_mfma_f32_16x16x32_bf16 v[236:239], a[88:91], a[56:59], v[236:239]\n" \
  "ds_read_b64_tr_b16 a[72:73], v29 offset:16384\n" \
  "v_mfma_f32_16x16x32_bf16 v[236:239], a[92:95], a[60:63], v[236:239]\n" \
  "ds_read_b64_tr_b16 a[74:75], v29 offset:17408\n" \
  "v_mfma_f32_16x16x32_bf16 v[180:183], a[92:95], a[52:55], v[180:183]\n" \
  "ds_read_b64_tr_b16 a[76:77], v29 offset:24576\n" \
  "v_mfma_f32_16x16x32_bf16 v[180:183], a[88:91], a[48:51], v[180:183]\n" \
  "ds_read_b64_tr_b16 a[78:79], v29 offset:25600\n" \
  "v_mfma_f32_16x16x32_bf16 v[124:127], a[88:91], a[40:43], v[124:127]\n" \
  "v_mfma_f32_16x16x32_bf16 v[124:127], a[92:95], a[44:47], v[124:127]\n" \
  "v_mfma_f32_16x16x32_bf16 v[68:71], a[92:95], a[36:39], v[68:71]\n" \
  "v_mfma_f32_16x16x32_bf16 v[68:71], a[88:91], a[32:35], v[68:71]\n" \
  "s_waitcnt lgkmcnt(4)\n" \
  "v_mfma_f32_16x16x32_bf16 v[72:75], a[64:67], a[32:35], v[72:75]\n" \
  "ds_read_b64_tr_b16 a[80:81], v30 offset:0\n" \
  "v_mfma_f32_16x16x32_bf16 v[72:75], a[68:71], a[36:39], v[72:75]\n" \
  "ds_read_b64_tr_b16 a[82:83], v30 offset:1024\n" \
  "v_mfma_f32_16x16x32_bf16 v[128:131], a[68:71], a[44:47], v[128:131]\n" \
  "ds_read_b64_tr_b16 a[84:85], v30 offset:8192\n" \
  "v_mfma_f32_16x16x32_bf16 v[128:131], a[64:67], a[40:43], v[128:131]\n" \
  "ds_read_b64_tr_b16 a[86:87], v30 offset:9216\n" \
  "v_mfma_f32_16x16x32_bf16 v[184:187], a[64:67], a[48:51], v[184:187]\n" \
  "v_mfma_f32_16x16x32_bf16 v[184:187], a[68:71], a[52:55], v[184:187]\n" \
  "v_mfma_f32_16x16x32_bf16 v[240:243], a[68:71], a[60:63], v[240:243]\n" \
  "v_mfma_f32_16x16x32_bf16 v[240:243], a[64:67], a[56:59], v[240:243]\n" \
  "s_waitcnt lgkmcnt(4)\n" \
  "v_mfma_f32_16x16x32_bf16 v[244:247], a[72:75], a[56:59], v[244:247]\n" \
  "ds_read_b64_tr_b16 a[88:89], v30 offset:16384\n" \
  "v_mfma_f32_16x16x32_bf16 v[244:247], a[76:79], a[60:63], v[244:247]\n" \
  "ds_read_b64_tr_b16 a[90:91], v30 offset:17408\n" \
  "v_mfma_f32_16x16x32_bf16 v[188:191], a[76:79], a[52:55], v[188:191]\n" \
  "ds_read_b64_tr_b16 a[92:93], v30 offset:24576\n" \
  "v_mfma_f32_16x16x32_bf16 v[188:191], a[72:75], a[48:51], v[188:191]\n" \
  "ds_read_b64_tr_b16 a[94:95], v30 offset:25600\n" \
  "v_mfma_f32_16x16x32_bf16 v[132:135], a[72:75], a[40:43], v[132:135]\n" \
  "v_mfma_f32_16x16x32_bf16 v[132:135], a[76:79], a[44:47], v[132:135]\n" \
  "v_mfma_f32_16x16x32_bf16 v[76:79], a[76:79], a[36:39], v[76:79]\n" \
  "v_mfma_f32_16x16x32_bf16 v[76:79], a[72:75], a[32:35], v[76:79]\n" \
  "s_waitcnt lgkmcnt(4)\n" \
  "v_mfma_f32_16x16x32_bf16 v[80:83], a[80:83], a[32:35], v[80:83]\n" \
  "v_mfma_f32_16x16x32_bf16 v[80:83], a[84:87], a[36:39], v[80:83]\n" \
  "v_mfma_f32_16x16x32_bf16 v[136:139], a[84:87], a[44:47], v[136:139]\n" \
  "v_mfma_f32_16x16x32_bf16 v[136:139], a[80:83], a[40:43], v[136:139]\n" \
  "v_mfma_f32_16x16x32_bf16 v[192:195], a[80:83], a[48:51], v[192:195]\n" \
  "v_mfma_f32_16x16x32_bf16 v[192:195], a[84:87], a[52:55], v[192:195]\n" \
  "v_mfma_f32_16x16x32_bf16 v[248:251], a[84:87], a[60:63], v[248:251]\n" \
  "v_mfma_f32_16x16x32_bf16 v[248:251], a[80:83], a[56:59], v[248:251]\n" \
  "s_waitcnt lgkmcnt(0)\n" \
  "v_mfma_f32_16x16x32_bf16 v[252:255], a[88:91], a[56:59], v[252:255]\n" \
  "v_mfma_f32_16x16x32_bf16 v[252:255], a[92:95], a[60:63], v[252:255]\n" \
  "v_mfma_f32_16x16x32_bf16 v[196:199], a[92:95], a[52:55], v[196:199]\n" \
  "v_mfma_f32_16x16x32_bf16 v[196:199], a[88:91], a[48:51], v[196:199]\n" \
  "v_mfma_f32_16x16x32_bf16 v[140:143], a[88:91], a[40:43], v[140:143]\n" \
  "v_mfma_f32_16x16x32_bf16 v[140:143], a[92:95], a[44:47], v[140:143]\n" \
  "v_mfma_f32_16x16x32_bf16 v[84:87], a[92:95], a[36:39], v[84:87]\n" \
  "v_mfma_f32_16x16x32_bf16 v[84:87], a[88:91], a[32:35], v[84:87]\n" \
  "s_waitcnt vmcnt(0) lgkmcnt(0)\n" \
  "s_nop 7\n"
#define G1_CLOB "v24","v25","v26","v27","v28","v29","v30","a0","a1","a2","a3","a4","a5","a6","a7","a8","a9","a10","a11","a12","a13","a14","a15","a16","a17","a18","a19","a20","a21","a22","a23","a24","a25","a26","a27","a28","a29","a30","a31","a32","a33","a34","a35","a36","a37","a38","a39","a40","a41","a42","a43","a44","a45","a46","a47","a48","a49","a50","a51","a52","a53","a54","a55","a56","a57","a58","a59","a60","a61","a62","a63","a64","a65","a66","a67","a68","a69","a70","a71","a72","a73","a74","a75","a76","a77","a78","a79","a80","a81","a82","a83","a84","a85","a86","a87","a88","a89","a90","a91","a92","a93","a94","a95","vcc","s40","s41","s42","s43","s44","s45","s46","s47","s48","s49","s50","s51","s52","s53","s54","s55","s56","s57","s58","s59","s60","s61","s62","s63","s64","s65","s66","s67","s68","s69","s70","s71","scc","memory"
#define G2_ASM \
  "s_mov_b32 s40, %[pa0]\n" \
  "s_mov_b32 s41, %[pa1]\n" \
  "s_mov_b32 s42, %[sza]\n" \
  "s_mov_b32 s43, 0x20000\n" \
  "s_mov_b32 s44, %[pw00]\n" \
  "s_mov_b32 s45, %[pw01]\n" \
  "s_mov_b32 s46, %[szw]\n" \
  "s_mov_b32 s47, 0x20000\n" \
  "s_mov_b32 s60, %[ws0]\n" \
  "s_mov_b32 s61, %[ws1]\n" \
  "s_mov_b32 s63, 0x20000\n" \
  "s_mov_b32 s68, %[ws0]\n" \
  "s_mov_b32 s69, %[ws1]\n" \
  "s_mov_b32 s71, 0x20000\n" \
  "s_mov_b32 s72, %[ws0]\n" \
  "s_mov_b32 s73, %[ws1]\n" \
  "s_mov_b32 s75, 0x20000\n" \
  "s_mov_b32 s52, %[ra]\n" \
  "s_mov_b32 s53, %[ra0]\n" \
  "v_mov_b32 v24, %[vl0]\n" \
  "v_mov_b32 v25, %[vl1]\n" \
  "v_mov_b32 v26, %[vl2]\n" \
  "v_mov_b32 v27, %[vl3]\n" \
  "v_mov_b32 v28, %[vl4]\n" \
  "v_mov_b32 v29, %[vl5]\n" \
  "v_mov_b32 v30, %[vl6]\n" \
  "v_mov_b32 v31, %[vl7]\n" \
  "ds_read_b64_tr_b16 a[96:97], v24 offset:0\n" \
  "ds_read_b64_tr_b16 a[98:99], v24 offset:1024\n" \
  "ds_read_b64_tr_b16 a[100:101], v24 offset:8192\n" \
  "ds_read_b64_tr_b16 a[102:103], v24 offset:9216\n" \
  "ds_read_b64_tr_b16 a[104:105], v25 offset:0\n" \
  "ds_read_b64_tr_b16 a[106:107], v25 offset:1024\n" \
  "ds_read_b64_tr_b16 a[108:109], v25 offset:8192\n" \
  "ds_read_b64_tr_b16 a[110:111], v25 offset:9216\n" \
  "s_waitcnt vmcnt(1)\n" \
  "v_cmp_ne_u64 vcc, %[tag], %[ft]\n" \
  "s_mov_b32 s64, 262144\n" \
  "s_nop 0\n" \
  "s_cbranch_vccz Lga%=\n" \
  "Lpa%=:\n" \
  "global_load_dwordx2 %[ft], %[fa0], off sc1\n" \
  "s_waitcnt vmcnt(0)\n" \
  "v_cmp_ne_u64 vcc, %[tag], %[ft]\n" \
  "s_nop 1\n" \
  "s_cbranch_vccz Lga%=\n" \
  "s_sub_u32 s64, s64, 1\n" \
  "s_cbranch_scc0 Lpa%=\n" \
  "Lga%=:\n" \
  "buffer_load_dwordx4 a[0:3], %[voa0], s[40:43], s52 offen offset:0 sc1\n" \
  "buffer_load_dwordx4 a[4:7], %[voa0], s[40:43], s52 offen offset:1024 sc1\n" \
  "buffer_load_dwordx4 a[8:11], %[voa1], s[40:43], s52 offen offset:0 sc1\n" \
  "buffer_load_dwordx4 a[12:15], %[voa1], s[40:43], s52 offen offset:1024 sc1\n" \
  "buffer_load_dwordx4 a[16:19], %[voa2], s[40:43], s52 offen offset:0 sc1\n" \
  "buffer_load_dwordx4 a[20:23], %[voa2], s[40:43], s52 offen offset:1024 sc1\n" \
  "buffer_load_dwordx4 a[24:27], %[voa3], s[40:43], s52 offen offset:0 sc1\n" \
  "buffer_load_dwordx4 a[28:31], %[voa3], s[40:43], s52 offen offset:1024 sc1\n" \
  "s_add_u32 s52, s52, 2048\n" \
  "buffer_load_dwordx4 a[32:35], %[voa0], s[40:43], s52 offen offset:0 sc1\n" \
  "buffer_load_dwordx4 a[36:39], %[voa0], s[40:43], s52 offen offset:1024 sc1\n" \
  "buffer_load_dwordx4 a[40:43], %[voa1], s[40:43], s52 offen offset:0 sc1\n" \
  "buffer_load_dwordx4 a[44:47], %[voa1], s[40:43], s52 offen offset:1024 sc1\n" \
  "buffer_load_dwordx4 a[48:51], %[voa2], s[40:43], s52 offen offset:0 sc1\n" \
  "buffer_load_dwordx4 a[52:55], %[voa2], s[40:43], s52 offen offset:1024 sc1\n" \
  "buffer_load_dwordx4 a[56:59], %[voa3], s[40:43], s52 offen offset:0 sc1\n" \
  "buffer_load_dwordx4 a[60:63], %[voa3], s[40:43], s52 offen offset:1024 sc1\n" \
  "s_add_u32 s52, s52, 2048\n" \
  "s_waitcnt vmcnt(8) lgkmcnt(4)\n" \
  "buffer_store_dwordx2 %[tagv], %[fb], %[srdw], 0 offen sc1\n" \
  "v_mfma_f32_16x16x32_bf16 v[32:35], a[96:99], a[0:3], 0\n" \
  "ds_read_b64_tr_b16 a[112:113], v26 offset:0\n" \
  "v_mfma_f32_16x16x32_bf16 v[32:35], a[100:103], a[4:7], v[32:35]\n" \
  "ds_read_b64_tr_b16 a[114:115], v26 offset:1024\n" \
  "v_mfma_f32_16x16x32_bf16 v[64:67], a[100:103], a[12:15], 0\n" \
  "ds_read_b64_tr_b16 a[116:117], v26 offset:8192\n" \
  "v_mfma_f32_16x16x32_bf16 v[64:67], a[96:99], a[8:11], v[64:67]\n" \
  "ds_read_b64_tr_b16 a[118:119], v26 offset:9216\n" \
  "v_mfma_f32_16x16x32_bf16 v[96:99], a[96:99], a[16:19], 0\n" \
  "buffer_load_dwordx4 a[64:67], %[voa0], s[40:43], s52 offen offset:0 sc1\n" \
  "v_mfma_f32_16x16x32_bf16 v[96:99], a[100:103], a[20:23], v[96:99]\n" \
  "v_mfma_f32_16x16x32_bf16 v[128:131], a[100:103], a[28:31], 0\n" \
  "buffer_load_dwordx4 a[68:71], %[voa0], s[40:43], s52 offen offset:1024 sc1\n" \
  "v_mfma_f32_16x16x32_bf16 v[128:131], a[96:99], a[24:27], v[128:131]\n" \
  "s_waitcnt lgkmcnt(4)\n" \
  "v_mfma_f32_16x16x32_bf16 v[132:135], a[104:107], a[24:27], 0\n" \
  "ds_read_b64_tr_b16 a[120:121], v27 offset:0\n" \
  "v_mfma_f32_16x16x32_bf16 v[132:135], a[108:111], a[28:31], v[132:135]\n" \
  "ds_read_b64_tr_b16 a[122:123], v27 offset:1024\n" \
  "v_mfma_f32_16x16x32_bf16 v[100:103], a[108:111], a[20:23], 0\n" \
  "ds_read_b64_tr_b16 a[124:125], v27 offset:8192\n" \
  "v_mfma_f32_16x16x32_bf16 v[100:103], a[104:107], a[16:19], v[100:103]\n" \
  "ds_read_b64_tr_b16 a[126:127], v27 offset:9216\n" \
  "v_mfma_f32_16x16x32_bf16 v[68:71], a[104:107], a[8:11], 0\n" \
  "buffer_load_dwordx4 a[72:75], %[voa1], s[40:43], s52 offen offset:0 sc1\n" \
  "v_mfma_f32_16x16x32_bf16 v[68:71], a[108:111], a[12:15], v[68:71]\n" \
  "v_mfma_f32_16x16x32_bf16 v[36:39], a[108:111], a[4:7], 0\n" \
  "v_mfma_f32_16x16x32_bf16 v[36:39], a[104:107], a[0:3], v[36:39]\n" \
  "s_waitcnt lgkmcnt(4)\n" \
  "v_mfma_f32_16x16x32_bf16 v[40:43], a[112:115], a[0:3], 0\n" \
  "ds_read_b64_tr_b16 a[96:97], v28 offset:0\n" \
  "v_mfma_f32_16x16x32_bf16 v[40:43], a[116:119], a[4:7], v[40:43]\n" \
  "ds_read_b64_tr_b16 a[98:99], v28 offset:1024\n" \
  "v_mfma_f32_16x16x32_bf16 v[72:75], a[116:119], a[12:15], 0\n" \
  "ds_read_b64_tr_b16 a[100:101], v28 offset:8192\n" \
  "v_mfma_f32_16x16x32_bf16 v[72:75], a[112:115], a[8:11], v[72:75]\n" \
  "ds_read_b64_tr_b16 a[102:103], v28 offset:9216\n" \
  "v_mfma_f32_16x16x32_bf16 v[104:107], a[112:115], a[16:19], 0\n" \
  "buffer_load_dwordx4 a[76:79], %[voa1], s[40:43], s52 offen offset:1024 sc1\n" \
  "v_mfma_f32_16x16x32_bf16 v[104:107], a[116:119], a[20:23], v[104:107]\n" \
  "v_mfma_f32_16x16x32_bf16 v[136:139], a[116:119], a[28:31], 0\n" \
  "buffer_load_dwordx4 a[80:83], %[voa2], s[40:43], s52 offen offset:0 sc1\n" \
  "v_mfma_f32_16x16x32_bf16 v[136:139], a[112:115], a[24:27], v[136:139]\n" \
  "s_waitcnt lgkmcnt(4)\n" \
  "v_mfma_f32_16x16x32_bf16 v[140:143], a[120:123], a[24:27], 0\n" \
  "ds_read_b64_tr_b16 a[104:105], v29 offset:0\n" \
  "v_mfma_f32_16x16x32_bf16 v[140:143], a[124:127], a[28:31], v[140:143]\n" \
  "ds_read_b64_tr_b16 a[106:107], v29 offset:1024\n" \
  "v_mfma_f32_16x16x32_bf16 v[108:111], a[124:127], a[20:23], 0\n" \
  "ds_read_b64_tr_b16 a[108:109], v29 offset:8192\n" \
  "v_mfma_f32_16x16x32_bf16 v[108:111], a[120:123], a[16:19], v[108:111]\n" \
  "ds_read_b64_tr_b16 a[110:111], v29 offset:9216\n" \
  "v_mfma_f32_16x16x32_bf16 v[76:79], a[120:123], a[8:11], 0\n" \
  "buffer_load_dwordx4 a[84:87], %[voa2], s[40:43], s52 offen offset:1024 sc1\n" \
  "v_mfma_f32_16x16x32_bf16 v[76:79], a[124:127], a[12:15], v[76:79]\n" \
  "v_mfma_f32_16x16x32_bf16 v[44:47], a[124:127], a[4:7], 0\n" \
  "v_mfma_f32_16x16x32_bf16 v[44:47], a[120:123], a[0:3], v[44:47]\n" \
  "s_waitcnt lgkmcnt(4)\n" \
  "v_mfma_f32_16x16x32_bf16 v[48:51], a[96:99], a[0:3], 0\n" \
  "ds_read_b64_tr_b16 a[112:113], v30 offset:0\n" \
  "v_mfma_f32_16x16x32_bf16 v[48:51], a[100:103], a[4:7], v[48:51]\n" \
  "ds_read_b64_tr_b16 a[114:115], v30 offset:1024\n" \
  "v_mfma_f32_16x16x32_bf16 v[80:83], a[100:103], a[12:15], 0\n" \
  "ds_read_b64_tr_b16 a[116:117], v30 offset:8192\n" \
  "v_mfma_f32_16x16x32_bf16 v[80:83], a[96:99], a[8:11], v[80:83]\n" \
  "ds_read_b64_tr_b16 a[118:119], v30 offset:9216\n" \
  "v_mfma_f32_16x16x32_bf16 v[112:115], a[96:99], a[16:19], 0\n" \
  "buffer_load_dwordx4 a[88:91], %[voa3], s[40:43], s52 offen offset:0 sc1\n" \
  "v_mfma_f32_16x16x32_bf16 v[112:115], a[100:103], a[20:23], v[112:115]\n" \
  "v_mfma_f32_16x16x32_bf16 v[144:147], a[100:103], a[28:31], 0\n" \
  "buffer_load_dwordx4 a[92:95], %[voa3], s[40:43], s52 offen offset:1024 sc1\n" \
  "s_add_u32 s52, s52, 2048\n" \
  "v_mfma_f32_16x16x32_bf16 v[144:147], a[96:99], a[24:27], v[144:147]\n" \
  "s_waitcnt lgkmcnt(4)\n" \
  "v_mfma_f32_16x16x32_bf16 v[148:151], a[104:107], a[24:27], 0\n" \
  "ds_read_b64_tr_b16 a[120:121], v31 offset:0\n" \
  "v_mfma_f32_16x16x32_bf16 v[148:151], a[108:111], a[28:31], v[148:151]\n" \
  "ds_read_b64_tr_b16 a[122:123], v31 offset:1024\n" \
  "v_mfma_f32_16x16x32_bf16 v[116:119], a[108:111], a[20:23], 0\n" \
  "ds_read_b64_tr_b16 a[124:125], v31 offset:8192\n" \
  "s_add_u32 m0, %[ldsw], 49152\n" \
  "v_mfma_f32_16x16x32_bf16 v[116:119], a[104:107], a[16:19], v[116:119]\n" \
  "ds_read_b64_tr_b16 a[126:127], v31 offset:9216\n" \
  "s_mov_b32 s56, 688128\n" \
  "v_mfma_f32_16x16x32_bf16 v[84:87], a[104:107], a[8:11], 0\n" \
  "buffer_load_dwordx4 %[vow0], s[44:47], s56 offen lds\n" \
  "v_mfma_f32_16x16x32_bf16 v[84:87], a[108:111], a[12:15], v[84:87]\n" \
  "v_mfma_f32_16x16x32_bf16 v[52:55], a[108:111], a[4:7], 0\n" \
  "v_mfma_f32_16x16x32_bf16 v[52:55], a[104:107], a[0:3], v[52:55]\n" \
  "s_waitcnt lgkmcnt(4)\n" \
  "v_mfma_f32_16x16x32_bf16 v[56:59], a[112:115], a[0:3], 0\n" \
  "ds_read_b64_tr_b16 a[96:97], v24 offset:16384\n" \
  "v_mfma_f32_16x16x32_bf16 v[56:59], a[116:119], a[4:7], v[56:59]\n" \
  "ds_read_b64_tr_b16 a[98:99], v24 offset:17408\n" \
  "v_mfma_f32_16x16x32_bf16 v[88:91], a[116:119], a[12:15], 0\n" \
  "ds_read_b64_tr_b16 a[100:101], v24 offset:24576\n" \
  "s_add_u32 m0, %[ldsw], 50176\n" \
  "v_mfma_f32_16x16x32_bf16 v[88:91], a[112:115], a[8:11], v[88:91]\n" \
  "ds_read_b64_tr_b16 a[102:103], v24 offset:25600\n" \
  "s_mov_b32 s57, 696320\n" \
  "v_mfma_f32_16x16x32_bf16 v[120:123], a[112:115], a[16:19], 0\n" \
  "buffer_load_dwordx4 %[vow0], s[44:47], s57 offen lds\n" \
  "s_add_u32 m0, %[ldsw], 51200\n" \
  "v_mfma_f32_16x16x32_bf16 v[120:123], a[116:119], a[20:23], v[120:123]\n" \
  "s_mov_b32 s56, 704512\n" \
  "v_mfma_f32_16x16x32_bf16 v[152:155], a[116:119], a[28:31], 0\n" \
  "buffer_load_dwordx4 %[vow1], s[44:47], s56 offen lds\n" \
  "v_mfma_f32_16x16x32_bf16 v[152:155], a[112:115], a[24:27], v[152:155]\n" \
  "s_waitcnt lgkmcnt(4)\n" \
  "v_mfma_f32_16x16x32_bf16 v[156:159], a[120:123], a[24:27], 0\n" \
  "ds_read_b64_tr_b16 a[104:105], v25 offset:16384\n" \
  "v_mfma_f32_16x16x32_bf16 v[156:159], a[124:127], a[28:31], v[156:159]\n" \
  "ds_read_b64_tr_b16 a[106:107], v25 offset:17408\n" \
  "v_mfma_f32_16x16x32_bf16 v[124:127], a[124:127], a[20:23], 0\n" \
  "ds_read_b64_tr_b16 a[108:109], v25 offset:24576\n" \
  "s_add_u32 m0, %[ldsw], 52224\n" \
  "v_mfma_f32_16x16x32_bf16 v[124:127], a[120:123], a[16:19], v[124:127]\n" \
  "ds_read_b64_tr_b16 a[110:111], v25 offset:25600\n" \
  "s_mov_b32 s57, 712704\n" \
  "v_mfma_f32_16x16x32_bf16 v[92:95], a[120:123], a[8:11], 0\n" \
  "buffer_load_dwordx4 %[vow1], s[44:47], s57 offen lds\n" \
  "v_mfma_f32_16x16x32_bf16 v[92:95], a[124:127], a[12:15], v[92:95]\n" \
  "v_mfma_f32_16x16x32_bf16 v[60:63], a[124:127], a[4:7], 0\n" \
  "v_mfma_f32_16x16x32_bf16 v[60:63], a[120:123], a[0:3], v[60:63]\n" \
  "s_waitcnt vmcnt(13) lgkmcnt(4)\n" \
  "v_mfma_f32_16x16x32_bf16 v[32:35], a[96:99], a[32:35], v[32:35]\n" \
  "ds_read_b64_tr_b16 a[112:113], v26 offset:16384\n" \
  "v_mfma_f32_16x16x32_bf16 v[32:35], a[100:103], a[36:39], v[32:35]\n" \
  "ds_read_b64_tr_b16 a[114:115], v26 offset:17408\n" \
  "v_mfma_f32_16x16x32_bf16 v[64:67], a[100:103], a[44:47], v[64:67]\n" \
  "ds_read_b64_tr_b16 a[116:117], v26 offset:24576\n" \
  "v_mfma_f32_16x16x32_bf16 v[64:67], a[96:99], a[40:43], v[64:67]\n" \
  "ds_read_b64_tr_b16 a[118:119], v26 offset:25600\n" \
  "v_mfma_f32_16x16x32_bf16 v[96:99], a[96:99], a[48:51], v[96:99]\n" \
  "buffer_load_dwordx4 a[0:3], %[voa0], s[40:43], s52 offen offset:0 sc1\n" \
  "v_mfma_f32_16x16x32_bf16 v[96:99], a[100:103], a[52:55], v[96:99]\n" \
  "v_mfma_f32_16x16x32_bf16 v[128:131], a[100:103], a[60:63], v[128:131]\n" \
  "buffer_load_dwordx4 a[4:7], %[voa0], s[40:43], s52 offen offset:1024 sc1\n" \
  "v_mfma_f32_16x16x32_bf16 v[128:131], a[96:99], a[56:59], v[128:131]\n" \
  "s_waitcnt lgkmcnt(4)\n" \
  "v_mfma_f32_16x16x32_bf16 v[132:135], a[104:107], a[56:59], v[132:135]\n" \
  "ds_read_b64_tr_b16 a[120:121], v27 offset:16384\n" \
  "v_mfma_f32_16x16x32_bf16 v[132:135], a[108:111], a[60:63], v[132:135]\n" \
  "ds_read_b64_tr_b16 a[122:123], v27 offset:17408\n" \
  "v_mfma_f32_16x16x32_bf16 v[100:103], a[108:111], a[52:55], v[100:103]\n" \
  "ds_read_b64_tr_b16 a[124:125], v27 offset:24576\n" \
  "v_mfma_f32_16x16x32_bf16 v[100:103], a[104:107], a[48:51], v[100:103]\n" \
  "ds_read_b64_tr_b16 a[126:127], v27 offset:25600\n" \
  "v_mfma_f32_16x16x32_bf16 v[68:71], a[104:107], a[40:43], v[68:71]\n" \
  "buffer_load_dwordx4 a[8:11], %[voa1], s[40:43], s52 offen offset:0 sc1\n" \
  "v_mfma_f32_16x16x32_bf16 v[68:71], a[108:111], a[44:47], v[68:71]\n" \
  "v_mfma_f32_16x16x32_bf16 v[36:39], a[108:111], a[36:39], v[36:39]\n" \
  "v_mfma_f32_16x16x32_bf16 v[36:39], a[104:107], a[32:35], v[36:39]\n" \
  "s_waitcnt lgkmcnt(4)\n" \
  "v_mfma_f32_16x16x32_bf16 v[40:43], a[112:115], a[32:35], v[40:43]\n" \
  "ds_read_b64_tr_b16 a[96:97], v28 offset:16384\n" \
  "v_mfma_f32_16x16x32_bf16 v[40:43], a[116:119], a[36:39], v[40:43]\n" \
  "ds_read_b64_tr_b16 a[98:99], v28 offset:17408\n" \
  "v_mfma_f32_16x16x32_bf16 v[72:75], a[116:119], a[44:47], v[72:75]\n" \
  "ds_read_b64_tr_b16 a[100:101], v28 offset:24576\n" \
  "v_mfma_f32_16x16x32_bf16 v[72:75], a[112:115], a[40:43], v[72:75]\n" \
  "ds_read_b64_tr_b16 a[102:103], v28 offset:25600\n" \
  "v_mfma_f32_16x16x32_bf16 v[104:107], a[112:115], a[48:51], v[104:107]\n" \
  "buffer_load_dwordx4 a[12:15], %[voa1], s[40:43], s52 offen offset:1024 sc1\n" \
  "v_mfma_f32_16x16x32_bf16 v[104:107], a[116:119], a[52:55], v[104:107]\n" \
  "v_mfma_f32_16x16x32_bf16 v[136:139], a[116:119], a[60:63], v[136:139]\n" \
  "buffer_load_dwordx4 a[16:19], %[voa2], s[40:43], s52 offen offset:0 sc1\n" \
  "v_mfma_f32_16x16x32_bf16 v[136:139], a[112:115], a[56:59], v[136:139]\n" \
  "s_waitcnt lgkmcnt(4)\n" \
  "v_mfma_f32_16x16x32_bf16 v[140:143], a[120:123], a[56:59], v[140:143]\n" \
  "ds_read_b64_tr_b16 a[104:105], v29 offset:16384\n" \
  "v_mfma_f32_16x16x32_bf16 v[140:143], a[124:127], a[60:63], v[140:143]\n" \
  "ds_read_b64_tr_b16 a[106:107], v29 offset:17408\n" \
  "v_mfma_f32_16x16x32_bf16 v[108:111], a[124:127], a[52:55], v[108:111]\n" \
  "ds_read_b64_tr_b16 a[108:109], v29 offset:24576\n" \
  "v_mfma_f32_16x16x32_bf16 v[108:111], a[120:123], a[48:51], v[108:111]\n" \
  "ds_read_b64_tr_b16 a[110:111], v29 offset:25600\n" \
  "v_mfma_f32_16x16x32_bf16 v[76:79], a[120:123], a[40:43], v[76:79]\n" \
  "buffer_load_dwordx4 a[20:23], %[voa2], s[40:43], s52 offen offset:1024 sc1\n" \
  "v_mfma_f32_16x16x32_bf16 v[76:79], a[124:127], a[44:47], v[76:79]\n" \
  "v_mfma_f32_16x16x32_bf16 v[44:47], a[124:127], a[36:39], v[44:47]\n" \
  "v_mfma_f32_16x16x32_bf16 v[44:47], a[120:123], a[32:35], v[44:47]\n" \
  "s_waitcnt lgkmcnt(4)\n" \
  "v_mfma_f32_16x16x32_bf16 v[48:51], a[96:99], a[32:35], v[48:51]\n" \
  "ds_read_b64_tr_b16 a[112:113], v30 offset:16384\n" \
  "v_mfma_f32_16x16x32_bf16 v[48:51], a[100:103], a[36:39], v[48:51]\n" \
  "ds_read_b64_tr_b16 a[114:115], v30 offset:17408\n" \
  "v_mfma_f32_16x16x32_bf16 v[80:83], a[100:103], a[44:47], v[80:83]\n" \
  "ds_read_b64_tr_b16 a[116:117], v30 offset:24576\n" \
  "v_mfma_f32_16x16x32_bf16 v[80:83], a[96:99], a[40:43], v[80:83]\n" \
  "ds_read_b64_tr_b16 a[118:119], v30 offset:25600\n" \
  "v_mfma_f32_16x16x32_bf16 v[112:115], a[96:99], a[48:51], v[112:115]\n" \
  "buffer_load_dwordx4 a[24:27], %[voa3], s[40:43], s52 offen offset:0 sc1\n" \
  "v_mfma_f32_16x16x32_bf16 v[112:115], a[100:103], a[52:55], v[112:115]\n" \
  "v_mfma_f32_16x16x32_bf16 v[144:147], a[100:103], a[60:63], v[144:147]\n" \
  "buffer_load_dwordx4 a[28:31], %[voa3], s[40:43], s52 offen offset:1024 sc1\n" \
  "s_add_u32 s52, s52, 2048\n" \
  "v_mfma_f32_16x16x32_bf16 v[144:147], a[96:99], a[56:59], v[144:147]\n" \
  "s_waitcnt lgkmcnt(4)\n" \
  "v_mfma_f32_16x16x32_bf16 v[148:151], a[104:107], a[56:59], v[148:151]\n" \
  "ds_read_b64_tr_b16 a[120:121], v31 offset:16384\n" \
  "v_mfma_f32_16x16x32_bf16 v[148:151], a[108:111], a[60:63], v[148:151]\n" \
  "ds_read_b64_tr_b16 a[122:123], v31 offset:17408\n" \
  "v_mfma_f32_16x16x32_bf16 v[116:119], a[108:111], a[52:55], v[116:119]\n" \
  "ds_read_b64_tr_b16 a[124:125], v31 offset:24576\n" \
  "s_add_u32 m0, %[ldsw], 65536\n" \
  "v_mfma_f32_16x16x32_bf16 v[116:119], a[104:107], a[48:51], v[116:119]\n" \
  "ds_read_b64_tr_b16 a[126:127], v31 offset:25600\n" \
  "s_mov_b32 s56, 917504\n" \
  "v_mfma_f32_16x16x32_bf16 v[84:87], a[104:107], a[40:43], v[84:87]\n" \
  "buffer_load_dwordx4 %[vow0], s[44:47], s56 offen lds\n" \
  "v_mfma_f32_16x16x32_bf16 v[84:87], a[108:111], a[44:47], v[84:87]\n" \
  "v_mfma_f32_16x16x32_bf16 v[52:55], a[108:111], a[36:39], v[52:55]\n" \
  "v_mfma_f32_16x16x32_bf16 v[52:55], a[104:107], a[32:35], v[52:55]\n" \
  "s_waitcnt lgkmcnt(4)\n" \
  "v_mfma_f32_16x16x32_bf16 v[56:59], a[112:115], a[32:35], v[56:59]\n" \
  "ds_read_b64_tr_b16 a[96:97], v24 offset:32768\n" \
  "v_mfma_f32_16x16x32_bf16 v[56:59], a[116:119], a[36:39], v[56:59]\n" \
  "ds_read_b64_tr_b16 a[98:99], v24 offset:33792\n" \
  "v_mfma_f32_16x16x32_bf16 v[88:91], a[116:119], a[44:47], v[88:91]\n" \
  "ds_read_b64_tr_b16 a[100:101], v24 offset:40960\n" \
  "s_add_u32 m0, %[ldsw], 66560\n" \
  "v_mfma_f32_16x16x32_bf16 v[88:91], a[112:115], a[40:43], v[88:91]\n" \
  "ds_read_b64_tr_b16 a[102:103], v24 offset:41984\n" \
  "s_mov_b32 s57, 925696\n" \
  "v_mfma_f32_16x16x32_bf16 v[120:123], a[112:115], a[48:51], v[120:123]\n" \
  "buffer_load_dwordx4 %[vow0], s[44:47], s57 offen lds\n" \
  "s_add_u32 m0, %[ldsw], 67584\n" \
  "v_mfma_f32_16x16x32_bf16 v[120:123], a[116:119], a[52:55], v[120:123]\n" \
  "s_mov_b32 s56, 933888\n" \
  "v_mfma_f32_16x16x32_bf16 v[152:155], a[116:119], a[60:63], v[152:155]\n" \
  "buffer_load_dwordx4 %[vow1], s[44:47], s56 offen lds\n" \
  "v_mfma_f32_16x16x32_bf16 v[152:155], a[112:115], a[56:59], v[152:155]\n" \
  "s_waitcnt lgkmcnt(4)\n" \
  "v_mfma_f32_16x16x32_bf16 v[156:159], a[120:123], a[56:59], v[156:159]\n" \
  "ds_read_b64_tr_b16 a[104:105], v25 offset:32768\n" \
  "v_mfma_f32_16x16x32_bf16 v[156:159], a[124:127], a[60:63], v[156:159]\n" \
  "ds_read_b64_tr_b16 a[106:107], v25 offset:33792\n" \
  "v_mfma_f32_16x16x32_bf16 v[124:127], a[124:127], a[52:55], v[124:127]\n" \
  "ds_read_b64_tr_b16 a[108:109], v25 offset:40960\n" \
  "s_add_u32 m0, %[ldsw], 68608\n" \
  "v_mfma_f32_16x16x32_bf16 v[124:127], a[120:123], a[48:51], v[124:127]\n" \
  "ds_read_b64_tr_b16 a[110:111], v25 offset:41984\n" \
  "s_mov_b32 s57, 942080\n" \
  "v_mfma_f32_16x16x32_bf16 v[92:95], a[120:123], a[40:43], v[92:95]\n" \
  "buffer_load_dwordx4 %[vow1], s[44:47], s57 offen lds\n" \
  "v_mfma_f32_16x16x32_bf16 v[92:95], a[124:127], a[44:47], v[92:95]\n" \
  "v_mfma_f32_16x16x32_bf16 v[60:63], a[124:127], a[36:39], v[60:63]\n" \
  "v_mfma_f32_16x16x32_bf16 v[60:63], a[120:123], a[32:35], v[60:63]\n" \
  "s_waitcnt vmcnt(16) lgkmcnt(4)\n" \
  "v_mfma_f32_16x16x32_bf16 v[32:35], a[96:99], a[64:67], v[32:35]\n" \
  "ds_read_b64_tr_b16 a[112:113], v26 offset:32768\n" \
  "v_mfma_f32_16x16x32_bf16 v[32:35], a[100:103], a[68:71], v[32:35]\n" \
  "ds_read_b64_tr_b16 a[114:115], v26 offset:33792\n" \
  "v_mfma_f32_16x16x32_bf16 v[64:67], a[100:103], a[76:79], v[64:67]\n" \
  "ds_read_b64_tr_b16 a[116:117], v26 offset:40960\n" \
  "v_mfma_f32_16x16x32_bf16 v[64:67], a[96:99], a[72:75], v[64:67]\n" \
  "ds_read_b64_tr_b16 a[118:119], v26 offset:41984\n" \
  "v_mfma_f32_16x16x32_bf16 v[96:99], a[96:99], a[80:83], v[96:99]\n" \
  "buffer_load_dwordx4 a[40:43], %[voa1], s[40:43], s52 offen offset:0 sc1\n" \
  "v_mfma_f32_16x16x32_bf16 v[96:99], a[100:103], a[84:87], v[96:99]\n" \
  "v_mfma_f32_16x16x32_bf16 v[128:131], a[100:103], a[92:95], v[128:131]\n" \
  "buffer_load_dwordx4 a[44:47], %[voa1], s[40:43], s52 offen offset:1024 sc1\n" \
  "v_mfma_f32_16x16x32_bf16 v[128:131], a[96:99], a[88:91], v[128:131]\n" \
  "s_waitcnt lgkmcnt(4)\n" \
  "v_mfma_f32_16x16x32_bf16 v[132:135], a[104:107], a[88:91], v[132:135]\n" \
  "ds_read_b64_tr_b16 a[120:121], v27 offset:32768\n" \
  "v_mfma_f32_16x16x32_bf16 v[132:135], a[108:111], a[92:95], v[132:135]\n" \
  "ds_read_b64_tr_b16 a[122:123], v27 offset:33792\n" \
  "v_mfma_f32_16x16x32_bf16 v[100:103], a[108:111], a[84:87], v[100:103]\n" \
  "ds_read_b64_tr_b16 a[124:125], v27 offset:40960\n" \
  "v_mfma_f32_16x16x32_bf16 v[100:103], a[104:107], a[80:83], v[100:103]\n" \
  "ds_read_b64_tr_b16 a[126:127], v27 offset:41984\n" \
  "v_mfma_f32_16x16x32_bf16 v[68:71], a[104:107], a[72:75], v[68:71]\n" \
  "v_mfma_f32_16x16x32_bf16 v[68:71], a[108:111], a[76:79], v[68:71]\n" \
  "v_mfma_f32_16x16x32_bf16 v[36:39], a[108:111], a[68:71], v[36:39]\n" \
  "buffer_load_dwordx4 a[48:51], %[voa2], s[40:43], s52 offen offset:0 sc1\n" \
  "v_mfma_f32_16x16x32_bf16 v[36:39], a[104:107], a[64:67], v[36:39]\n" \
  "s_waitcnt lgkmcnt(4)\n" \
  "v_mfma_f32_16x16x32_bf16 v[40:43], a[112:115], a[64:67], v[40:43]\n" \
  "ds_read_b64_tr_b16 a[96:97], v28 offset:32768\n" \
  "v_mfma_f32_16x16x32_bf16 v[40:43], a[116:119], a[68:71], v[40:43]\n" \
  "ds_read_b64_tr_b16 a[98:99], v28 offset:33792\n" \
  "v_mfma_f32_16x16x32_bf16 v[72:75], a[116:119], a[76:79], v[72:75]\n" \
  "ds_read_b64_tr_b16 a[100:101], v28 offset:40960\n" \
  "v_mfma_f32_16x16x32_bf16 v[72:75], a[112:115], a[72:75], v[72:75]\n" \
  "ds_read_b64_tr_b16 a[102:103], v28 offset:41984\n" \
  "v_mfma_f32_16x16x32_bf16 v[104:107], a[112:115], a[80:83], v[104:107]\n" \
  "buffer_load_dwordx4 a[52:55], %[voa2], s[40:43], s52 offen offset:1024 sc1\n" \
  "v_mfma_f32_16x16x32_bf16 v[104:107], a[116:119], a[84:87], v[104:107]\n" \
  "v_mfma_f32_16x16x32_bf16 v[136:139], a[116:119], a[92:95], v[136:139]\n" \
  "v_mfma_f32_16x16x32_bf16 v[136:139], a[112:115], a[88:91], v[136:139]\n" \
  "s_waitcnt lgkmcnt(4)\n" \
  "v_mfma_f32_16x16x32_bf16 v[140:143], a[120:123], a[88:91], v[140:143]\n" \
  "ds_read_b64_tr_b16 a[104:105], v29 offset:32768\n" \
  "v_mfma_f32_16x16x32_bf16 v[140:143], a[124:127], a[92:95], v[140:143]\n" \
  "ds_read_b64_tr_b16 a[106:107], v29 offset:33792\n" \
  "v_mfma_f32_16x16x32_bf16 v[108:111], a[124:127], a[84:87], v[108:111]\n" \
  "ds_read_b64_tr_b16 a[108:109], v29 offset:40960\n" \
  "v_mfma_f32_16x16x32_bf16 v[108:111], a[120:123], a[80:83], v[108:111]\n" \
  "ds_read_b64_tr_b16 a[110:111], v29 offset:41984\n" \
  "v_mfma_f32_16x16x32_bf16 v[76:79], a[120:123], a[72:75], v[76:79]\n" \
  "buffer_load_dwordx4 a[56:59], %[voa3], s[40:43], s52 offen offset:0 sc1\n" \
  "v_mfma_f32_16x16x32_bf16 v[76:79], a[124:127], a[76:79], v[76:79]\n" \
  "v_mfma_f32_16x16x32_bf16 v[44:47], a[124:127], a[68:71], v[44:47]\n" \
  "v_mfma_f32_16x16x32_bf16 v[44:47], a[120:123], a[64:67], v[44:47]\n" \
  "s_waitcnt lgkmcnt(4)\n" \
  "v_mfma_f32_16x16x32_bf16 v[48:51], a[96:99], a[64:67], v[48:51]\n" \
  "ds_read_b64_tr_b16 a[112:113], v30 offset:32768\n" \
  "v_mfma_f32_16x16x32_bf16 v[48:51], a[100:103], a[68:71], v[48:51]\n" \
  "ds_read_b64_tr_b16 a[114:115], v30 offset:33792\n" \
  "v_mfma_f32_16x16x32_bf16 v[80:83], a[100:103], a[76:79], v[80:83]\n" \
  "ds_read_b64_tr_b16 a[116:117], v30 offset:40960\n" \
  "v_mfma_f32_16x16x32_bf16 v[80:83], a[96:99], a[72:75], v[80:83]\n" \
  "ds_read_b64_tr_b16 a[118:119], v30 offset:41984\n" \
  "v_mfma_f32_16x16x32_bf16 v[112:115], a[96:99], a[80:83], v[112:115]\n" \
  "buffer_load_dwordx4 a[60:63], %[voa3], s[40:43], s52 offen offset:1024 sc1\n" \
  "s_add_u32 s52, s52, 2048\n" \
  "s_add_u32 m0, %[ldsw], 81920\n" \
  "v_mfma_f32_16x16x32_bf16 v[112:115], a[100:103], a[84:87], v[112:115]\n" \
  "s_mov_b32 s56, 1146880\n" \
  "v_mfma_f32_16x16x32_bf16 v[144:147], a[100:103], a[92:95], v[144:147]\n" \
  "buffer_load_dwordx4 %[vow0], s[44:47], s56 offen lds\n" \
  "v_mfma_f32_16x16x32_bf16 v[144:147], a[96:99], a[88:91], v[144:147]\n" \
  "s_waitcnt lgkmcnt(4)\n" \
  "v_mfma_f32_16x16x32_bf16 v[148:151], a[104:107], a[88:91], v[148:151]\n" \
  "ds_read_b64_tr_b16 a[120:121], v31 offset:32768\n" \
  "v_mfma_f32_16x16x32_bf16 v[148:151], a[108:111], a[92:95], v[148:151]\n" \
  "ds_read_b64_tr_b16 a[122:123], v31 offset:33792\n" \
  "v_mfma_f32_16x16x32_bf16 v[116:119], a[108:111], a[84:87], v[116:119]\n" \
  "ds_read_b64_tr_b16 a[124:125], v31 offset:40960\n" \
  "v_mfma_f32_16x16x32_bf16 v[116:119], a[104:107], a[80:83], v[116:119]\n" \
  "ds_read_b64_tr_b16 a[126:127], v31 offset:41984\n" \
  "v_mfma_f32_16x16x32_bf16 v[84:87], a[104:107], a[72:75], v[84:87]\n" \
  "s_add_u32 m0, %[ldsw], 82944\n" \
  "v_mfma_f32_16x16x32_bf16 v[84:87], a[108:111], a[76:79], v[84:87]\n" \
  "s_mov_b32 s57, 1155072\n" \
  "v_mfma_f32_16x16x32_bf16 v[52:55], a[108:111], a[68:71], v[52:55]\n" \
  "buffer_load_dwordx4 %[vow0], s[44:47], s57 offen lds\n" \
  "v_mfma_f32_16x16x32_bf16 v[52:55], a[104:107], a[64:67], v[52:55]\n" \
  "s_waitcnt vmcnt(20) lgkmcnt(4)\n" \
  "s_barrier\n" \
  "v_mfma_f32_16x16x32_bf16 v[56:59], a[112:115], a[64:67], v[56:59]\n" \
  "ds_read_b64_tr_b16 a[96:97], v24 offset:49152\n" \
  "v_mfma_f32_16x16x32_bf16 v[56:59], a[116:119], a[68:71], v[56:59]\n" \
  "ds_read_b64_tr_b16 a[98:99], v24 offset:50176\n" \
  "v_mfma_f32_16x16x32_bf16 v[88:91], a[116:119], a[76:79], v[88:91]\n" \
  "ds_read_b64_tr_b16 a[100:101], v24 offset:57344\n" \
  "s_add_u32 m0, %[ldsw], 83968\n" \
  "v_mfma_f32_16x16x32_bf16 v[88:91], a[112:115], a[72:75], v[88:91]\n" \
  "ds_read_b64_tr_b16 a[102:103], v24 offset:58368\n" \
  "s_mov_b32 s56, 1163264\n" \
  "v_mfma_f32_16x16x32_bf16 v[120:123], a[112:115], a[80:83], v[120:123]\n" \
  "buffer_load_dwordx4 %[vow1], s[44:47], s56 offen lds\n" \
  "v_mfma_f32_16x16x32_bf16 v[120:123], a[116:119], a[84:87], v[120:123]\n" \
  "v_mfma_f32_16x16x32_bf16 v[152:155], a[116:119], a[92:95], v[152:155]\n" \
  "v_mfma_f32_16x16x32_bf16 v[152:155], a[112:115], a[88:91], v[152:155]\n" \
  "s_waitcnt lgkmcnt(4)\n" \
  "v_mfma_f32_16x16x32_bf16 v[156:159], a[120:123], a[88:91], v[156:159]\n" \
  "ds_read_b64_tr_b16 a[104:105], v25 offset:49152\n" \
  "v_mfma_f32_16x16x32_bf16 v[156:159], a[124:127], a[92:95], v[156:159]\n" \
  "ds_read_b64_tr_b16 a[106:107], v25 offset:50176\n" \
  "v_mfma_f32_16x16x32_bf16 v[124:127], a[124:127], a[84:87], v[124:127]\n" \
  "ds_read_b64_tr_b16 a[108:109], v25 offset:57344\n" \
  "s_add_u32 m0, %[ldsw], 84992\n" \
  "v_mfma_f32_16x16x32_bf16 v[124:127], a[120:123], a[80:83], v[124:127]\n" \
  "ds_read_b64_tr_b16 a[110:111], v25 offset:58368\n" \
  "s_mov_b32 s57, 1171456\n" \
  "v_mfma_f32_16x16x32_bf16 v[92:95], a[120:123], a[72:75], v[92:95]\n" \
  "buffer_load_dwordx4 %[vow1], s[44:47], s57 offen lds\n" \
  "v_mfma_f32_16x16x32_bf16 v[92:95], a[124:127], a[76:79], v[92:95]\n" \
  "v_mfma_f32_16x16x32_bf16 v[60:63], a[124:127], a[68:71], v[60:63]\n" \
  "v_mfma_f32_16x16x32_bf16 v[60:63], a[120:123], a[64:67], v[60:63]\n" \
  "s_waitcnt vmcnt(14) lgkmcnt(4)\n" \
  "v_mfma_f32_16x16x32_bf16 v[32:35], a[96:99], a[0:3], v[32:35]\n" \
  "ds_read_b64_tr_b16 a[112:113], v26 offset:49152\n" \
  "v_add_u32 v24, 65536, %[vl0]\n" \
  "v_mfma_f32_16x16x32_bf16 v[32:35], a[100:103], a[4:7], v[32:35]\n" \
  "ds_read_b64_tr_b16 a[114:115], v26 offset:50176\n" \
  "v_add_u32 v25, 65536, %[vl1]\n" \
  "v_mfma_f32_16x16x32_bf16 v[64:67], a[100:103], a[12:15], v[64:67]\n" \
  "ds_read_b64_tr_b16 a[116:117], v26 offset:57344\n" \
  "v_mfma_f32_16x16x32_bf16 v[64:67], a[96:99], a[8:11], v[64:67]\n" \
  "ds_read_b64_tr_b16 a[118:119], v26 offset:58368\n" \
  "v_mfma_f32_16x16x32_bf16 v[96:99], a[96:99], a[16:19], v[96:99]\n" \
  "buffer_load_dwordx4 a[72:75], %[voa1], s[40:43], s52 offen offset:0 sc1\n" \
  "v_add_u32 v26, 65536, %[vl2]\n" \
  "v_mfma_f32_16x16x32_bf16 v[96:99], a[100:103], a[20:23], v[96:99]\n" \
  "v_mfma_f32_16x16x32_bf16 v[128:131], a[100:103], a[28:31], v[128:131]\n" \
  "buffer_load_dwordx4 a[76:79], %[voa1], s[40:43], s52 offen offset:1024 sc1\n" \
  "v_mfma_f32_16x16x32_bf16 v[128:131], a[96:99], a[24:27], v[128:131]\n" \
  "s_waitcnt lgkmcnt(4)\n" \
  "v_mfma_f32_16x16x32_bf16 v[132:135], a[104:107], a[24:27], v[132:135]\n" \
  "ds_read_b64_tr_b16 a[120:121], v27 offset:49152\n" \
  "v_mfma_f32_16x16x32_bf16 v[132:135], a[108:111], a[28:31], v[132:135]\n" \
  "ds_read_b64_tr_b16 a[122:123], v27 offset:50176\n" \
  "v_mfma_f32_16x16x32_bf16 v[100:103], a[108:111], a[20:23], v[100:103]\n" \
  "ds_read_b64_tr_b16 a[124:125], v27 offset:57344\n" \
  "v_mfma_f32_16x16x32_bf16 v[100:103], a[104:107], a[16:19], v[100:103]\n" \
  "ds_read_b64_tr_b16 a[126:127], v27 offset:58368\n" \
  "v_mfma_f32_16x16x32_bf16 v[68:71], a[104:107], a[8:11], v[68:71]\n" \
  "v_add_u32 v27, 65536, %[vl3]\n" \
  "v_mfma_f32_16x16x32_bf16 v[68:71], a[108:111], a[12:15], v[68:71]\n" \
  "v_mfma_f32_16x16x32_bf16 v[36:39], a[108:111], a[4:7], v[36:39]\n" \
  "buffer_load_dwordx4 a[80:83], %[voa2], s[40:43], s52 offen offset:0 sc1\n" \
  "v_mfma_f32_16x16x32_bf16 v[36:39], a[104:107], a[0:3], v[36:39]\n" \
  "s_waitcnt lgkmcnt(4)\n" \
  "v_mfma_f32_16x16x32_bf16 v[40:43], a[112:115], a[0:3], v[40:43]\n" \
  "ds_read_b64_tr_b16 a[96:97], v28 offset:49152\n" \
  "v_mfma_f32_16x16x32_bf16 v[40:43], a[116:119], a[4:7], v[40:43]\n" \
  "ds_read_b64_tr_b16 a[98:99], v28 offset:50176\n" \
  "v_mfma_f32_16x16x32_bf16 v[72:75], a[116:119], a[12:15], v[72:75]\n" \
  "ds_read_b64_tr_b16 a[100:101], v28 offset:57344\n" \
  "v_mfma_f32_16x16x32_bf16 v[72:75], a[112:115], a[8:11], v[72:75]\n" \
  "ds_read_b64_tr_b16 a[102:103], v28 offset:58368\n" \
  "v_mfma_f32_16x16x32_bf16 v[104:107], a[112:115], a[16:19], v[104:107]\n" \
  "buffer_load_dwordx4 a[84:87], %[voa2], s[40:43], s52 offen offset:1024 sc1\n" \
  "v_add_u32 v28, 65536, %[vl4]\n" \
  "v_mfma_f32_16x16x32_bf16 v[104:107], a[116:119], a[20:23], v[104:107]\n" \
  "v_mfma_f32_16x16x32_bf16 v[136:139], a[116:119], a[28:31], v[136:139]\n" \
  "v_mfma_f32_16x16x32_bf16 v[136:139], a[112:115], a[24:27], v[136:139]\n" \
  "s_waitcnt lgkmcnt(4)\n" \
  "v_mfma_f32_16x16x32_bf16 v[140:143], a[120:123], a[24:27], v[140:143]\n" \
  "ds_read_b64_tr_b16 a[104:105], v29 offset:49152\n" \
  "v_mfma_f32_16x16x32_bf16 v[140:143], a[124:127], a[28:31], v[140:143]\n" \
  "ds_read_b64_tr_b16 a[106:107], v29 offset:50176\n" \
  "v_mfma_f32_16x16x32_bf16 v[108:111], a[124:127], a[20:23], v[108:111]\n" \
  "ds_read_b64_tr_b16 a[108:109], v29 offset:57344\n" \
  "v_mfma_f32_16x16x32_bf16 v[108:111], a[120:123], a[16:19], v[108:111]\n" \
  "ds_read_b64_tr_b16 a[110:111], v29 offset:58368\n" \
  "v_mfma_f32_16x16x32_bf16 v[76:79], a[120:123], a[8:11], v[76:79]\n" \
  "buffer_load_dwordx4 a[88:91], %[voa3], s[40:43], s52 offen offset:0 sc1\n" \
  "v_add_u32 v29, 65536, %[vl5]\n" \
  "v_mfma_f32_16x16x32_bf16 v[76:79], a[124:127], a[12:15], v[76:79]\n" \
  "v_mfma_f32_16x16x32_bf16 v[44:47], a[124:127], a[4:7], v[44:47]\n" \
  "v_mfma_f32_16x16x32_bf16 v[44:47], a[120:123], a[0:3], v[44:47]\n" \
  "s_waitcnt lgkmcnt(4)\n" \
  "v_mfma_f32_16x16x32_bf16 v[48:51], a[96:99], a[0:3], v[48:51]\n" \
  "ds_read_b64_tr_b16 a[112:113], v30 offset:49152\n" \
  "v_mfma_f32_16x16x32_bf16 v[48:51], a[100:103], a[4:7], v[48:51]\n" \
  "ds_read_b64_tr_b16 a[114:115], v30 offset:50176\n" \
  "v_mfma_f32_16x16x32_bf16 v[80:83], a[100:103], a[12:15], v[80:83]\n" \
  "ds_read_b64_tr_b16 a[116:117], v30 offset:57344\n" \
  "v_mfma_f32_16x16x32_bf16 v[80:83], a[96:99], a[8:11], v[80:83]\n" \
  "ds_read_b64_tr_b16 a[118:119], v30 offset:58368\n" \
  "v_mfma_f32_16x16x32_bf16 v[112:115], a[96:99], a[16:19], v[112:115]\n" \
  "buffer_load_dwordx4 a[92:95], %[voa3], s[40:43], s52 offen offset:1024 sc1\n" \
  "s_add_u32 s52, s52, 2048\n" \
  "s_add_u32 m0, %[ldsw], 98304\n" \
  "v_mfma_f32_16x16x32_bf16 v[112:115], a[100:103], a[20:23], v[112:115]\n" \
  "s_mov_b32 s56, 1376256\n" \
  "v_add_u32 v30, 65536, %[vl6]\n" \
  "v_mfma_f32_16x16x32_bf16 v[144:147], a[100:103], a[28:31], v[144:147]\n" \
  "buffer_load_dwordx4 %[vow0], s[44:47], s56 offen lds\n" \
  "v_mfma_f32_16x16x32_bf16 v[144:147], a[96:99], a[24:27], v[144:147]\n" \
  "s_waitcnt lgkmcnt(4)\n" \
  "v_mfma_f32_16x16x32_bf16 v[148:151], a[104:107], a[24:27], v[148:151]\n" \
  "ds_read_b64_tr_b16 a[120:121], v31 offset:49152\n" \
  "v_mfma_f32_16x16x32_bf16 v[148:151], a[108:111], a[28:31], v[148:151]\n" \
  "ds_read_b64_tr_b16 a[122:123], v31 offset:50176\n" \
  "v_mfma_f32_16x16x32_bf16 v[116:119], a[108:111], a[20:23], v[116:119]\n" \
  "ds_read_b64_tr_b16 a[124:125], v31 offset:57344\n" \
  "v_mfma_f32_16x16x32_bf16 v[116:119], a[104:107], a[16:19], v[116:119]\n" \
  "ds_read_b64_tr_b16 a[126:127], v31 offset:58368\n" \
  "v_mfma_f32_16x16x32_bf16 v[84:87], a[104:107], a[8:11], v[84:87]\n" \
  "s_add_u32 m0, %[ldsw], 99328\n" \
  "v_add_u32 v31, 65536, %[vl7]\n" \
  "v_mfma_f32_16x16x32_bf16 v[84:87], a[108:111], a[12:15], v[84:87]\n" \
  "s_mov_b32 s57, 1384448\n" \
  "v_mfma_f32_16x16x32_bf16 v[52:55], a[108:111], a[4:7], v[52:55]\n" \
  "buffer_load_dwordx4 %[vow0], s[44:47], s57 offen lds\n" \
  "v_mfma_f32_16x16x32_bf16 v[52:55], a[104:107], a[0:3], v[52:55]\n" \
  "s_waitcnt vmcnt(18) lgkmcnt(4)\n" \
  "s_barrier\n" \
  "v_mfma_f32_16x16x32_bf16 v[56:59], a[112:115], a[0:3], v[56:59]\n" \
  "ds_read_b64_tr_b16 a[96:97], v24 offset:0\n" \
  "v_mfma_f32_16x16x32_bf16 v[56:59], a[116:119], a[4:7], v[56:59]\n" \
  "ds_read_b64_tr_b16 a[98:99], v24 offset:1024\n" \
  "v_mfma_f32_16x16x32_bf16 v[88:91], a[116:119], a[12:15], v[88:91]\n" \
  "ds_read_b64_tr_b16 a[100:101], v24 offset:8192\n" \
  "s_add_u32 m0, %[ldsw], 100352\n" \
  "v_mfma_f32_16x16x32_bf16 v[88:91], a[112:115], a[8:11], v[88:91]\n" \
  "ds_read_b64_tr_b16 a[102:103], v24 offset:9216\n" \
  "s_mov_b32 s56, 1392640\n" \
  "v_mfma_f32_16x16x32_bf16 v[120:123], a[112:115], a[16:19], v[120:123]\n" \
  "buffer_load_dwordx4 %[vow1], s[44:47], s56 offen lds\n" \
  "v_mfma_f32_16x16x32_bf16 v[120:123], a[116:119], a[20:23], v[120:123]\n" \
  "v_mfma_f32_16x16x32_bf16 v[152:155], a[116:119], a[28:31], v[152:155]\n" \
  "v_mfma_f32_16x16x32_bf16 v[152:155], a[112:115], a[24:27], v[152:155]\n" \
  "s_waitcnt lgkmcnt(4)\n" \
  "v_mfma_f32_16x16x32_bf16 v[156:159], a[120:123], a[24:27], v[156:159]\n" \
  "ds_read_b64_tr_b16 a[104:105], v25 offset:0\n" \
  "v_mfma_f32_16x16x32_bf16 v[156:159], a[124:127], a[28:31], v[156:159]\n" \
  "ds_read_b64_tr_b16 a[106:107], v25 offset:1024\n" \
  "v_mfma_f32_16x16x32_bf16 v[124:127], a[124:127], a[20:23], v[124:127]\n" \
  "ds_read_b64_tr_b16 a[108:109], v25 offset:8192\n" \
  "s_add_u32 m0, %[ldsw], 101376\n" \
  "v_mfma_f32_16x16x32_bf16 v[124:127], a[120:123], a[16:19], v[124:127]\n" \
  "ds_read_b64_tr_b16 a[110:111], v25 offset:9216\n" \
  "s_mov_b32 s57, 1400832\n" \
  "v_mfma_f32_16x16x32_bf16 v[92:95], a[120:123], a[8:11], v[92:95]\n" \
  "buffer_load_dwordx4 %[vow1], s[44:47], s57 offen lds\n" \
  "v_mfma_f32_16x16x32_bf16 v[92:95], a[124:127], a[12:15], v[92:95]\n" \
  "v_mfma_f32_16x16x32_bf16 v[60:63], a[124:127], a[4:7], v[60:63]\n" \
  "v_mfma_f32_16x16x32_bf16 v[60:63], a[120:123], a[0:3], v[60:63]\n" \
  "s_waitcnt vmcnt(14) lgkmcnt(4)\n" \
  "global_load_dwordx2 %[ft], %[fa], off sc1\n" \
  "v_mfma_f32_16x16x32_bf16 v[64:67], a[96:99], a[40:43], v[64:67]\n" \
  "ds_read_b64_tr_b16 a[112:113], v26 offset:0\n" \
  "v_mfma_f32_16x16x32_bf16 v[64:67], a[100:103], a[44:47], v[64:67]\n" \
  "ds_read_b64_tr_b16 a[114:115], v26 offset:1024\n" \
  "v_mfma_f32_16x16x32_bf16 v[96:99], a[100:103], a[52:55], v[96:99]\n" \
  "ds_read_b64_tr_b16 a[116:117], v26 offset:8192\n" \
  "v_mfma_f32_16x16x32_bf16 v[96:99], a[96:99], a[48:51], v[96:99]\n" \
  "ds_read_b64_tr_b16 a[118:119], v26 offset:9216\n" \
  "v_mfma_f32_16x16x32_bf16 v[128:131], a[96:99], a[56:59], v[128:131]\n" \
  "buffer_load_dwordx4 a[8:11], %[voa1], s[40:43], s52 offen offset:0 sc1\n" \
  "v_mfma_f32_16x16x32_bf16 v[128:131], a[100:103], a[60:63], v[128:131]\n" \
  "buffer_load_dwordx4 a[12:15], %[voa1], s[40:43], s52 offen offset:1024 sc1\n" \
  "s_waitcnt lgkmcnt(4)\n" \
  "v_mfma_f32_16x16x32_bf16 v[132:135], a[104:107], a[56:59], v[132:135]\n" \
  "ds_read_b64_tr_b16 a[120:121], v27 offset:0\n" \
  "v_mfma_f32_16x16x32_bf16 v[132:135], a[108:111], a[60:63], v[132:135]\n" \
  "ds_read_b64_tr_b16 a[122:123], v27 offset:1024\n" \
  "v_mfma_f32_16x16x32_bf16 v[100:103], a[108:111], a[52:55], v[100:103]\n" \
  "ds_read_b64_tr_b16 a[124:125], v27 offset:8192\n" \
  "v_mfma_f32_16x16x32_bf16 v[100:103], a[104:107], a[48:51], v[100:103]\n" \
  "ds_read_b64_tr_b16 a[126:127], v27 offset:9216\n" \
  "v_mfma_f32_16x16x32_bf16 v[68:71], a[104:107], a[40:43], v[68:71]\n" \
  "v_mfma_f32_16x16x32_bf16 v[68:71], a[108:111], a[44:47], v[68:71]\n" \
  "buffer_load_dwordx4 a[16:19], %[voa2], s[40:43], s52 offen offset:0 sc1\n" \
  "s_waitcnt lgkmcnt(4)\n" \
  "v_mfma_f32_16x16x32_bf16 v[72:75], a[112:115], a[40:43], v[72:75]\n" \
  "ds_read_b64_tr_b16 a[96:97], v28 offset:0\n" \
  "v_mfma_f32_16x16x32_bf16 v[72:75], a[116:119], a[44:47], v[72:75]\n" \
  "ds_read_b64_tr_b16 a[98:99], v28 offset:1024\n" \
  "v_mfma_f32_16x16x32_bf16 v[104:107], a[116:119], a[52:55], v[104:107]\n" \
  "ds_read_b64_tr_b16 a[100:101], v28 offset:8192\n" \
  "v_mfma_f32_16x16x32_bf16 v[104:107], a[112:115], a[48:51], v[104:107]\n" \
  "ds_read_b64_tr_b16 a[102:103], v28 offset:9216\n" \
  "v_mfma_f32_16x16x32_bf16 v[136:139], a[112:115], a[56:59], v[136:139]\n" \
  "buffer_load_dwordx4 a[20:23], %[voa2], s[40:43], s52 offen offset:1024 sc1\n" \
  "v_mfma_f32_16x16x32_bf16 v[136:139], a[116:119], a[60:63], v[136:139]\n" \
  "s_waitcnt lgkmcnt(4)\n" \
  "v_mfma_f32_16x16x32_bf16 v[140:143], a[120:123], a[56:59], v[140:143]\n" \
  "ds_read_b64_tr_b16 a[104:105], v29 offset:0\n" \
  "v_mfma_f32_16x16x32_bf16 v[140:143], a[124:127], a[60:63], v[140:143]\n" \
  "ds_read_b64_tr_b16 a[106:107], v29 offset:1024\n" \
  "v_mfma_f32_16x16x32_bf16 v[108:111], a[124:127], a[52:55], v[108:111]\n" \
  "ds_read_b64_tr_b16 a[108:109], v29 offset:8192\n" \
  "v_mfma_f32_16x16x32_bf16 v[108:111], a[120:123], a[48:51], v[108:111]\n" \
  "ds_read_b64_tr_b16 a[110:111], v29 offset:9216\n" \
  "v_mfma_f32_16x16x32_bf16 v[76:79], a[120:123], a[40:43], v[76:79]\n" \
  "buffer_load_dwordx4 a[24:27], %[voa3], s[40:43], s52 offen offset:0 sc1\n" \
  "v_mfma_f32_16x16x32_bf16 v[76:79], a[124:127], a[44:47], v[76:79]\n" \
  "s_waitcnt lgkmcnt(4)\n" \
  "v_mfma_f32_16x16x32_bf16 v[80:83], a[96:99], a[40:43], v[80:83]\n" \
  "ds_read_b64_tr_b16 a[112:113], v30 offset:0\n" \
  "v_mfma_f32_16x16x32_bf16 v[80:83], a[100:103], a[44:47], v[80:83]\n" \
  "ds_read_b64_tr_b16 a[114:115], v30 offset:1024\n" \
  "v_mfma_f32_16x16x32_bf16 v[112:115], a[100:103], a[52:55], v[112:115]\n" \
  "ds_read_b64_tr_b16 a[116:117], v30 offset:8192\n" \
  "v_mfma_f32_16x16x32_bf16 v[112:115], a[96:99], a[48:51], v[112:115]\n" \
  "ds_read_b64_tr_b16 a[118:119], v30 offset:9216\n" \
  "v_mfma_f32_16x16x32_bf16 v[144:147], a[96:99], a[56:59], v[144:147]\n" \
  "buffer_load_dwordx4 a[28:31], %[voa3], s[40:43], s52 offen offset:1024 sc1\n" \
  "s_add_u32 s52, s52, 2048\n" \
  "s_add_u32 m0, %[ldsw], 114688\n" \
  "s_mov_b32 s56, 1605632\n" \
  "v_mfma_f32_16x16x32_bf16 v[144:147], a[100:103], a[60:63], v[144:147]\n" \
  "buffer_load_dwordx4 %[vow0], s[44:47], s56 offen lds\n" \
  "s_waitcnt lgkmcnt(4)\n" \
  "v_mfma_f32_16x16x32_bf16 v[148:151], a[104:107], a[56:59], v[148:151]\n" \
  "ds_read_b64_tr_b16 a[120:121], v31 offset:0\n" \
  "v_mfma_f32_16x16x32_bf16 v[148:151], a[108:111], a[60:63], v[148:151]\n" \
  "ds_read_b64_tr_b16 a[122:123], v31 offset:1024\n" \
  "v_mfma_f32_16x16x32_bf16 v[116:119], a[108:111], a[52:55], v[116:119]\n" \
  "ds_read_b64_tr_b16 a[124:125], v31 offset:8192\n" \
  "v_mfma_f32_16x16x32_bf16 v[116:119], a[104:107], a[48:51], v[116:119]\n" \
  "ds_read_b64_tr_b16 a[126:127], v31 offset:9216\n" \
  "s_add_u32 m0, %[ldsw], 115712\n" \
  "v_mfma_f32_16x16x32_bf16 v[84:87], a[104:107], a[40:43], v[84:87]\n" \
  "s_mov_b32 s57, 1613824\n" \
  "v_mfma_f32_16x16x32_bf16 v[84:87], a[108:111], a[44:47], v[84:87]\n" \
  "buffer_load_dwordx4 %[vow0], s[44:47], s57 offen lds\n" \
  "s_waitcnt vmcnt(19) lgkmcnt(4)\n" \
  "s_barrier\n" \
  "v_mfma_f32_16x16x32_bf16 v[88:91], a[112:115], a[40:43], v[88:91]\n" \
  "ds_read_b64_tr_b16 a[96:97], v24 offset:16384\n" \
  "v_mfma_f32_16x16x32_bf16 v[88:91], a[116:119], a[44:47], v[88:91]\n" \
  "ds_read_b64_tr_b16 a[98:99], v24 offset:17408\n" \
  "v_mfma_f32_16x16x32_bf16 v[120:123], a[116:119], a[52:55], v[120:123]\n" \
  "ds_read_b64_tr_b16 a[100:101], v24 offset:24576\n" \
  "s_add_u32 m0, %[ldsw], 116736\n" \
  "v_mfma_f32_16x16x32_bf16 v[120:123], a[112:115], a[48:51], v[120:123]\n" \
  "ds_read_b64_tr_b16 a[102:103], v24 offset:25600\n" \
  "s_mov_b32 s56, 1622016\n" \
  "v_mfma_f32_16x16x32_bf16 v[152:155], a[112:115], a[56:59], v[152:155]\n" \
  "buffer_load_dwordx4 %[vow1], s[44:47], s56 offen lds\n" \
  "v_mfma_f32_16x16x32_bf16 v[152:155], a[116:119], a[60:63], v[152:155]\n" \
  "s_waitcnt lgkmcnt(4)\n" \
  "v_mfma_f32_16x16x32_bf16 v[156:159], a[120:123], a[56:59], v[156:159]\n" \
  "ds_read_b64_tr_b16 a[104:105], v25 offset:16384\n" \
  "v_mfma_f32_16x16x32_bf16 v[156:159], a[124:127], a[60:63], v[156:159]\n" \
  "ds_read_b64_tr_b16 a[106:107], v25 offset:17408\n" \
  "v_mfma_f32_16x16x32_bf16 v[124:127], a[124:127], a[52:55], v[124:127]\n" \
  "ds_read_b64_tr_b16 a[108:109], v25 offset:24576\n" \
  "s_add_u32 m0, %[ldsw], 117760\n" \
  "v_mfma_f32_16x16x32_bf16 v[124:127], a[120:123], a[48:51], v[124:127]\n" \
  "ds_read_b64_tr_b16 a[110:111], v25 offset:25600\n" \
  "s_mov_b32 s57, 1630208\n" \
  "v_mfma_f32_16x16x32_bf16 v[92:95], a[120:123], a[40:43], v[92:95]\n" \
  "buffer_load_dwordx4 %[vow1], s[44:47], s57 offen lds\n" \
  "v_mfma_f32_16x16x32_bf16 v[92:95], a[124:127], a[44:47], v[92:95]\n" \
  "s_waitcnt vmcnt(10) lgkmcnt(4)\n" \
  "v_cmp_ne_u64 vcc, %[tag], %[ft]\n" \
  "s_mov_b32 s64, 262144\n" \
  "s_nop 0\n" \
  "s_cbranch_vccz LgoB%=\n" \
  "LpollB%=:\n" \
  "global_load_dwordx2 %[ft], %[fa], off sc1\n" \
  "s_waitcnt vmcnt(0)\n" \
  "v_cmp_ne_u64 vcc, %[tag], %[ft]\n" \
  "s_nop 1\n" \
  "s_cbranch_vccz LgoB%=\n" \
  "s_sub_u32 s64, s64, 1\n" \
  "s_cbranch_scc0 LpollB%=\n" \
  "LgoB%=:\n" \
  "v_mfma_f32_16x16x32_bf16 v[64:67], a[96:99], a[72:75], v[64:67]\n" \
  "ds_read_b64_tr_b16 a[112:113], v26 offset:16384\n" \
  "v_mfma_f32_16x16x32_bf16 v[64:67], a[100:103], a[76:79], v[64:67]\n" \
  "ds_read_b64_tr_b16 a[114:115], v26 offset:17408\n" \
  "v_mfma_f32_16x16x32_bf16 v[96:99], a[100:103], a[84:87], v[96:99]\n" \
  "ds_read_b64_tr_b16 a[116:117], v26 offset:24576\n" \
  "v_mfma_f32_16x16x32_bf16 v[96:99], a[96:99], a[80:83], v[96:99]\n" \
  "ds_read_b64_tr_b16 a[118:119], v26 offset:25600\n" \
  "v_mfma_f32_16x16x32_bf16 v[128:131], a[96:99], a[88:91], v[128:131]\n" \
  "buffer_load_dwordx4 a[40:43], %[voa1], s[40:43], s52 offen offset:0 sc1\n" \
  "v_mfma_f32_16x16x32_bf16 v[128:131], a[100:103], a[92:95], v[128:131]\n" \
  "buffer_load_dwordx4 a[44:47], %[voa1], s[40:43], s52 offen offset:1024 sc1\n" \
  "s_waitcnt lgkmcnt(4)\n" \
  "v_mfma_f32_16x16x32_bf16 v[132:135], a[104:107], a[88:91], v[132:135]\n" \
  "ds_read_b64_tr_b16 a[120:121], v27 offset:16384\n" \
  "v_mfma_f32_16x16x32_bf16 v[132:135], a[108:111], a[92:95], v[132:135]\n" \
  "ds_read_b64_tr_b16 a[122:123], v27 offset:17408\n" \
  "v_mfma_f32_16x16x32_bf16 v[100:103], a[108:111], a[84:87], v[100:103]\n" \
  "ds_read_b64_tr_b16 a[124:125], v27 offset:24576\n" \
  "v_mfma_f32_16x16x32_bf16 v[100:103], a[104:107], a[80:83], v[100:103]\n" \
  "ds_read_b64_tr_b16 a[126:127], v27 offset:25600\n" \
  "v_mfma_f32_16x16x32_bf16 v[68:71], a[104:107], a[72:75], v[68:71]\n" \
  "v_mfma_f32_16x16x32_bf16 v[68:71], a[108:111], a[76:79], v[68:71]\n" \
  "buffer_load_dwordx4 a[48:51], %[voa2], s[40:43], s52 offen offset:0 sc1\n" \
  "s_waitcnt lgkmcnt(4)\n" \
  "v_mfma_f32_16x16x32_bf16 v[72:75], a[112:115], a[72:75], v[72:75]\n" \
  "ds_read_b64_tr_b16 a[96:97], v28 offset:16384\n" \
  "v_mfma_f32_16x16x32_bf16 v[72:75], a[116:119], a[76:79], v[72:75]\n" \
  "ds_read_b64_tr_b16 a[98:99], v28 offset:17408\n" \
  "v_mfma_f32_16x16x32_bf16 v[104:107], a[116:119], a[84:87], v[104:107]\n" \
  "ds_read_b64_tr_b16 a[100:101], v28 offset:24576\n" \
  "v_mfma_f32_16x16x32_bf16 v[104:107], a[112:115], a[80:83], v[104:107]\n" \
  "ds_read_b64_tr_b16 a[102:103], v28 offset:25600\n" \
  "v_mfma_f32_16x16x32_bf16 v[136:139], a[112:115], a[88:91], v[136:139]\n" \
  "buffer_load_dwordx4 a[52:55], %[voa2], s[40:43], s52 offen offset:1024 sc1\n" \
  "v_mfma_f32_16x16x32_bf16 v[136:139], a[116:119], a[92:95], v[136:139]\n" \
  "s_waitcnt lgkmcnt(4)\n" \
  "v_mfma_f32_16x16x32_bf16 v[140:143], a[120:123], a[88:91], v[140:143]\n" \
  "ds_read_b64_tr_b16 a[104:105], v29 offset:16384\n" \
  "v_mfma_f32_16x16x32_bf16 v[140:143], a[124:127], a[92:95], v[140:143]\n" \
  "ds_read_b64_tr_b16 a[106:107], v29 offset:17408\n" \
  "v_mfma_f32_16x16x32_bf16 v[108:111], a[124:127], a[84:87], v[108:111]\n" \
  "ds_read_b64_tr_b16 a[108:109], v29 offset:24576\n" \
  "v_mfma_f32_16x16x32_bf16 v[108:111], a[120:123], a[80:83], v[108:111]\n" \
  "ds_read_b64_tr_b16 a[110:111], v29 offset:25600\n" \
  "v_mfma_f32_16x16x32_bf16 v[76:79], a[120:123], a[72:75], v[76:79]\n" \
  "buffer_load_dwordx4 a[56:59], %[voa3], s[40:43], s52 offen offset:0 sc1\n" \
  "v_mfma_f32_16x16x32_bf16 v[76:79], a[124:127], a[76:79], v[76:79]\n" \
  "s_waitcnt lgkmcnt(4)\n" \
  "v_mfma_f32_16x16x32_bf16 v[80:83], a[96:99], a[72:75], v[80:83]\n" \
  "ds_read_b64_tr_b16 a[112:113], v30 offset:16384\n" \
  "v_mfma_f32_16x16x32_bf16 v[80:83], a[100:103], a[76:79], v[80:83]\n" \
  "ds_read_b64_tr_b16 a[114:115], v30 offset:17408\n" \
  "v_mfma_f32_16x16x32_bf16 v[112:115], a[100:103], a[84:87], v[112:115]\n" \
  "ds_read_b64_tr_b16 a[116:117], v30 offset:24576\n" \
  "v_mfma_f32_16x16x32_bf16 v[112:115], a[96:99], a[80:83], v[112:115]\n" \
  "ds_read_b64_tr_b16 a[118:119], v30 offset:25600\n" \
  "v_mfma_f32_16x16x32_bf16 v[144:147], a[96:99], a[88:91], v[144:147]\n" \
  "buffer_load_dwordx4 a[60:63], %[voa3], s[40:43], s52 offen offset:1024 sc1\n" \
  "s_add_u32 s52, s52, 2048\n" \
  "s_add_u32 m0, %[ldsw], 131072\n" \
  "s_bfe_u32 s58, %[w2t], 131072\n" "s_lshl_b32 s58, s58, 17\n" "s_add_u32 s56, s58, 131072\n" \
  "v_mfma_f32_16x16x32_bf16 v[144:147], a[100:103], a[92:95], v[144:147]\n" \
  "buffer_load_dwordx4 %[vow0], s[44:47], s56 offen lds\n" \
  "s_waitcnt lgkmcnt(4)\n" \
  "v_mfma_f32_16x16x32_bf16 v[148:151], a[104:107], a[88:91], v[148:151]\n" \
  "ds_read_b64_tr_b16 a[120:121], v31 offset:16384\n" \
  "v_mfma_f32_16x16x32_bf16 v[148:151], a[108:111], a[92:95], v[148:151]\n" \
  "ds_read_b64_tr_b16 a[122:123], v31 offset:17408\n" \
  "v_mfma_f32_16x16x32_bf16 v[116:119], a[108:111], a[84:87], v[116:119]\n" \
  "ds_read_b64_tr_b16 a[124:125], v31 offset:24576\n" \
  "v_mfma_f32_16x16x32_bf16 v[116:119], a[104:107], a[80:83], v[116:119]\n" \
  "ds_read_b64_tr_b16 a[126:127], v31 offset:25600\n" \
  "s_add_u32 m0, %[ldsw], 132096\n" \
  "v_mfma_f32_16x16x32_bf16 v[84:87], a[104:107], a[72:75], v[84:87]\n" \
  "s_add_u32 s57, s56, 8192\n" \
  "v_mfma_f32_16x16x32_bf16 v[84:87], a[108:111], a[76:79], v[84:87]\n" \
  "buffer_load_dwordx4 %[vow0], s[44:47], s57 offen lds\n" \
  "s_waitcnt vmcnt(19) lgkmcnt(4)\n" \
  "s_barrier\n" \
  "v_mfma_f32_16x16x32_bf16 v[88:91], a[112:115], a[72:75], v[88:91]\n" \
  "ds_read_b64_tr_b16 a[96:97], v24 offset:32768\n" \
  "v_mfma_f32_16x16x32_bf16 v[88:91], a[116:119], a[76:79], v[88:91]\n" \
  "ds_read_b64_tr_b16 a[98:99], v24 offset:33792\n" \
  "v_mfma_f32_16x16x32_bf16 v[120:123], a[116:119], a[84:87], v[120:123]\n" \
  "ds_read_b64_tr_b16 a[100:101], v24 offset:40960\n" \
  "s_add_u32 m0, %[ldsw], 133120\n" \
  "v_mfma_f32_16x16x32_bf16 v[120:123], a[112:115], a[80:83], v[120:123]\n" \
  "ds_read_b64_tr_b16 a[102:103], v24 offset:41984\n" \
  "s_bfe_u32 s58, %[w2t], 131074\n" "s_lshl_b32 s58, s58, 17\n" "s_add_u32 s56, s58, 147456\n" \
  "v_mfma_f32_16x16x32_bf16 v[152:155], a[112:115], a[88:91], v[152:155]\n" \
  "buffer_load_dwordx4 %[vow1], s[44:47], s56 offen lds\n" \
  "v_mfma_f32_16x16x32_bf16 v[152:155], a[116:119], a[92:95], v[152:155]\n" \
  "s_waitcnt lgkmcnt(4)\n" \
  "v_mfma_f32_16x16x32_bf16 v[156:159], a[120:123], a[88:91], v[156:159]\n" \
  "ds_read_b64_tr_b16 a[104:105], v25 offset:32768\n" \
  "v_mfma_f32_16x16x32_bf16 v[156:159], a[124:127], a[92:95], v[156:159]\n" \
  "ds_read_b64_tr_b16 a[106:107], v25 offset:33792\n" \
  "v_mfma_f32_16x16x32_bf16 v[124:127], a[124:127], a[84:87], v[124:127]\n" \
  "ds_read_b64_tr_b16 a[108:109], v25 offset:40960\n" \
  "s_add_u32 m0, %[ldsw], 134144\n" \
  "v_mfma_f32_16x16x32_bf16 v[124:127], a[120:123], a[80:83], v[124:127]\n" \
  "ds_read_b64_tr_b16 a[110:111], v25 offset:41984\n" \
  "s_add_u32 s57, s56, 8192\n" \
  "v_mfma_f32_16x16x32_bf16 v[92:95], a[120:123], a[72:75], v[92:95]\n" \
  "buffer_load_dwordx4 %[vow1], s[44:47], s57 offen lds\n" \
  "v_mfma_f32_16x16x32_bf16 v[92:95], a[124:127], a[76:79], v[92:95]\n" \
  "s_waitcnt vmcnt(14) lgkmcnt(4)\n" \
  "v_mfma_f32_16x16x32_bf16 v[64:67], a[96:99], a[8:11], v[64:67]\n" \
  "ds_read_b64_tr_b16 a[112:113], v26 offset:32768\n" \
  "v_mfma_f32_16x16x32_bf16 v[64:67], a[100:103], a[12:15], v[64:67]\n" \
  "ds_read_b64_tr_b16 a[114:115], v26 offset:33792\n" \
  "v_mfma_f32_16x16x32_bf16 v[96:99], a[100:103], a[20:23], v[96:99]\n" \
  "ds_read_b64_tr_b16 a[116:117], v26 offset:40960\n" \
  "v_mfma_f32_16x16x32_bf16 v[96:99], a[96:99], a[16:19], v[96:99]\n" \
  "ds_read_b64_tr_b16 a[118:119], v26 offset:41984\n" \
  "v_mfma_f32_16x16x32_bf16 v[128:131], a[96:99], a[24:27], v[128:131]\n" \
  "buffer_load_dwordx4 a[72:75], %[voa1], s[40:43], s52 offen offset:0 sc1\n" \
  "v_mfma_f32_16x16x32_bf16 v[128:131], a[100:103], a[28:31], v[128:131]\n" \
  "buffer_load_dwordx4 a[76:79], %[voa1], s[40:43], s52 offen offset:1024 sc1\n" \
  "s_waitcnt lgkmcnt(4)\n" \
  "v_mfma_f32_16x16x32_bf16 v[132:135], a[104:107], a[24:27], v[132:135]\n" \
  "ds_read_b64_tr_b16 a[120:121], v27 offset:32768\n" \
  "v_mfma_f32_16x16x32_bf16 v[132:135], a[108:111], a[28:31], v[132:135]\n" \
  "ds_read_b64_tr_b16 a[122:123], v27 offset:33792\n" \
  "v_mfma_f32_16x16x32_bf16 v[100:103], a[108:111], a[20:23], v[100:103]\n" \
  "ds_read_b64_tr_b16 a[124:125], v27 offset:40960\n" \
  "v_mfma_f32_16x16x32_bf16 v[100:103], a[104:107], a[16:19], v[100:103]\n" \
  "ds_read_b64_tr_b16 a[126:127], v27 offset:41984\n" \
  "v_mfma_f32_16x16x32_bf16 v[68:71], a[104:107], a[8:11], v[68:71]\n" \
  "v_mfma_f32_16x16x32_bf16 v[68:71], a[108:111], a[12:15], v[68:71]\n" \
  "buffer_load_dwordx4 a[80:83], %[voa2], s[40:43], s52 offen offset:0 sc1\n" \
  "s_waitcnt lgkmcnt(4)\n" \
  "v_mfma_f32_16x16x32_bf16 v[72:75], a[112:115], a[8:11], v[72:75]\n" \
  "ds_read_b64_tr_b16 a[96:97], v28 offset:32768\n" \
  "v_mfma_f32_16x16x32_bf16 v[72:75], a[116:119], a[12:15], v[72:75]\n" \
  "ds_read_b64_tr_b16 a[98:99], v28 offset:33792\n" \
  "v_mfma_f32_16x16x32_bf16 v[104:107], a[116:119], a[20:23], v[104:107]\n" \
  "ds_read_b64_tr_b16 a[100:101], v28 offset:40960\n" \
  "v_mfma_f32_16x16x32_bf16 v[104:107], a[112:115], a[16:19], v[104:107]\n" \
  "ds_read_b64_tr_b16 a[102:103], v28 offset:41984\n" \
  "v_mfma_f32_16x16x32_bf16 v[136:139], a[112:115], a[24:27], v[136:139]\n" \
  "buffer_load_dwordx4 a[84:87], %[voa2], s[40:43], s52 offen offset:1024 sc1\n" \
  "v_mfma_f32_16x16x32_bf16 v[136:139], a[116:119], a[28:31], v[136:139]\n" \
  "s_waitcnt lgkmcnt(4)\n" \
  "v_mfma_f32_16x16x32_bf16 v[140:143], a[120:123], a[24:27], v[140:143]\n" \
  "ds_read_b64_tr_b16 a[104:105], v29 offset:32768\n" \
  "v_mfma_f32_16x16x32_bf16 v[140:143], a[124:127], a[28:31], v[140:143]\n" \
  "ds_read_b64_tr_b16 a[106:107], v29 offset:33792\n" \
  "v_mfma_f32_16x16x32_bf16 v[108:111], a[124:127], a[20:23], v[108:111]\n" \
  "ds_read_b64_tr_b16 a[108:109], v29 offset:40960\n" \
  "v_mfma_f32_16x16x32_bf16 v[108:111], a[120:123], a[16:19], v[108:111]\n" \
  "ds_read_b64_tr_b16 a[110:111], v29 offset:41984\n" \
  "v_mfma_f32_16x16x32_bf16 v[76:79], a[120:123], a[8:11], v[76:79]\n" \
  "buffer_load_dwordx4 a[88:91], %[voa3], s[40:43], s52 offen offset:0 sc1\n" \
  "v_mfma_f32_16x16x32_bf16 v[76:79], a[124:127], a[12:15], v[76:79]\n" \
  "s_waitcnt lgkmcnt(4)\n" \
  "v_mfma_f32_16x16x32_bf16 v[80:83], a[96:99], a[8:11], v[80:83]\n" \
  "ds_read_b64_tr_b16 a[112:113], v30 offset:32768\n" \
  "v_mfma_f32_16x16x32_bf16 v[80:83], a[100:103], a[12:15], v[80:83]\n" \
  "ds_read_b64_tr_b16 a[114:115], v30 offset:33792\n" \
  "v_mfma_f32_16x16x32_bf16 v[112:115], a[100:103], a[20:23], v[112:115]\n" \
  "ds_read_b64_tr_b16 a[116:117], v30 offset:40960\n" \
  "v_mfma_f32_16x16x32_bf16 v[112:115], a[96:99], a[16:19], v[112:115]\n" \
  "ds_read_b64_tr_b16 a[118:119], v30 offset:41984\n" \
  "v_mfma_f32_16x16x32_bf16 v[144:147], a[96:99], a[24:27], v[144:147]\n" \
  "buffer_load_dwordx4 a[92:95], %[voa3], s[40:43], s52 offen offset:1024 sc1\n" \
  "s_add_u32 s52, s52, 2048\n" \
  "s_add_u32 m0, %[ldsw], 147456\n" \
  "s_bfe_u32 s58, %[w2t], 131076\n" "s_lshl_b32 s58, s58, 17\n" "s_add_u32 s56, s58, 393216\n" \
  "v_mfma_f32_16x16x32_bf16 v[144:147], a[100:103], a[28:31], v[144:147]\n" \
  "buffer_load_dwordx4 %[vow0], s[44:47], s56 offen lds\n" \
  "s_waitcnt lgkmcnt(4)\n" \
  "v_mfma_f32_16x16x32_bf16 v[148:151], a[104:107], a[24:27], v[148:151]\n" \
  "ds_read_b64_tr_b16 a[120:121], v31 offset:32768\n" \
  "v_mfma_f32_16x16x32_bf16 v[148:151], a[108:111], a[28:31], v[148:151]\n" \
  "ds_read_b64_tr_b16 a[122:123], v31 offset:33792\n" \
  "v_mfma_f32_16x16x32_bf16 v[116:119], a[108:111], a[20:23], v[116:119]\n" \
  "ds_read_b64_tr_b16 a[124:125], v31 offset:40960\n" \
  "v_mfma_f32_16x16x32_bf16 v[116:119], a[104:107], a[16:19], v[116:119]\n" \
  "ds_read_b64_tr_b16 a[126:127], v31 offset:41984\n" \
  "s_add_u32 m0, %[ldsw], 148480\n" \
  "v_mfma_f32_16x16x32_bf16 v[84:87], a[104:107], a[8:11], v[84:87]\n" \
  "s_add_u32 s57, s56, 8192\n" \
  "v_mfma_f32_16x16x32_bf16 v[84:87], a[108:111], a[12:15], v[84:87]\n" \
  "buffer_load_dwordx4 %[vow0], s[44:47], s57 offen lds\n" \
  "s_waitcnt vmcnt(18) lgkmcnt(4)\n" \
  "s_barrier\n" \
  "v_mfma_f32_16x16x32_bf16 v[88:91], a[112:115], a[8:11], v[88:91]\n" \
  "ds_read_b64_tr_b16 a[96:97], v24 offset:49152\n" \
  "v_mfma_f32_16x16x32_bf16 v[88:91], a[116:119], a[12:15], v[88:91]\n" \
  "ds_read_b64_tr_b16 a[98:99], v24 offset:50176\n" \
  "v_mfma_f32_16x16x32_bf16 v[120:123], a[116:119], a[20:23], v[120:123]\n" \
  "ds_read_b64_tr_b16 a[100:101], v24 offset:57344\n" \
  "s_add_u32 m0, %[ldsw], 149504\n" \
  "v_mfma_f32_16x16x32_bf16 v[120:123], a[112:115], a[16:19], v[120:123]\n" \
  "ds_read_b64_tr_b16 a[102:103], v24 offset:58368\n" \
  "s_bfe_u32 s58, %[w2t], 131078\n" "s_lshl_b32 s58, s58, 17\n" "s_add_u32 s56, s58, 409600\n" \
  "v_mfma_f32_16x16x32_bf16 v[152:155], a[112:115], a[24:27], v[152:155]\n" \
  "buffer_load_dwordx4 %[vow1], s[44:47], s56 offen lds\n" \
  "v_mfma_f32_16x16x32_bf16 v[152:155], a[116:119], a[28:31], v[152:155]\n" \
  "s_waitcnt lgkmcnt(4)\n" \
  "v_mfma_f32_16x16x32_bf16 v[156:159], a[120:123], a[24:27], v[156:159]\n" \
  "ds_read_b64_tr_b16 a[104:105], v25 offset:49152\n" \
  "v_mfma_f32_16x16x32_bf16 v[156:159], a[124:127], a[28:31], v[156:159]\n" \
  "ds_read_b64_tr_b16 a[106:107], v25 offset:50176\n" \
  "v_mfma_f32_16x16x32_bf16 v[124:127], a[124:127], a[20:23], v[124:127]\n" \
  "ds_read_b64_tr_b16 a[108:109], v25 offset:57344\n" \
  "s_add_u32 m0, %[ldsw], 150528\n" \
  "v_mfma_f32_16x16x32_bf16 v[124:127], a[120:123], a[16:19], v[124:127]\n" \
  "ds_read_b64_tr_b16 a[110:111], v25 offset:58368\n" \
  "s_add_u32 s57, s56, 8192\n" \
  "v_mfma_f32_16x16x32_bf16 v[92:95], a[120:123], a[8:11], v[92:95]\n" \
  "buffer_load_dwordx4 %[vow1], s[44:47], s57 offen lds\n" \
  "v_mfma_f32_16x16x32_bf16 v[92:95], a[124:127], a[12:15], v[92:95]\n" \
  "s_waitcnt vmcnt(14) lgkmcnt(4)\n" \
  "v_mfma_f32_16x16x32_bf16 v[64:67], a[96:99], a[40:43], v[64:67]\n" \
  "ds_read_b64_tr_b16 a[112:113], v26 offset:49152\n" \
  "v_add_u32 v24, 131072, %[vl0]\n" \
  "v_mfma_f32_16x16x32_bf16 v[64:67], a[100:103], a[44:47], v[64:67]\n" \
  "ds_read_b64_tr_b16 a[114:115], v26 offset:50176\n" \
  "v_add_u32 v25, 131072, %[vl1]\n" \
  "v_mfma_f32_16x16x32_bf16 v[96:99], a[100:103], a[52:55], v[96:99]\n" \
  "ds_read_b64_tr_b16 a[116:117], v26 offset:57344\n" \
  "v_mfma_f32_16x16x32_bf16 v[96:99], a[96:99], a[48:51], v[96:99]\n" \
  "ds_read_b64_tr_b16 a[118:119], v26 offset:58368\n" \
  "v_mfma_f32_16x16x32_bf16 v[128:131], a[96:99], a[56:59], v[128:131]\n" \
  "buffer_load_dwordx4 a[8:11], %[voa1], s[40:43], s52 offen offset:0 sc1\n" \
  "v_add_u32 v26, 131072, %[vl2]\n" \
  "v_mfma_f32_16x16x32_bf16 v[128:131], a[100:103], a[60:63], v[128:131]\n" \
  "buffer_load_dwordx4 a[12:15], %[voa1], s[40:43], s52 offen offset:1024 sc1\n" \
  "s_waitcnt lgkmcnt(4)\n" \
  "v_mfma_f32_16x16x32_bf16 v[132:135], a[104:107], a[56:59], v[132:135]\n" \
  "ds_read_b64_tr_b16 a[120:121], v27 offset:49152\n" \
  "v_mfma_f32_16x16x32_bf16 v[132:135], a[108:111], a[60:63], v[132:135]\n" \
  "ds_read_b64_tr_b16 a[122:123], v27 offset:50176\n" \
  "v_mfma_f32_16x16x32_bf16 v[100:103], a[108:111], a[52:55], v[100:103]\n" \
  "ds_read_b64_tr_b16 a[124:125], v27 offset:57344\n" \
  "v_mfma_f32_16x16x32_bf16 v[100:103], a[104:107], a[48:51], v[100:103]\n" \
  "ds_read_b64_tr_b16 a[126:127], v27 offset:58368\n" \
  "v_mfma_f32_16x16x32_bf16 v[68:71], a[104:107], a[40:43], v[68:71]\n" \
  "v_add_u32 v27, 131072, %[vl3]\n" \
  "v_mfma_f32_16x16x32_bf16 v[68:71], a[108:111], a[44:47], v[68:71]\n" \
  "buffer_load_dwordx4 a[16:19], %[voa2], s[40:43], s52 offen offset:0 sc1\n" \
  "s_waitcnt lgkmcnt(4)\n" \
  "v_mfma_f32_16x16x32_bf16 v[72:75], a[112:115], a[40:43], v[72:75]\n" \
  "ds_read_b64_tr_b16 a[96:97], v28 offset:49152\n" \
  "v_mfma_f32_16x16x32_bf16 v[72:75], a[116:119], a[44:47], v[72:75]\n" \
  "ds_read_b64_tr_b16 a[98:99], v28 offset:50176\n" \
  "v_mfma_f32_16x16x32_bf16 v[104:107], a[116:119], a[52:55], v[104:107]\n" \
  "ds_read_b64_tr_b16 a[100:101], v28 offset:57344\n" \
  "v_mfma_f32_16x16x32_bf16 v[104:107], a[112:115], a[48:51], v[104:107]\n" \
  "ds_read_b64_tr_b16 a[102:103], v28 offset:58368\n" \
  "v_mfma_f32_16x16x32_bf16 v[136:139], a[112:115], a[56:59], v[136:139]\n" \
  "buffer_load_dwordx4 a[20:23], %[voa2], s[40:43], s52 offen offset:1024 sc1\n" \
  "v_add_u32 v28, 131072, %[vl4]\n" \
  "v_mfma_f32_16x16x32_bf16 v[136:139], a[116:119], a[60:63], v[136:139]\n" \
  "s_waitcnt lgkmcnt(4)\n" \
  "v_mfma_f32_16x16x32_bf16 v[140:143], a[120:123], a[56:59], v[140:143]\n" \
  "ds_read_b64_tr_b16 a[104:105], v29 offset:49152\n" \
  "v_mfma_f32_16x16x32_bf16 v[140:143], a[124:127], a[60:63], v[140:143]\n" \
  "ds_read_b64_tr_b16 a[106:107], v29 offset:50176\n" \
  "v_mfma_f32_16x16x32_bf16 v[108:111], a[124:127], a[52:55], v[108:111]\n" \
  "ds_read_b64_tr_b16 a[108:109], v29 offset:57344\n" \
  "v_mfma_f32_16x16x32_bf16 v[108:111], a[120:123], a[48:51], v[108:111]\n" \
  "ds_read_b64_tr_b16 a[110:111], v29 offset:58368\n" \
  "v_mfma_f32_16x16x32_bf16 v[76:79], a[120:123], a[40:43], v[76:79]\n" \
  "buffer_load_dwordx4 a[24:27], %[voa3], s[40:43], s52 offen offset:0 sc1\n" \
  "v_add_u32 v29, 131072, %[vl5]\n" \
  "v_mfma_f32_16x16x32_bf16 v[76:79], a[124:127], a[44:47], v[76:79]\n" \
  "s_waitcnt lgkmcnt(4)\n" \
  "v_mfma_f32_16x16x32_bf16 v[80:83], a[96:99], a[40:43], v[80:83]\n" \
  "ds_read_b64_tr_b16 a[112:113], v30 offset:49152\n" \
  "v_mfma_f32_16x16x32_bf16 v[80:83], a[100:103], a[44:47], v[80:83]\n" \
  "ds_read_b64_tr_b16 a[114:115], v30 offset:50176\n" \
  "v_mfma_f32_16x16x32_bf16 v[112:115], a[100:103], a[52:55], v[112:115]\n" \
  "ds_read_b64_tr_b16 a[116:117], v30 offset:57344\n" \
  "v_mfma_f32_16x16x32_bf16 v[112:115], a[96:99], a[48:51], v[112:115]\n" \
  "ds_read_b64_tr_b16 a[118:119], v30 offset:58368\n" \
  "v_mfma_f32_16x16x32_bf16 v[144:147], a[96:99], a[56:59], v[144:147]\n" \
  "buffer_load_dwordx4 a[28:31], %[voa3], s[40:43], s52 offen offset:1024 sc1\n" \
  "s_add_u32 s52, s52, 2048\n" \
  "s_add_u32 m0, %[ldsw], 0\n" \
  "s_bfe_u32 s58, %[w2t], 131080\n" "s_lshl_b32 s58, s58, 17\n" "s_add_u32 s56, s58, 655360\n" \
  "v_mfma_f32_16x16x32_bf16 v[144:147], a[100:103], a[60:63], v[144:147]\n" \
  "buffer_load_dwordx4 %[vow0], s[44:47], s56 offen lds\n" \
  "s_waitcnt lgkmcnt(4)\n" \
  "v_mfma_f32_16x16x32_bf16 v[148:151], a[104:107], a[56:59], v[148:151]\n" \
  "ds_read_b64_tr_b16 a[120:121], v31 offset:49152\n" \
  "v_add_u32 v30, 131072, %[vl6]\n" \
  "v_mfma_f32_16x16x32_bf16 v[148:151], a[108:111], a[60:63], v[148:151]\n" \
  "ds_read_b64_tr_b16 a[122:123], v31 offset:50176\n" \
  "v_mfma_f32_16x16x32_bf16 v[116:119], a[108:111], a[52:55], v[116:119]\n" \
  "ds_read_b64_tr_b16 a[124:125], v31 offset:57344\n" \
  "v_mfma_f32_16x16x32_bf16 v[116:119], a[104:107], a[48:51], v[116:119]\n" \
  "ds_read_b64_tr_b16 a[126:127], v31 offset:58368\n" \
  "s_add_u32 m0, %[ldsw], 1024\n" \
  "v_mfma_f32_16x16x32_bf16 v[84:87], a[104:107], a[40:43], v[84:87]\n" \
  "s_add_u32 s57, s56, 8192\n" \
  "v_add_u32 v31, 131072, %[vl7]\n" \
  "v_mfma_f32_16x16x32_bf16 v[84:87], a[108:111], a[44:47], v[84:87]\n" \
  "buffer_load_dwordx4 %[vow0], s[44:47], s57 offen lds\n" \
  "s_waitcnt vmcnt(18) lgkmcnt(4)\n" \
  "s_barrier\n" \
  "v_mfma_f32_16x16x32_bf16 v[88:91], a[112:115], a[40:43], v[88:91]\n" \
  "ds_read_b64_tr_b16 a[96:97], v24 offset:0\n" \
  "v_mfma_f32_16x16x32_bf16 v[88:91], a[116:119], a[44:47], v[88:91]\n" \
  "ds_read_b64_tr_b16 a[98:99], v24 offset:1024\n" \
  "v_mfma_f32_16x16x32_bf16 v[120:123], a[116:119], a[52:55], v[120:123]\n" \
  "ds_read_b64_tr_b16 a[100:101], v24 offset:8192\n" \
  "s_add_u32 m0, %[ldsw], 2048\n" \
  "v_mfma_f32_16x16x32_bf16 v[120:123], a[112:115], a[48:51], v[120:123]\n" \
  "ds_read_b64_tr_b16 a[102:103], v24 offset:9216\n" \
  "s_bfe_u32 s58, %[w2t], 131082\n" "s_lshl_b32 s58, s58, 17\n" "s_add_u32 s56, s58, 671744\n" \
  "v_mfma_f32_16x16x32_bf16 v[152:155], a[112:115], a[56:59], v[152:155]\n" \
  "buffer_load_dwordx4 %[vow1], s[44:47], s56 offen lds\n" \
  "v_mfma_f32_16x16x32_bf16 v[152:155], a[116:119], a[60:63], v[152:155]\n" \
  "s_waitcnt lgkmcnt(4)\n" \
  "v_mfma_f32_16x16x32_bf16 v[156:159], a[120:123], a[56:59], v[156:159]\n" \
  "ds_read_b64_tr_b16 a[104:105], v25 offset:0\n" \
  "v_mfma_f32_16x16x32_bf16 v[156:159], a[124:127], a[60:63], v[156:159]\n" \
  "ds_read_b64_tr_b16 a[106:107], v25 offset:1024\n" \
  "v_mfma_f32_16x16x32_bf16 v[124:127], a[124:127], a[52:55], v[124:127]\n" \
  "ds_read_b64_tr_b16 a[108:109], v25 offset:8192\n" \
  "s_add_u32 m0, %[ldsw], 3072\n" \
  "v_mfma_f32_16x16x32_bf16 v[124:127], a[120:123], a[48:51], v[124:127]\n" \
  "ds_read_b64_tr_b16 a[110:111], v25 offset:9216\n" \
  "s_add_u32 s57, s56, 8192\n" \
  "v_mfma_f32_16x16x32_bf16 v[92:95], a[120:123], a[40:43], v[92:95]\n" \
  "buffer_load_dwordx4 %[vow1], s[44:47], s57 offen lds\n" \
  "v_mfma_f32_16x16x32_bf16 v[92:95], a[124:127], a[44:47], v[92:95]\n" \
  "s_waitcnt vmcnt(14) lgkmcnt(4)\n" \
  "v_mfma_f32_16x16x32_bf16 v[64:67], a[96:99], a[72:75], v[64:67]\n" \
  "ds_read_b64_tr_b16 a[112:113], v26 offset:0\n" \
  "v_mfma_f32_16x16x32_bf16 v[64:67], a[100:103], a[76:79], v[64:67]\n" \
  "ds_read_b64_tr_b16 a[114:115], v26 offset:1024\n" \
  "v_mfma_f32_16x16x32_bf16 v[96:99], a[100:103], a[84:87], v[96:99]\n" \
  "ds_read_b64_tr_b16 a[116:117], v26 offset:8192\n" \
  "v_mfma_f32_16x16x32_bf16 v[96:99], a[96:99], a[80:83], v[96:99]\n" \
  "ds_read_b64_tr_b16 a[118:119], v26 offset:9216\n" \
  "v_mfma_f32_16x16x32_bf16 v[128:131], a[96:99], a[88:91], v[128:131]\n" \
  "buffer_load_dwordx4 a[40:43], %[voa1], s[40:43], s52 offen offset:0 sc1\n" \
  "v_mfma_f32_16x16x32_bf16 v[128:131], a[100:103], a[92:95], v[128:131]\n" \
  "buffer_load_dwordx4 a[44:47], %[voa1], s[40:43], s52 offen offset:1024 sc1\n" \
  "s_waitcnt lgkmcnt(4)\n" \
  "v_mfma_f32_16x16x32_bf16 v[132:135], a[104:107], a[88:91], v[132:135]\n" \
  "ds_read_b64_tr_b16 a[120:121], v27 offset:0\n" \
  "v_mfma_f32_16x16x32_bf16 v[132:135], a[108:111], a[92:95], v[132:135]\n" \
  "ds_read_b64_tr_b16 a[122:123], v27 offset:1024\n" \
  "v_mfma_f32_16x16x32_bf16 v[100:103], a[108:111], a[84:87], v[100:103]\n" \
  "ds_read_b64_tr_b16 a[124:125], v27 offset:8192\n" \
  "v_mfma_f32_16x16x32_bf16 v[100:103], a[104:107], a[80:83], v[100:103]\n" \
  "ds_read_b64_tr_b16 a[126:127], v27 offset:9216\n" \
  "v_mfma_f32_16x16x32_bf16 v[68:71], a[104:107], a[72:75], v[68:71]\n" \
  "v_mfma_f32_16x16x32_bf16 v[68:71], a[108:111], a[76:79], v[68:71]\n" \
  "buffer_load_dwordx4 a[48:51], %[voa2], s[40:43], s52 offen offset:0 sc1\n" \
  "s_waitcnt lgkmcnt(4)\n" \
  "v_mfma_f32_16x16x32_bf16 v[72:75], a[112:115], a[72:75], v[72:75]\n" \
  "ds_read_b64_tr_b16 a[96:97], v28 offset:0\n" \
  "v_mfma_f32_16x16x32_bf16 v[72:75], a[116:119], a[76:79], v[72:75]\n" \
  "ds_read_b64_tr_b16 a[98:99], v28 offset:1024\n" \
  "v_mfma_f32_16x16x32_bf16 v[104:107], a[116:119], a[84:87], v[104:107]\n" \
  "ds_read_b64_tr_b16 a[100:101], v28 offset:8192\n" \
  "v_mfma_f32_16x16x32_bf16 v[104:107], a[112:115], a[80:83], v[104:107]\n" \
  "ds_read_b64_tr_b16 a[102:103], v28 offset:9216\n" \
  "v_mfma_f32_16x16x32_bf16 v[136:139], a[112:115], a[88:91], v[136:139]\n" \
  "buffer_load_dwordx4 a[52:55], %[voa2], s[40:43], s52 offen offset:1024 sc1\n" \
  "v_mfma_f32_16x16x32_bf16 v[136:139], a[116:119], a[92:95], v[136:139]\n" \
  "s_waitcnt lgkmcnt(4)\n" \
  "v_mfma_f32_16x16x32_bf16 v[140:143], a[120:123], a[88:91], v[140:143]\n" \
  "ds_read_b64_tr_b16 a[104:105], v29 offset:0\n" \
  "v_mfma_f32_16x16x32_bf16 v[140:143], a[124:127], a[92:95], v[140:143]\n" \
  "ds_read_b64_tr_b16 a[106:107], v29 offset:1024\n" \
  "v_mfma_f32_16x16x32_bf16 v[108:111], a[124:127], a[84:87], v[108:111]\n" \
  "ds_read_b64_tr_b16 a[108:109], v29 offset:8192\n" \
  "v_mfma_f32_16x16x32_bf16 v[108:111], a[120:123], a[80:83], v[108:111]\n" \
  "ds_read_b64_tr_b16 a[110:111], v29 offset:9216\n" \
  "v_mfma_f32_16x16x32_bf16 v[76:79], a[120:123], a[72:75], v[76:79]\n" \
  "buffer_load_dwordx4 a[56:59], %[voa3], s[40:43], s52 offen offset:0 sc1\n" \
  "v_mfma_f32_16x16x32_bf16 v[76:79], a[124:127], a[76:79], v[76:79]\n" \
  "s_waitcnt lgkmcnt(4)\n" \
  "v_mfma_f32_16x16x32_bf16 v[80:83], a[96:99], a[72:75], v[80:83]\n" \
  "ds_read_b64_tr_b16 a[112:113], v30 offset:0\n" \
  "v_mfma_f32_16x16x32_bf16 v[80:83], a[100:103], a[76:79], v[80:83]\n" \
  "ds_read_b64_tr_b16 a[114:115], v30 offset:1024\n" \
  "v_mfma_f32_16x16x32_bf16 v[112:115], a[100:103], a[84:87], v[112:115]\n" \
  "ds_read_b64_tr_b16 a[116:117], v30 offset:8192\n" \
  "v_mfma_f32_16x16x32_bf16 v[112:115], a[96:99], a[80:83], v[112:115]\n" \
  "ds_read_b64_tr_b16 a[118:119], v30 offset:9216\n" \
  "v_mfma_f32_16x16x32_bf16 v[144:147], a[96:99], a[88:91], v[144:147]\n" \
  "buffer_load_dwordx4 a[60:63], %[voa3], s[40:43], s52 offen offset:1024 sc1\n" \
  "s_add_u32 s52, s52, 2048\n" \
  "s_add_u32 m0, %[ldsw], 16384\n" \
  "s_bfe_u32 s58, %[w2t], 131084\n" "s_lshl_b32 s58, s58, 17\n" "s_add_u32 s56, s58, 1048576\n" \
  "v_mfma_f32_16x16x32_bf16 v[144:147], a[100:103], a[92:95], v[144:147]\n" \
  "buffer_load_dwordx4 %[vow0], s[44:47], s56 offen lds\n" \
  "s_waitcnt lgkmcnt(4)\n" \
  "v_mfma_f32_16x16x32_bf16 v[148:151], a[104:107], a[88:91], v[148:151]\n" \
  "ds_read_b64_tr_b16 a[120:121], v31 offset:0\n" \
  "v_mfma_f32_16x16x32_bf16 v[148:151], a[108:111], a[92:95], v[148:151]\n" \
  "ds_read_b64_tr_b16 a[122:123], v31 offset:1024\n" \
  "v_mfma_f32_16x16x32_bf16 v[116:119], a[108:111], a[84:87], v[116:119]\n" \
  "ds_read_b64_tr_b16 a[124:125], v31 offset:8192\n" \
  "v_mfma_f32_16x16x32_bf16 v[116:119], a[104:107], a[80:83], v[116:119]\n" \
  "ds_read_b64_tr_b16 a[126:127], v31 offset:9216\n" \
  "s_add_u32 m0, %[ldsw], 17408\n" \
  "v_mfma_f32_16x16x32_bf16 v[84:87], a[104:107], a[72:75], v[84:87]\n" \
  "s_add_u32 s57, s56, 8192\n" \
  "v_mfma_f32_16x16x32_bf16 v[84:87], a[108:111], a[76:79], v[84:87]\n" \
  "buffer_load_dwordx4 %[vow0], s[44:47], s57 offen lds\n" \
  "s_waitcnt vmcnt(18) lgkmcnt(4)\n" \
  "s_barrier\n" \
  "v_mfma_f32_16x16x32_bf16 v[88:91], a[112:115], a[72:75], v[88:91]\n" \
  "ds_read_b64_tr_b16 a[96:97], v24 offset:16384\n" \
  "v_mfma_f32_16x16x32_bf16 v[88:91], a[116:119], a[76:79], v[88:91]\n" \
  "ds_read_b64_tr_b16 a[98:99], v24 offset:17408\n" \
  "v_mfma_f32_16x16x32_bf16 v[120:123], a[116:119], a[84:87], v[120:123]\n" \
  "ds_read_b64_tr_b16 a[100:101], v24 offset:24576\n" \
  "s_add_u32 m0, %[ldsw], 18432\n" \
  "v_mfma_f32_16x16x32_bf16 v[120:123], a[112:115], a[80:83], v[120:123]\n" \
  "ds_read_b64_tr_b16 a[102:103], v24 offset:25600\n" \
  "s_bfe_u32 s58, %[w2t], 131086\n" "s_lshl_b32 s58, s58, 17\n" "s_add_u32 s56, s58, 1064960\n" \
  "v_mfma_f32_16x16x32_bf16 v[152:155], a[112:115], a[88:91], v[152:155]\n" \
  "buffer_load_dwordx4 %[vow1], s[44:47], s56 offen lds\n" \
  "v_mfma_f32_16x16x32_bf16 v[152:155], a[116:119], a[92:95], v[152:155]\n" \
  "s_waitcnt lgkmcnt(4)\n" \
  "v_mfma_f32_16x16x32_bf16 v[156:159], a[120:123], a[88:91], v[156:159]\n" \
  "ds_read_b64_tr_b16 a[104:105], v25 offset:16384\n" \
  "v_mfma_f32_16x16x32_bf16 v[156:159], a[124:127], a[92:95], v[156:159]\n" \
  "ds_read_b64_tr_b16 a[106:107], v25 offset:17408\n" \
  "v_mfma_f32_16x16x32_bf16 v[124:127], a[124:127], a[84:87], v[124:127]\n" \
  "ds_read_b64_tr_b16 a[108:109], v25 offset:24576\n" \
  "s_add_u32 m0, %[ldsw], 19456\n" \
  "v_mfma_f32_16x16x32_bf16 v[124:127], a[120:123], a[80:83], v[124:127]\n" \
  "ds_read_b64_tr_b16 a[110:111], v25 offset:25600\n" \
  "s_add_u32 s57, s56, 8192\n" \
  "v_mfma_f32_16x16x32_bf16 v[92:95], a[120:123], a[72:75], v[92:95]\n" \
  "buffer_load_dwordx4 %[vow1], s[44:47], s57 offen lds\n" \
  "v_mfma_f32_16x16x32_bf16 v[92:95], a[124:127], a[76:79], v[92:95]\n" \
  "s_waitcnt vmcnt(14) lgkmcnt(4)\n" \
  "v_mfma_f32_16x16x32_bf16 v[64:67], a[96:99], a[8:11], v[64:67]\n" \
  "ds_read_b64_tr_b16 a[112:113], v26 offset:16384\n" \
  "v_mov_b32 v24, %[vl0]\n" \
  "v_mfma_f32_16x16x32_bf16 v[64:67], a[100:103], a[12:15], v[64:67]\n" \
  "ds_read_b64_tr_b16 a[114:115], v26 offset:17408\n" \
  "v_mov_b32 v25, %[vl1]\n" \
  "v_mfma_f32_16x16x32_bf16 v[96:99], a[100:103], a[20:23], v[96:99]\n" \
  "ds_read_b64_tr_b16 a[116:117], v26 offset:24576\n" \
  "v_mfma_f32_16x16x32_bf16 v[96:99], a[96:99], a[16:19], v[96:99]\n" \
  "ds_read_b64_tr_b16 a[118:119], v26 offset:25600\n" \
  "v_mfma_f32_16x16x32_bf16 v[128:131], a[96:99], a[24:27], v[128:131]\n" \
  "buffer_load_dwordx4 a[72:75], %[voa1], s[40:43], s52 offen offset:0 sc1\n" \
  "v_mov_b32 v26, %[vl2]\n" \
  "v_mfma_f32_16x16x32_bf16 v[128:131], a[100:103], a[28:31], v[128:131]\n" \
  "buffer_load_dwordx4 a[76:79], %[voa1], s[40:43], s52 offen offset:1024 sc1\n" \
  "s_waitcnt lgkmcnt(4)\n" \
  "v_mfma_f32_16x16x32_bf16 v[132:135], a[104:107], a[24:27], v[132:135]\n" \
  "ds_read_b64_tr_b16 a[120:121], v27 offset:16384\n" \
  "v_mfma_f32_16x16x32_bf16 v[132:135], a[108:111], a[28:31], v[132:135]\n" \
  "ds_read_b64_tr_b16 a[122:123], v27 offset:17408\n" \
  "v_mfma_f32_16x16x32_bf16 v[100:103], a[108:111], a[20:23], v[100:103]\n" \
  "ds_read_b64_tr_b16 a[124:125], v27 offset:24576\n" \
  "v_mfma_f32_16x16x32_bf16 v[100:103], a[104:107], a[16:19], v[100:103]\n" \
  "ds_read_b64_tr_b16 a[126:127], v27 offset:25600\n" \
  "v_mfma_f32_16x16x32_bf16 v[68:71], a[104:107], a[8:11], v[68:71]\n" \
  "buffer_load_dwordx4 a[80:83], %[voa2], s[40:43], s52 offen offset:0 sc1\n" \
  "v_mov_b32 v27, %[vl3]\n" \
  "v_mfma_f32_16x16x32_bf16 v[68:71], a[108:111], a[12:15], v[68:71]\n" \
  "s_waitcnt lgkmcnt(4)\n" \
  "v_mfma_f32_16x16x32_bf16 v[72:75], a[112:115], a[8:11], v[72:75]\n" \
  "ds_read_b64_tr_b16 a[96:97], v28 offset:16384\n" \
  "v_mfma_f32_16x16x32_bf16 v[72:75], a[116:119], a[12:15], v[72:75]\n" \
  "ds_read_b64_tr_b16 a[98:99], v28 offset:17408\n" \
  "v_mfma_f32_16x16x32_bf16 v[104:107], a[116:119], a[20:23], v[104:107]\n" \
  "ds_read_b64_tr_b16 a[100:101], v28 offset:24576\n" \
  "v_mfma_f32_16x16x32_bf16 v[104:107], a[112:115], a[16:19], v[104:107]\n" \
  "ds_read_b64_tr_b16 a[102:103], v28 offset:25600\n" \
  "v_mfma_f32_16x16x32_bf16 v[136:139], a[112:115], a[24:27], v[136:139]\n" \
  "buffer_load_dwordx4 a[84:87], %[voa2], s[40:43], s52 offen offset:1024 sc1\n" \
  "v_mov_b32 v28, %[vl4]\n" \
  "v_mfma_f32_16x16x32_bf16 v[136:139], a[116:119], a[28:31], v[136:139]\n" \
  "buffer_load_dwordx4 a[88:91], %[voa3], s[40:43], s52 offen offset:0 sc1\n" \
  "s_waitcnt lgkmcnt(4)\n" \
  "v_mfma_f32_16x16x32_bf16 v[140:143], a[120:123], a[24:27], v[140:143]\n" \
  "ds_read_b64_tr_b16 a[104:105], v29 offset:16384\n" \
  "v_mfma_f32_16x16x32_bf16 v[140:143], a[124:127], a[28:31], v[140:143]\n" \
  "ds_read_b64_tr_b16 a[106:107], v29 offset:17408\n" \
  "v_mfma_f32_16x16x32_bf16 v[108:111], a[124:127], a[20:23], v[108:111]\n" \
  "ds_read_b64_tr_b16 a[108:109], v29 offset:24576\n" \
  "v_mfma_f32_16x16x32_bf16 v[108:111], a[120:123], a[16:19], v[108:111]\n" \
  "ds_read_b64_tr_b16 a[110:111], v29 offset:25600\n" \
  "v_mfma_f32_16x16x32_bf16 v[76:79], a[120:123], a[8:11], v[76:79]\n" \
  "buffer_load_dwordx4 a[92:95], %[voa3], s[40:43], s52 offen offset:1024 sc1\n" \
  "s_add_u32 s52, s52, 2048\n" \
  "v_mfma_f32_16x16x32_bf16 v[76:79], a[124:127], a[12:15], v[76:79]\n" \
  "v_mov_b32 v29, %[vl5]\n" \
  "s_waitcnt lgkmcnt(4)\n" \
  "v_mfma_f32_16x16x32_bf16 v[80:83], a[96:99], a[8:11], v[80:83]\n" \
  "ds_read_b64_tr_b16 a[112:113], v30 offset:16384\n" \
  "v_mfma_f32_16x16x32_bf16 v[80:83], a[100:103], a[12:15], v[80:83]\n" \
  "ds_read_b64_tr_b16 a[114:115], v30 offset:17408\n" \
  "v_mfma_f32_16x16x32_bf16 v[112:115], a[100:103], a[20:23], v[112:115]\n" \
  "ds_read_b64_tr_b16 a[116:117], v30 offset:24576\n" \
  "s_add_u32 m0, %[ldsw], 32768\n" \
  "v_mfma_f32_16x16x32_bf16 v[112:115], a[96:99], a[16:19], v[112:115]\n" \
  "ds_read_b64_tr_b16 a[118:119], v30 offset:25600\n" \
  "s_bfe_u32 s58, %[w2t], 131088\n" "s_lshl_b32 s58, s58, 17\n" "s_add_u32 s56, s58, 1310720\n" \
  "v_mfma_f32_16x16x32_bf16 v[144:147], a[96:99], a[24:27], v[144:147]\n" \
  "buffer_load_dwordx4 %[vow0], s[44:47], s56 offen lds\n" \
  "s_add_u32 m0, %[ldsw], 33792\n" \
  "s_add_u32 s57, s56, 8192\n" \
  "v_mfma_f32_16x16x32_bf16 v[144:147], a[100:103], a[28:31], v[144:147]\n" \
  "buffer_load_dwordx4 %[vow0], s[44:47], s57 offen lds\n" \
  "s_waitcnt lgkmcnt(4)\n" \
  "v_mfma_f32_16x16x32_bf16 v[148:151], a[104:107], a[24:27], v[148:151]\n" \
  "ds_read_b64_tr_b16 a[120:121], v31 offset:16384\n" \
  "v_mov_b32 v30, %[vl6]\n" \
  "v_mfma_f32_16x16x32_bf16 v[148:151], a[108:111], a[28:31], v[148:151]\n" \
  "ds_read_b64_tr_b16 a[122:123], v31 offset:17408\n" \
  "v_mfma_f32_16x16x32_bf16 v[116:119], a[108:111], a[20:23], v[116:119]\n" \
  "ds_read_b64_tr_b16 a[124:125], v31 offset:24576\n" \
  "s_add_u32 m0, %[ldsw], 34816\n" \
  "v_mfma_f32_16x16x32_bf16 v[116:119], a[104:107], a[16:19], v[116:119]\n" \
  "ds_read_b64_tr_b16 a[126:127], v31 offset:25600\n" \
  "s_bfe_u32 s58, %[w2t], 131090\n" "s_lshl_b32 s58, s58, 17\n" "s_add_u32 s56, s58, 1327104\n" \
  "v_mfma_f32_16x16x32_bf16 v[84:87], a[104:107], a[8:11], v[84:87]\n" \
  "buffer_load_dwordx4 %[vow1], s[44:47], s56 offen lds\n" \
  "v_mov_b32 v31, %[vl7]\n" \
  "v_mfma_f32_16x16x32_bf16 v[84:87], a[108:111], a[12:15], v[84:87]\n" \
  "s_waitcnt vmcnt(19) lgkmcnt(4)\n" \
  "s_barrier\n" \
  "v_mfma_f32_16x16x32_bf16 v[88:91], a[112:115], a[8:11], v[88:91]\n" \
  "ds_read_b64_tr_b16 a[96:97], v24 offset:0\n" \
  "v_mfma_f32_16x16x32_bf16 v[88:91], a[116:119], a[12:15], v[88:91]\n" \
  "ds_read_b64_tr_b16 a[98:99], v24 offset:1024\n" \
  "v_mfma_f32_16x16x32_bf16 v[120:123], a[116:119], a[20:23], v[120:123]\n" \
  "ds_read_b64_tr_b16 a[100:101], v24 offset:8192\n" \
  "s_add_u32 m0, %[ldsw], 35840\n" \
  "v_mfma_f32_16x16x32_bf16 v[120:123], a[112:115], a[16:19], v[120:123]\n" \
  "ds_read_b64_tr_b16 a[102:103], v24 offset:9216\n" \
  "s_add_u32 s57, s56, 8192\n" \
  "v_mfma_f32_16x16x32_bf16 v[152:155], a[112:115], a[24:27], v[152:155]\n" \
  "buffer_load_dwordx4 %[vow1], s[44:47], s57 offen lds\n" \
  "v_mfma_f32_16x16x32_bf16 v[152:155], a[116:119], a[28:31], v[152:155]\n" \
  "buffer_load_dwordx4 a[128:131], %[voa0], s[40:43], s53 offen offset:0 sc1\n" \
  "s_waitcnt lgkmcnt(4)\n" \
  "v_mfma_f32_16x16x32_bf16 v[156:159], a[120:123], a[24:27], v[156:159]\n" \
  "ds_read_b64_tr_b16 a[104:105], v25 offset:0\n" \
  "v_mfma_f32_16x16x32_bf16 v[156:159], a[124:127], a[28:31], v[156:159]\n" \
  "ds_read_b64_tr_b16 a[106:107], v25 offset:1024\n" \
  "v_mfma_f32_16x16x32_bf16 v[124:127], a[124:127], a[20:23], v[124:127]\n" \
  "ds_read_b64_tr_b16 a[108:109], v25 offset:8192\n" \
  "v_mfma_f32_16x16x32_bf16 v[124:127], a[120:123], a[16:19], v[124:127]\n" \
  "ds_read_b64_tr_b16 a[110:111], v25 offset:9216\n" \
  "v_mfma_f32_16x16x32_bf16 v[92:95], a[120:123], a[8:11], v[92:95]\n" \
  "buffer_load_dwordx4 a[132:135], %[voa0], s[40:43], s53 offen offset:1024 sc1\n" \
  "s_add_u32 s53, s53, 2048\n" \
  "v_mfma_f32_16x16x32_bf16 v[92:95], a[124:127], a[12:15], v[92:95]\n" \
  "s_waitcnt vmcnt(16) lgkmcnt(4)\n" \
  "v_mfma_f32_16x16x32_bf16 v[64:67], a[96:99], a[40:43], v[64:67]\n" \
  "ds_read_b64_tr_b16 a[112:113], v26 offset:0\n" \
  "v_mfma_f32_16x16x32_bf16 v[64:67], a[100:103], a[44:47], v[64:67]\n" \
  "ds_read_b64_tr_b16 a[114:115], v26 offset:1024\n" \
  "v_mfma_f32_16x16x32_bf16 v[96:99], a[100:103], a[52:55], v[96:99]\n" \
  "ds_read_b64_tr_b16 a[116:117], v26 offset:8192\n" \
  "v_mfma_f32_16x16x32_bf16 v[96:99], a[96:99], a[48:51], v[96:99]\n" \
  "ds_read_b64_tr_b16 a[118:119], v26 offset:9216\n" \
  "v_mfma_f32_16x16x32_bf16 v[128:131], a[96:99], a[56:59], v[128:131]\n" \
  "buffer_load_dwordx4 a[8:11], %[voa1], s[40:43], s52 offen offset:0 sc1\n" \
  "v_mfma_f32_16x16x32_bf16 v[128:131], a[100:103], a[60:63], v[128:131]\n" \
  "buffer_load_dwordx4 a[12:15], %[voa1], s[40:43], s52 offen offset:1024 sc1\n" \
  "s_waitcnt lgkmcnt(4)\n" \
  "v_mfma_f32_16x16x32_bf16 v[132:135], a[104:107], a[56:59], v[132:135]\n" \
  "ds_read_b64_tr_b16 a[120:121], v27 offset:0\n" \
  "v_mfma_f32_16x16x32_bf16 v[132:135], a[108:111], a[60:63], v[132:135]\n" \
  "ds_read_b64_tr_b16 a[122:123], v27 offset:1024\n" \
  "v_mfma_f32_16x16x32_bf16 v[100:103], a[108:111], a[52:55], v[100:103]\n" \
  "ds_read_b64_tr_b16 a[124:125], v27 offset:8192\n" \
  "v_mfma_f32_16x16x32_bf16 v[100:103], a[104:107], a[48:51], v[100:103]\n" \
  "ds_read_b64_tr_b16 a[126:127], v27 offset:9216\n" \
  "v_mfma_f32_16x16x32_bf16 v[68:71], a[104:107], a[40:43], v[68:71]\n" \
  "buffer_load_dwordx4 a[16:19], %[voa2], s[40:43], s52 offen offset:0 sc1\n" \
  "v_mfma_f32_16x16x32_bf16 v[68:71], a[108:111], a[44:47], v[68:71]\n" \
  "s_waitcnt lgkmcnt(4)\n" \
  "v_mfma_f32_16x16x32_bf16 v[72:75], a[112:115], a[40:43], v[72:75]\n" \
  "ds_read_b64_tr_b16 a[96:97], v28 offset:0\n" \
  "v_mfma_f32_16x16x32_bf16 v[72:75], a[116:119], a[44:47], v[72:75]\n" \
  "ds_read_b64_tr_b16 a[98:99], v28 offset:1024\n" \
  "v_mfma_f32_16x16x32_bf16 v[104:107], a[116:119], a[52:55], v[104:107]\n" \
  "ds_read_b64_tr_b16 a[100:101], v28 offset:8192\n" \
  "v_mfma_f32_16x16x32_bf16 v[104:107], a[112:115], a[48:51], v[104:107]\n" \
  "ds_read_b64_tr_b16 a[102:103], v28 offset:9216\n" \
  "v_mfma_f32_16x16x32_bf16 v[136:139], a[112:115], a[56:59], v[136:139]\n" \
  "buffer_load_dwordx4 a[20:23], %[voa2], s[40:43], s52 offen offset:1024 sc1\n" \
  "v_mfma_f32_16x16x32_bf16 v[136:139], a[116:119], a[60:63], v[136:139]\n" \
  "buffer_load_dwordx4 a[24:27], %[voa3], s[40:43], s52 offen offset:0 sc1\n" \
  "s_waitcnt lgkmcnt(4)\n" \
  "v_mfma_f32_16x16x32_bf16 v[140:143], a[120:123], a[56:59], v[140:143]\n" \
  "ds_read_b64_tr_b16 a[104:105], v29 offset:0\n" \
  "v_mfma_f32_16x16x32_bf16 v[140:143], a[124:127], a[60:63], v[140:143]\n" \
  "ds_read_b64_tr_b16 a[106:107], v29 offset:1024\n" \
  "v_mfma_f32_16x16x32_bf16 v[108:111], a[124:127], a[52:55], v[108:111]\n" \
  "ds_read_b64_tr_b16 a[108:109], v29 offset:8192\n" \
  "v_mfma_f32_16x16x32_bf16 v[108:111], a[120:123], a[48:51], v[108:111]\n" \
  "ds_read_b64_tr_b16 a[110:111], v29 offset:9216\n" \
  "v_mfma_f32_16x16x32_bf16 v[76:79], a[120:123], a[40:43], v[76:79]\n" \
  "buffer_load_dwordx4 a[28:31], %[voa3], s[40:43], s52 offen offset:1024 sc1\n" \
  "s_add_u32 s52, s52, 2048\n" \
  "v_mfma_f32_16x16x32_bf16 v[76:79], a[124:127], a[44:47], v[76:79]\n" \
  "s_waitcnt lgkmcnt(4)\n" \
  "v_mfma_f32_16x16x32_bf16 v[80:83], a[96:99], a[40:43], v[80:83]\n" \
  "ds_read_b64_tr_b16 a[112:113], v30 offset:0\n" \
  "v_mfma_f32_16x16x32_bf16 v[80:83], a[100:103], a[44:47], v[80:83]\n" \
  "ds_read_b64_tr_b16 a[114:115], v30 offset:1024\n" \
  "v_mfma_f32_16x16x32_bf16 v[112:115], a[100:103], a[52:55], v[112:115]\n" \
  "ds_read_b64_tr_b16 a[116:117], v30 offset:8192\n" \
  "s_add_u32 m0, %[ldsw], 49152\n" \
  "v_mfma_f32_16x16x32_bf16 v[112:115], a[96:99], a[48:51], v[112:115]\n" \
  "ds_read_b64_tr_b16 a[118:119], v30 offset:9216\n" \
  "s_bfe_u32 s58, %[w2t], 131092\n" "s_lshl_b32 s58, s58, 17\n" "s_add_u32 s56, s58, 1572864\n" \
  "v_mfma_f32_16x16x32_bf16 v[144:147], a[96:99], a[56:59], v[144:147]\n" \
  "buffer_load_dwordx4 %[vow0], s[44:47], s56 offen lds\n" \
  "s_add_u32 m0, %[ldsw], 50176\n" \
  "s_add_u32 s57, s56, 8192\n" \
  "v_mfma_f32_16x16x32_bf16 v[144:147], a[100:103], a[60:63], v[144:147]\n" \
  "buffer_load_dwordx4 %[vow0], s[44:47], s57 offen lds\n" \
  "s_waitcnt lgkmcnt(4)\n" \
  "v_mfma_f32_16x16x32_bf16 v[148:151], a[104:107], a[56:59], v[148:151]\n" \
  "ds_read_b64_tr_b16 a[120:121], v31 offset:0\n" \
  "v_mfma_f32_16x16x32_bf16 v[148:151], a[108:111], a[60:63], v[148:151]\n" \
  "ds_read_b64_tr_b16 a[122:123], v31 offset:1024\n" \
  "v_mfma_f32_16x16x32_bf16 v[116:119], a[108:111], a[52:55], v[116:119]\n" \
  "ds_read_b64_tr_b16 a[124:125], v31 offset:8192\n" \
  "s_add_u32 m0, %[ldsw], 51200\n" \
  "v_mfma_f32_16x16x32_bf16 v[116:119], a[104:107], a[48:51], v[116:119]\n" \
  "ds_read_b64_tr_b16 a[126:127], v31 offset:9216\n" \
  "s_bfe_u32 s58, %[w2t], 131094\n" "s_lshl_b32 s58, s58, 17\n" "s_add_u32 s56, s58, 1589248\n" \
  "v_mfma_f32_16x16x32_bf16 v[84:87], a[104:107], a[40:43], v[84:87]\n" \
  "buffer_load_dwordx4 %[vow1], s[44:47], s56 offen lds\n" \
  "v_mfma_f32_16x16x32_bf16 v[84:87], a[108:111], a[44:47], v[84:87]\n" \
  "s_waitcnt vmcnt(21) lgkmcnt(4)\n" \
  "s_barrier\n" \
  "v_mfma_f32_16x16x32_bf16 v[88:91], a[112:115], a[40:43], v[88:91]\n" \
  "ds_read_b64_tr_b16 a[96:97], v24 offset:16384\n" \
  "v_mfma_f32_16x16x32_bf16 v[88:91], a[116:119], a[44:47], v[88:91]\n" \
  "ds_read_b64_tr_b16 a[98:99], v24 offset:17408\n" \
  "v_mfma_f32_16x16x32_bf16 v[120:123], a[116:119], a[52:55], v[120:123]\n" \
  "ds_read_b64_tr_b16 a[100:101], v24 offset:24576\n" \
  "s_add_u32 m0, %[ldsw], 52224\n" \
  "v_mfma_f32_16x16x32_bf16 v[120:123], a[112:115], a[48:51], v[120:123]\n" \
  "ds_read_b64_tr_b16 a[102:103], v24 offset:25600\n" \
  "s_add_u32 s57, s56, 8192\n" \
  "v_mfma_f32_16x16x32_bf16 v[152:155], a[112:115], a[56:59], v[152:155]\n" \
  "buffer_load_dwordx4 %[vow1], s[44:47], s57 offen lds\n" \
  "v_mfma_f32_16x16x32_bf16 v[152:155], a[116:119], a[60:63], v[152:155]\n" \
  "buffer_load_dwordx4 a[136:139], %[voa0], s[40:43], s53 offen offset:0 sc1\n" \
  "s_waitcnt lgkmcnt(4)\n" \
  "v_mfma_f32_16x16x32_bf16 v[156:159], a[120:123], a[56:59], v[156:159]\n" \
  "ds_read_b64_tr_b16 a[104:105], v25 offset:16384\n" \
  "v_mfma_f32_16x16x32_bf16 v[156:159], a[124:127], a[60:63], v[156:159]\n" \
  "ds_read_b64_tr_b16 a[106:107], v25 offset:17408\n" \
  "v_mfma_f32_16x16x32_bf16 v[124:127], a[124:127], a[52:55], v[124:127]\n" \
  "ds_read_b64_tr_b16 a[108:109], v25 offset:24576\n" \
  "v_mfma_f32_16x16x32_bf16 v[124:127], a[120:123], a[48:51], v[124:127]\n" \
  "ds_read_b64_tr_b16 a[110:111], v25 offset:25600\n" \
  "v_mfma_f32_16x16x32_bf16 v[92:95], a[120:123], a[40:43], v[92:95]\n" \
  "buffer_load_dwordx4 a[140:143], %[voa0], s[40:43], s53 offen offset:1024 sc1\n" \
  "s_add_u32 s53, s53, 2048\n" \
  "v_mfma_f32_16x16x32_bf16 v[92:95], a[124:127], a[44:47], v[92:95]\n" \
  "s_waitcnt vmcnt(18) lgkmcnt(4)\n" \
  "v_mfma_f32_16x16x32_bf16 v[64:67], a[96:99], a[72:75], v[64:67]\n" \
  "ds_read_b64_tr_b16 a[112:113], v26 offset:16384\n" \
  "v_mfma_f32_16x16x32_bf16 v[64:67], a[100:103], a[76:79], v[64:67]\n" \
  "ds_read_b64_tr_b16 a[114:115], v26 offset:17408\n" \
  "v_mfma_f32_16x16x32_bf16 v[96:99], a[100:103], a[84:87], v[96:99]\n" \
  "ds_read_b64_tr_b16 a[116:117], v26 offset:24576\n" \
  "v_mfma_f32_16x16x32_bf16 v[96:99], a[96:99], a[80:83], v[96:99]\n" \
  "ds_read_b64_tr_b16 a[118:119], v26 offset:25600\n" \
  "v_mfma_f32_16x16x32_bf16 v[128:131], a[96:99], a[88:91], v[128:131]\n" \
  "buffer_load_dwordx4 a[40:43], %[voa1], s[40:43], s52 offen offset:0 sc1\n" \
  "v_mfma_f32_16x16x32_bf16 v[128:131], a[100:103], a[92:95], v[128:131]\n" \
  "buffer_load_dwordx4 a[44:47], %[voa1], s[40:43], s52 offen offset:1024 sc1\n" \
  "s_waitcnt lgkmcnt(4)\n" \
  "v_mfma_f32_16x16x32_bf16 v[132:135], a[104:107], a[88:91], v[132:135]\n" \
  "ds_read_b64_tr_b16 a[120:121], v27 offset:16384\n" \
  "v_mfma_f32_16x16x32_bf16 v[132:135], a[108:111], a[92:95], v[132:135]\n" \
  "ds_read_b64_tr_b16 a[122:123], v27 offset:17408\n" \
  "v_mfma_f32_16x16x32_bf16 v[100:103], a[108:111], a[84:87], v[100:103]\n" \
  "ds_read_b64_tr_b16 a[124:125], v27 offset:24576\n" \
  "v_mfma_f32_16x16x32_bf16 v[100:103], a[104:107], a[80:83], v[100:103]\n" \
  "ds_read_b64_tr_b16 a[126:127], v27 offset:25600\n" \
  "v_mfma_f32_16x16x32_bf16 v[68:71], a[104:107], a[72:75], v[68:71]\n" \
  "buffer_load_dwordx4 a[48:51], %[voa2], s[40:43], s52 offen offset:0 sc1\n" \
  "v_mfma_f32_16x16x32_bf16 v[68:71], a[108:111], a[76:79], v[68:71]\n" \
  "s_waitcnt lgkmcnt(4)\n" \
  "v_mfma_f32_16x16x32_bf16 v[72:75], a[112:115], a[72:75], v[72:75]\n" \
  "ds_read_b64_tr_b16 a[96:97], v28 offset:16384\n" \
  "v_mfma_f32_16x16x32_bf16 v[72:75], a[116:119], a[76:79], v[72:75]\n" \
  "ds_read_b64_tr_b16 a[98:99], v28 offset:17408\n" \
  "v_mfma_f32_16x16x32_bf16 v[104:107], a[116:119], a[84:87], v[104:107]\n" \
  "ds_read_b64_tr_b16 a[100:101], v28 offset:24576\n" \
  "v_mfma_f32_16x16x32_bf16 v[104:107], a[112:115], a[80:83], v[104:107]\n" \
  "ds_read_b64_tr_b16 a[102:103], v28 offset:25600\n" \
  "v_mfma_f32_16x16x32_bf16 v[136:139], a[112:115], a[88:91], v[136:139]\n" \
  "buffer_load_dwordx4 a[52:55], %[voa2], s[40:43], s52 offen offset:1024 sc1\n" \
  "v_mfma_f32_16x16x32_bf16 v[136:139], a[116:119], a[92:95], v[136:139]\n" \
  "buffer_load_dwordx4 a[56:59], %[voa3], s[40:43], s52 offen offset:0 sc1\n" \
  "s_waitcnt lgkmcnt(4)\n" \
  "v_mfma_f32_16x16x32_bf16 v[140:143], a[120:123], a[88:91], v[140:143]\n" \
  "ds_read_b64_tr_b16 a[104:105], v29 offset:16384\n" \
  "v_mfma_f32_16x16x32_bf16 v[140:143], a[124:127], a[92:95], v[140:143]\n" \
  "ds_read_b64_tr_b16 a[106:107], v29 offset:17408\n" \
  "v_mfma_f32_16x16x32_bf16 v[108:111], a[124:127], a[84:87], v[108:111]\n" \
  "ds_read_b64_tr_b16 a[108:109], v29 offset:24576\n" \
  "v_mfma_f32_16x16x32_bf16 v[108:111], a[120:123], a[80:83], v[108:111]\n" \
  "ds_read_b64_tr_b16 a[110:111], v29 offset:25600\n" \
  "v_mfma_f32_16x16x32_bf16 v[76:79], a[120:123], a[72:75], v[76:79]\n" \
  "buffer_load_dwordx4 a[60:63], %[voa3], s[40:43], s52 offen offset:1024 sc1\n" \
  "v_mfma_f32_16x16x32_bf16 v[76:79], a[124:127], a[76:79], v[76:79]\n" \
  "s_waitcnt lgkmcnt(4)\n" \
  "v_mfma_f32_16x16x32_bf16 v[80:83], a[96:99], a[72:75], v[80:83]\n" \
  "ds_read_b64_tr_b16 a[112:113], v30 offset:16384\n" \
  "v_mfma_f32_16x16x32_bf16 v[80:83], a[100:103], a[76:79], v[80:83]\n" \
  "ds_read_b64_tr_b16 a[114:115], v30 offset:17408\n" \
  "v_mfma_f32_16x16x32_bf16 v[112:115], a[100:103], a[84:87], v[112:115]\n" \
  "ds_read_b64_tr_b16 a[116:117], v30 offset:24576\n" \
  "v_mfma_f32_16x16x32_bf16 v[112:115], a[96:99], a[80:83], v[112:115]\n" \
  "ds_read_b64_tr_b16 a[118:119], v30 offset:25600\n" \
  "v_mfma_f32_16x16x32_bf16 v[144:147], a[96:99], a[88:91], v[144:147]\n" \
  "buffer_load_dwordx4 a[144:147], %[voa0], s[40:43], s53 offen offset:0 sc1\n" \
  "v_mfma_f32_16x16x32_bf16 v[144:147], a[100:103], a[92:95], v[144:147]\n" \
  "buffer_load_dwordx4 a[148:151], %[voa0], s[40:43], s53 offen offset:1024 sc1\n" \
  "s_add_u32 s53, s53, 2048\n" \
  "s_waitcnt lgkmcnt(4)\n" \
  "v_mfma_f32_16x16x32_bf16 v[148:151], a[104:107], a[88:91], v[148:151]\n" \
  "ds_read_b64_tr_b16 a[120:121], v31 offset:16384\n" \
  "v_mfma_f32_16x16x32_bf16 v[148:151], a[108:111], a[92:95], v[148:151]\n" \
  "ds_read_b64_tr_b16 a[122:123], v31 offset:17408\n" \
  "v_mfma_f32_16x16x32_bf16 v[116:119], a[108:111], a[84:87], v[116:119]\n" \
  "ds_read_b64_tr_b16 a[124:125], v31 offset:24576\n" \
  "v_mfma_f32_16x16x32_bf16 v[116:119], a[104:107], a[80:83], v[116:119]\n" \
  "ds_read_b64_tr_b16 a[126:127], v31 offset:25600\n" \
  "v_mfma_f32_16x16x32_bf16 v[84:87], a[104:107], a[72:75], v[84:87]\n" \
  "buffer_load_dwordx4 a[152:155], %[voa0], s[40:43], s53 offen offset:0 sc1\n" \
  "v_mfma_f32_16x16x32_bf16 v[84:87], a[108:111], a[76:79], v[84:87]\n" \
  "s_waitcnt vmcnt(23) lgkmcnt(4)\n" \
  "s_barrier\n" \
  "v_mfma_f32_16x16x32_bf16 v[88:91], a[112:115], a[72:75], v[88:91]\n" \
  "ds_read_b64_tr_b16 a[96:97], v24 offset:32768\n" \
  "v_mfma_f32_16x16x32_bf16 v[88:91], a[116:119], a[76:79], v[88:91]\n" \
  "ds_read_b64_tr_b16 a[98:99], v24 offset:33792\n" \
  "v_mfma_f32_16x16x32_bf16 v[120:123], a[116:119], a[84:87], v[120:123]\n" \
  "ds_read_b64_tr_b16 a[100:101], v24 offset:40960\n" \
  "v_mfma_f32_16x16x32_bf16 v[120:123], a[112:115], a[80:83], v[120:123]\n" \
  "ds_read_b64_tr_b16 a[102:103], v24 offset:41984\n" \
  "v_mfma_f32_16x16x32_bf16 v[152:155], a[112:115], a[88:91], v[152:155]\n" \
  "buffer_load_dwordx4 a[156:159], %[voa0], s[40:43], s53 offen offset:1024 sc1\n" \
  "s_add_u32 s53, s53, 2048\n" \
  "v_mfma_f32_16x16x32_bf16 v[152:155], a[116:119], a[92:95], v[152:155]\n" \
  "buffer_load_dwordx4 a[160:163], %[voa0], s[40:43], s53 offen offset:0 sc1\n" \
  "s_waitcnt lgkmcnt(4)\n" \
  "v_mfma_f32_16x16x32_bf16 v[156:159], a[120:123], a[88:91], v[156:159]\n" \
  "ds_read_b64_tr_b16 a[104:105], v25 offset:32768\n" \
  "v_mfma_f32_16x16x32_bf16 v[156:159], a[124:127], a[92:95], v[156:159]\n" \
  "ds_read_b64_tr_b16 a[106:107], v25 offset:33792\n" \
  "v_mfma_f32_16x16x32_bf16 v[124:127], a[124:127], a[84:87], v[124:127]\n" \
  "ds_read_b64_tr_b16 a[108:109], v25 offset:40960\n" \
  "v_mfma_f32_16x16x32_bf16 v[124:127], a[120:123], a[80:83], v[124:127]\n" \
  "ds_read_b64_tr_b16 a[110:111], v25 offset:41984\n" \
  "v_mfma_f32_16x16x32_bf16 v[92:95], a[120:123], a[72:75], v[92:95]\n" \
  "buffer_load_dwordx4 a[164:167], %[voa0], s[40:43], s53 offen offset:1024 sc1\n" \
  "s_add_u32 s53, s53, 2048\n" \
  "v_mfma_f32_16x16x32_bf16 v[92:95], a[124:127], a[76:79], v[92:95]\n" \
  "s_waitcnt vmcnt(18) lgkmcnt(4)\n" \
  "v_mfma_f32_16x16x32_bf16 v[64:67], a[96:99], a[8:11], v[64:67]\n" \
  "ds_read_b64_tr_b16 a[112:113], v26 offset:32768\n" \
  "v_mfma_f32_16x16x32_bf16 v[64:67], a[100:103], a[12:15], v[64:67]\n" \
  "ds_read_b64_tr_b16 a[114:115], v26 offset:33792\n" \
  "v_mfma_f32_16x16x32_bf16 v[96:99], a[100:103], a[20:23], v[96:99]\n" \
  "ds_read_b64_tr_b16 a[116:117], v26 offset:40960\n" \
  "v_mfma_f32_16x16x32_bf16 v[96:99], a[96:99], a[16:19], v[96:99]\n" \
  "ds_read_b64_tr_b16 a[118:119], v26 offset:41984\n" \
  "v_mfma_f32_16x16x32_bf16 v[128:131], a[96:99], a[24:27], v[128:131]\n" \
  "buffer_load_dwordx4 a[168:171], %[voa0], s[40:43], s53 offen offset:0 sc1\n" \
  "v_mfma_f32_16x16x32_bf16 v[128:131], a[100:103], a[28:31], v[128:131]\n" \
  "buffer_load_dwordx4 a[172:175], %[voa0], s[40:43], s53 offen offset:1024 sc1\n" \
  "s_add_u32 s53, s53, 2048\n" \
  "s_waitcnt lgkmcnt(4)\n" \
  "v_mfma_f32_16x16x32_bf16 v[132:135], a[104:107], a[24:27], v[132:135]\n" \
  "ds_read_b64_tr_b16 a[120:121], v27 offset:32768\n" \
  "v_mfma_f32_16x16x32_bf16 v[132:135], a[108:111], a[28:31], v[132:135]\n" \
  "ds_read_b64_tr_b16 a[122:123], v27 offset:33792\n" \
  "v_mfma_f32_16x16x32_bf16 v[100:103], a[108:111], a[20:23], v[100:103]\n" \
  "ds_read_b64_tr_b16 a[124:125], v27 offset:40960\n" \
  "v_mfma_f32_16x16x32_bf16 v[100:103], a[104:107], a[16:19], v[100:103]\n" \
  "ds_read_b64_tr_b16 a[126:127], v27 offset:41984\n" \
  "v_mfma_f32_16x16x32_bf16 v[68:71], a[104:107], a[8:11], v[68:71]\n" \
  "v_mfma_f32_16x16x32_bf16 v[68:71], a[108:111], a[12:15], v[68:71]\n" \
  "buffer_load_dwordx4 a[176:179], %[voa0], s[40:43], s53 offen offset:0 sc1\n" \
  "s_waitcnt lgkmcnt(4)\n" \
  "v_mfma_f32_16x16x32_bf16 v[72:75], a[112:115], a[8:11], v[72:75]\n" \
  "ds_read_b64_tr_b16 a[96:97], v28 offset:32768\n" \
  "v_mfma_f32_16x16x32_bf16 v[72:75], a[116:119], a[12:15], v[72:75]\n" \
  "ds_read_b64_tr_b16 a[98:99], v28 offset:33792\n" \
  "v_mfma_f32_16x16x32_bf16 v[104:107], a[116:119], a[20:23], v[104:107]\n" \
  "ds_read_b64_tr_b16 a[100:101], v28 offset:40960\n" \
  "v_mfma_f32_16x16x32_bf16 v[104:107], a[112:115], a[16:19], v[104:107]\n" \
  "ds_read_b64_tr_b16 a[102:103], v28 offset:41984\n" \
  "v_mfma_f32_16x16x32_bf16 v[136:139], a[112:115], a[24:27], v[136:139]\n" \
  "buffer_load_dwordx4 a[180:183], %[voa0], s[40:43], s53 offen offset:1024 sc1\n" \
  "s_add_u32 s53, s53, 2048\n" \
  "v_mfma_f32_16x16x32_bf16 v[136:139], a[116:119], a[28:31], v[136:139]\n" \
  "s_waitcnt lgkmcnt(4)\n" \
  "v_mfma_f32_16x16x32_bf16 v[140:143], a[120:123], a[24:27], v[140:143]\n" \
  "ds_read_b64_tr_b16 a[104:105], v29 offset:32768\n" \
  "v_mfma_f32_16x16x32_bf16 v[140:143], a[124:127], a[28:31], v[140:143]\n" \
  "ds_read_b64_tr_b16 a[106:107], v29 offset:33792\n" \
  "v_mfma_f32_16x16x32_bf16 v[108:111], a[124:127], a[20:23], v[108:111]\n" \
  "ds_read_b64_tr_b16 a[108:109], v29 offset:40960\n" \
  "v_mfma_f32_16x16x32_bf16 v[108:111], a[120:123], a[16:19], v[108:111]\n" \
  "ds_read_b64_tr_b16 a[110:111], v29 offset:41984\n" \
  "v_mfma_f32_16x16x32_bf16 v[76:79], a[120:123], a[8:11], v[76:79]\n" \
  "buffer_load_dwordx4 a[184:187], %[voa0], s[40:43], s53 offen offset:0 sc1\n" \
  "v_mfma_f32_16x16x32_bf16 v[76:79], a[124:127], a[12:15], v[76:79]\n" \
  "s_waitcnt lgkmcnt(4)\n" \
  "v_mfma_f32_16x16x32_bf16 v[80:83], a[96:99], a[8:11], v[80:83]\n" \
  "ds_read_b64_tr_b16 a[112:113], v30 offset:32768\n" \
  "v_mfma_f32_16x16x32_bf16 v[80:83], a[100:103], a[12:15], v[80:83]\n" \
  "ds_read_b64_tr_b16 a[114:115], v30 offset:33792\n" \
  "v_mfma_f32_16x16x32_bf16 v[112:115], a[100:103], a[20:23], v[112:115]\n" \
  "ds_read_b64_tr_b16 a[116:117], v30 offset:40960\n" \
  "v_mfma_f32_16x16x32_bf16 v[112:115], a[96:99], a[16:19], v[112:115]\n" \
  "ds_read_b64_tr_b16 a[118:119], v30 offset:41984\n" \
  "v_mfma_f32_16x16x32_bf16 v[144:147], a[96:99], a[24:27], v[144:147]\n" \
  "buffer_load_dwordx4 a[188:191], %[voa0], s[40:43], s53 offen offset:1024 sc1\n" \
  "s_add_u32 s53, s53, 2048\n" \
  "v_mfma_f32_16x16x32_bf16 v[144:147], a[100:103], a[28:31], v[144:147]\n" \
  "buffer_load_dwordx4 a[192:195], %[voa0], s[40:43], s53 offen offset:0 sc1\n" \
  "s_waitcnt lgkmcnt(4)\n" \
  "v_mfma_f32_16x16x32_bf16 v[148:151], a[104:107], a[24:27], v[148:151]\n" \
  "ds_read_b64_tr_b16 a[120:121], v31 offset:32768\n" \
  "v_mfma_f32_16x16x32_bf16 v[148:151], a[108:111], a[28:31], v[148:151]\n" \
  "ds_read_b64_tr_b16 a[122:123], v31 offset:33792\n" \
  "v_mfma_f32_16x16x32_bf16 v[116:119], a[108:111], a[20:23], v[116:119]\n" \
  "ds_read_b64_tr_b16 a[124:125], v31 offset:40960\n" \
  "v_mfma_f32_16x16x32_bf16 v[116:119], a[104:107], a[16:19], v[116:119]\n" \
  "ds_read_b64_tr_b16 a[126:127], v31 offset:41984\n" \
  "v_mfma_f32_16x16x32_bf16 v[84:87], a[104:107], a[8:11], v[84:87]\n" \
  "v_mfma_f32_16x16x32_bf16 v[84:87], a[108:111], a[12:15], v[84:87]\n" \
  "buffer_load_dwordx4 a[196:199], %[voa0], s[40:43], s53 offen offset:1024 sc1\n" \
  "s_add_u32 s53, s53, 2048\n" \
  "s_waitcnt vmcnt(22) lgkmcnt(4)\n" \
  "s_barrier\n" \
  "v_mfma_f32_16x16x32_bf16 v[88:91], a[112:115], a[8:11], v[88:91]\n" \
  "ds_read_b64_tr_b16 a[96:97], v24 offset:49152\n" \
  "v_mfma_f32_16x16x32_bf16 v[88:91], a[116:119], a[12:15], v[88:91]\n" \
  "ds_read_b64_tr_b16 a[98:99], v24 offset:50176\n" \
  "v_mfma_f32_16x16x32_bf16 v[120:123], a[116:119], a[20:23], v[120:123]\n" \
  "ds_read_b64_tr_b16 a[100:101], v24 offset:57344\n" \
  "v_mfma_f32_16x16x32_bf16 v[120:123], a[112:115], a[16:19], v[120:123]\n" \
  "ds_read_b64_tr_b16 a[102:103], v24 offset:58368\n" \
  "v_mfma_f32_16x16x32_bf16 v[152:155], a[112:115], a[24:27], v[152:155]\n" \
  "buffer_load_dwordx4 a[200:203], %[voa0], s[40:43], s53 offen offset:0 sc1\n" \
  "v_mfma_f32_16x16x32_bf16 v[152:155], a[116:119], a[28:31], v[152:155]\n" \
  "s_waitcnt lgkmcnt(4)\n" \
  "v_mfma_f32_16x16x32_bf16 v[156:159], a[120:123], a[24:27], v[156:159]\n" \
  "ds_read_b64_tr_b16 a[104:105], v25 offset:49152\n" \
  "v_mfma_f32_16x16x32_bf16 v[156:159], a[124:127], a[28:31], v[156:159]\n" \
  "ds_read_b64_tr_b16 a[106:107], v25 offset:50176\n" \
  "v_mfma_f32_16x16x32_bf16 v[124:127], a[124:127], a[20:23], v[124:127]\n" \
  "ds_read_b64_tr_b16 a[108:109], v25 offset:57344\n" \
  "v_mfma_f32_16x16x32_bf16 v[124:127], a[120:123], a[16:19], v[124:127]\n" \
  "ds_read_b64_tr_b16 a[110:111], v25 offset:58368\n" \
  "v_mfma_f32_16x16x32_bf16 v[92:95], a[120:123], a[8:11], v[92:95]\n" \
  "buffer_load_dwordx4 a[204:207], %[voa0], s[40:43], s53 offen offset:1024 sc1\n" \
  "v_mfma_f32_16x16x32_bf16 v[92:95], a[124:127], a[12:15], v[92:95]\n" \
  "s_waitcnt vmcnt(16) lgkmcnt(4)\n" \
  "v_mfma_f32_16x16x32_bf16 v[64:67], a[96:99], a[40:43], v[64:67]\n" \
  "ds_read_b64_tr_b16 a[112:113], v26 offset:49152\n" \
  "v_add_u32 v24, 65536, %[vl0]\n" \
  "v_mfma_f32_16x16x32_bf16 v[64:67], a[100:103], a[44:47], v[64:67]\n" \
  "ds_read_b64_tr_b16 a[114:115], v26 offset:50176\n" \
  "v_add_u32 v25, 65536, %[vl1]\n" \
  "v_mfma_f32_16x16x32_bf16 v[96:99], a[100:103], a[52:55], v[96:99]\n" \
  "ds_read_b64_tr_b16 a[116:117], v26 offset:57344\n" \
  "v_mfma_f32_16x16x32_bf16 v[96:99], a[96:99], a[48:51], v[96:99]\n" \
  "ds_read_b64_tr_b16 a[118:119], v26 offset:58368\n" \
  "v_mfma_f32_16x16x32_bf16 v[128:131], a[96:99], a[56:59], v[128:131]\n" \
  "v_add_u32 v26, 65536, %[vl2]\n" \
  "v_mfma_f32_16x16x32_bf16 v[128:131], a[100:103], a[60:63], v[128:131]\n" \
  "s_waitcnt lgkmcnt(4)\n" \
  "v_mfma_f32_16x16x32_bf16 v[132:135], a[104:107], a[56:59], v[132:135]\n" \
  "ds_read_b64_tr_b16 a[120:121], v27 offset:49152\n" \
  "v_mfma_f32_16x16x32_bf16 v[132:135], a[108:111], a[60:63], v[132:135]\n" \
  "ds_read_b64_tr_b16 a[122:123], v27 offset:50176\n" \
  "v_mfma_f32_16x16x32_bf16 v[100:103], a[108:111], a[52:55], v[100:103]\n" \
  "ds_read_b64_tr_b16 a[124:125], v27 offset:57344\n" \
  "v_mfma_f32_16x16x32_bf16 v[100:103], a[104:107], a[48:51], v[100:103]\n" \
  "ds_read_b64_tr_b16 a[126:127], v27 offset:58368\n" \
  "v_mfma_f32_16x16x32_bf16 v[68:71], a[104:107], a[40:43], v[68:71]\n" \
  "v_add_u32 v27, 65536, %[vl3]\n" \
  "v_mfma_f32_16x16x32_bf16 v[68:71], a[108:111], a[44:47], v[68:71]\n" \
  "s_waitcnt lgkmcnt(4)\n" \
  "v_mfma_f32_16x16x32_bf16 v[72:75], a[112:115], a[40:43], v[72:75]\n" \
  "ds_read_b64_tr_b16 a[96:97], v28 offset:49152\n" \
  "v_mfma_f32_16x16x32_bf16 v[72:75], a[116:119], a[44:47], v[72:75]\n" \
  "ds_read_b64_tr_b16 a[98:99], v28 offset:50176\n" \
  "v_mfma_f32_16x16x32_bf16 v[104:107], a[116:119], a[52:55], v[104:107]\n" \
  "ds_read_b64_tr_b16 a[100:101], v28 offset:57344\n" \
  "v_mfma_f32_16x16x32_bf16 v[104:107], a[112:115], a[48:51], v[104:107]\n" \
  "ds_read_b64_tr_b16 a[102:103], v28 offset:58368\n" \
  "v_mfma_f32_16x16x32_bf16 v[136:139], a[112:115], a[56:59], v[136:139]\n" \
  "v_add_u32 v28, 65536, %[vl4]\n" \
  "v_cvt_pk_bf16_f32 v160, v64, v65\n" \
  "v_cvt_pk_bf16_f32 v161, v66, v67\n" \
  "v_mfma_f32_16x16x32_bf16 v[136:139], a[116:119], a[60:63], v[136:139]\n" \
  "v_cvt_pk_bf16_f32 v162, v68, v69\n" \
  "s_waitcnt lgkmcnt(4)\n" \
  "v_mfma_f32_16x16x32_bf16 v[140:143], a[120:123], a[56:59], v[140:143]\n" \
  "ds_read_b64_tr_b16 a[104:105], v29 offset:49152\n" \
  "v_cvt_pk_bf16_f32 v163, v70, v71\n" \
  "v_cvt_pk_bf16_f32 v164, v96, v97\n" \
  "v_mfma_f32_16x16x32_bf16 v[140:143], a[124:127], a[60:63], v[140:143]\n" \
  "ds_read_b64_tr_b16 a[106:107], v29 offset:50176\n" \
  "v_cvt_pk_bf16_f32 v165, v98, v99\n" \
  "v_mfma_f32_16x16x32_bf16 v[108:111], a[124:127], a[52:55], v[108:111]\n" \
  "ds_read_b64_tr_b16 a[108:109], v29 offset:57344\n" \
  "v_cvt_pk_bf16_f32 v166, v100, v101\n" \
  "v_cvt_pk_bf16_f32 v167, v102, v103\n" \
  "v_mfma_f32_16x16x32_bf16 v[108:111], a[120:123], a[48:51], v[108:111]\n" \
  "ds_read_b64_tr_b16 a[110:111], v29 offset:58368\n" \
  "v_cvt_pk_bf16_f32 v168, v128, v129\n" \
  "v_mfma_f32_16x16x32_bf16 v[76:79], a[120:123], a[40:43], v[76:79]\n" \
  "v_add_u32 v29, 65536, %[vl5]\n" \
  "v_cvt_pk_bf16_f32 v169, v130, v131\n" \
  "v_cvt_pk_bf16_f32 v170, v132, v133\n" \
  "v_mfma_f32_16x16x32_bf16 v[76:79], a[124:127], a[44:47], v[76:79]\n" \
  "v_cvt_pk_bf16_f32 v171, v134, v135\n" \
  "s_waitcnt lgkmcnt(4)\n" \
  "v_mfma_f32_16x16x32_bf16 v[80:83], a[96:99], a[40:43], v[80:83]\n" \
  "ds_read_b64_tr_b16 a[112:113], v30 offset:49152\n" \
  "v_mfma_f32_16x16x32_bf16 v[80:83], a[100:103], a[44:47], v[80:83]\n" \
  "ds_read_b64_tr_b16 a[114:115], v30 offset:50176\n" \
  "buffer_store_dwordx4 v[160:163], %[po1], %[srdp], 0 offen offset:0 sc1\n" \
  "v_mfma_f32_16x16x32_bf16 v[112:115], a[100:103], a[52:55], v[112:115]\n" \
  "ds_read_b64_tr_b16 a[116:117], v30 offset:57344\n" \
  "buffer_store_dwordx4 v[164:167], %[po2], %[srdp], 0 offen offset:0 sc1\n" \
  "v_mfma_f32_16x16x32_bf16 v[112:115], a[96:99], a[48:51], v[112:115]\n" \
  "ds_read_b64_tr_b16 a[118:119], v30 offset:58368\n" \
  "buffer_store_dwordx4 v[168:171], %[po3], %[srdp], 0 offen offset:0 sc1\n" \
  "v_mfma_f32_16x16x32_bf16 v[144:147], a[96:99], a[56:59], v[144:147]\n" \
  "v_add_u32 v30, 65536, %[vl6]\n" \
  "v_cvt_pk_bf16_f32 v172, v72, v73\n" \
  "v_cvt_pk_bf16_f32 v173, v74, v75\n" \
  "v_mfma_f32_16x16x32_bf16 v[144:147], a[100:103], a[60:63], v[144:147]\n" \
  "v_cvt_pk_bf16_f32 v174, v76, v77\n" \
  "s_waitcnt lgkmcnt(4)\n" \
  "v_mfma_f32_16x16x32_bf16 v[148:151], a[104:107], a[56:59], v[148:151]\n" \
  "ds_read_b64_tr_b16 a[120:121], v31 offset:49152\n" \
  "v_cvt_pk_bf16_f32 v175, v78, v79\n" \
  "v_cvt_pk_bf16_f32 v176, v104, v105\n" \
  "v_mfma_f32_16x16x32_bf16 v[148:151], a[108:111], a[60:63], v[148:151]\n" \
  "ds_read_b64_tr_b16 a[122:123], v31 offset:50176\n" \
  "v_cvt_pk_bf16_f32 v177, v106, v107\n" \
  "v_mfma_f32_16x16x32_bf16 v[116:119], a[108:111], a[52:55], v[116:119]\n" \
  "ds_read_b64_tr_b16 a[124:125], v31 offset:57344\n" \
  "v_cvt_pk_bf16_f32 v178, v108, v109\n" \
  "v_cvt_pk_bf16_f32 v179, v110, v111\n" \
  "v_mfma_f32_16x16x32_bf16 v[116:119], a[104:107], a[48:51], v[116:119]\n" \
  "ds_read_b64_tr_b16 a[126:127], v31 offset:58368\n" \
  "v_cvt_pk_bf16_f32 v180, v136, v137\n" \
  "v_mfma_f32_16x16x32_bf16 v[84:87], a[104:107], a[40:43], v[84:87]\n" \
  "v_add_u32 v31, 65536, %[vl7]\n" \
  "v_cvt_pk_bf16_f32 v181, v138, v139\n" \
  "v_cvt_pk_bf16_f32 v182, v140, v141\n" \
  "v_mfma_f32_16x16x32_bf16 v[84:87], a[108:111], a[44:47], v[84:87]\n" \
  "v_cvt_pk_bf16_f32 v183, v142, v143\n" \
  "s_waitcnt lgkmcnt(4)\n" \
  "v_mfma_f32_16x16x32_bf16 v[88:91], a[112:115], a[40:43], v[88:91]\n" \
  "ds_read_b64_tr_b16 a[208:209], v24 offset:0\n" \
  "ds_read_b64_tr_b16 a[210:211], v24 offset:1024\n" \
  "v_mfma_f32_16x16x32_bf16 v[88:91], a[116:119], a[44:47], v[88:91]\n" \
  "ds_read_b64_tr_b16 a[212:213], v24 offset:8192\n" \
  "buffer_store_dwordx4 v[172:175], %[po1], %[srdp], 0 offen offset:1024 sc1\n" \
  "v_mfma_f32_16x16x32_bf16 v[120:123], a[116:119], a[52:55], v[120:123]\n" \
  "ds_read_b64_tr_b16 a[214:215], v24 offset:9216\n" \
  "buffer_store_dwordx4 v[176:179], %[po2], %[srdp], 0 offen offset:1024 sc1\n" \
  "v_mfma_f32_16x16x32_bf16 v[120:123], a[112:115], a[48:51], v[120:123]\n" \
  "ds_read_b64_tr_b16 a[216:217], v25 offset:0\n" \
  "ds_read_b64_tr_b16 a[218:219], v25 offset:1024\n" \
  "buffer_store_dwordx4 v[180:183], %[po3], %[srdp], 0 offen offset:1024 sc1\n" \
  "v_mfma_f32_16x16x32_bf16 v[152:155], a[112:115], a[56:59], v[152:155]\n" \
  "ds_read_b64_tr_b16 a[220:221], v25 offset:8192\n" \
  "v_cvt_pk_bf16_f32 v184, v80, v81\n" \
  "v_cvt_pk_bf16_f32 v185, v82, v83\n" \
  "v_mfma_f32_16x16x32_bf16 v[152:155], a[116:119], a[60:63], v[152:155]\n" \
  "ds_read_b64_tr_b16 a[222:223], v25 offset:9216\n" \
  "v_cvt_pk_bf16_f32 v186, v84, v85\n" \
  "s_waitcnt lgkmcnt(8)\n" \
  "v_mfma_f32_16x16x32_bf16 v[156:159], a[120:123], a[56:59], v[156:159]\n" \
  "ds_read_b64_tr_b16 a[224:225], v26 offset:0\n" \
  "ds_read_b64_tr_b16 a[226:227], v26 offset:1024\n" \
  "v_cvt_pk_bf16_f32 v187, v86, v87\n" \
  "v_cvt_pk_bf16_f32 v188, v112, v113\n" \
  "v_mfma_f32_16x16x32_bf16 v[156:159], a[124:127], a[60:63], v[156:159]\n" \
  "ds_read_b64_tr_b16 a[228:229], v26 offset:8192\n" \
  "v_cvt_pk_bf16_f32 v189, v114, v115\n" \
  "v_mfma_f32_16x16x32_bf16 v[124:127], a[124:127], a[52:55], v[124:127]\n" \
  "ds_read_b64_tr_b16 a[230:231], v26 offset:9216\n" \
  "v_cvt_pk_bf16_f32 v190, v116, v117\n" \
  "v_cvt_pk_bf16_f32 v191, v118, v119\n" \
  "v_mfma_f32_16x16x32_bf16 v[124:127], a[120:123], a[48:51], v[124:127]\n" \
  "ds_read_b64_tr_b16 a[232:233], v27 offset:0\n" \
  "ds_read_b64_tr_b16 a[234:235], v27 offset:1024\n" \
  "v_cvt_pk_bf16_f32 v192, v144, v145\n" \
  "v_mfma_f32_16x16x32_bf16 v[92:95], a[120:123], a[40:43], v[92:95]\n" \
  "ds_read_b64_tr_b16 a[236:237], v27 offset:8192\n" \
  "v_cvt_pk_bf16_f32 v193, v146, v147\n" \
  "v_cvt_pk_bf16_f32 v194, v148, v149\n" \
  "v_mfma_f32_16x16x32_bf16 v[92:95], a[124:127], a[44:47], v[92:95]\n" \
  "ds_read_b64_tr_b16 a[238:239], v27 offset:9216\n" \
  "v_cvt_pk_bf16_f32 v195, v150, v151\n" \
  "s_waitcnt vmcnt(6) lgkmcnt(8)\n" \
  "v_mfma_f32_16x16x32_bf16 v[32:35], a[208:211], a[128:131], v[32:35]\n" \
  "ds_read_b64_tr_b16 a[96:97], v28 offset:0\n" \
  "ds_read_b64_tr_b16 a[98:99], v28 offset:1024\n" \
  "v_mfma_f32_16x16x32_bf16 v[32:35], a[212:215], a[132:135], v[32:35]\n" \
  "ds_read_b64_tr_b16 a[100:101], v28 offset:8192\n" \
  "ds_read_b64_tr_b16 a[102:103], v28 offset:9216\n" \
  "v_mfma_f32_16x16x32_bf16 v[36:39], a[220:223], a[132:135], v[36:39]\n" \
  "ds_read_b64_tr_b16 a[104:105], v29 offset:0\n" \
  "ds_read_b64_tr_b16 a[106:107], v29 offset:1024\n" \
  "buffer_store_dwordx4 v[184:187], %[po1], %[srdp], 0 offen offset:2048 sc1\n" \
  "v_mfma_f32_16x16x32_bf16 v[36:39], a[216:219], a[128:131], v[36:39]\n" \
  "ds_read_b64_tr_b16 a[108:109], v29 offset:8192\n" \
  "ds_read_b64_tr_b16 a[110:111], v29 offset:9216\n" \
  "s_waitcnt lgkmcnt(8)\n" \
  "v_mfma_f32_16x16x32_bf16 v[40:43], a[224:227], a[128:131], v[40:43]\n" \
  "ds_read_b64_tr_b16 a[112:113], v30 offset:0\n" \
  "ds_read_b64_tr_b16 a[114:115], v30 offset:1024\n" \
  "v_cvt_pk_bf16_f32 v196, v88, v89\n" "v_cvt_pk_bf16_f32 v197, v90, v91\n" \
  "v_mfma_f32_16x16x32_bf16 v[40:43], a[228:231], a[132:135], v[40:43]\n" \
  "ds_read_b64_tr_b16 a[116:117], v30 offset:8192\n" \
  "ds_read_b64_tr_b16 a[118:119], v30 offset:9216\n" \
  "buffer_store_dwordx4 v[188:191], %[po2], %[srdp], 0 offen offset:2048 sc1\n" \
  "v_mfma_f32_16x16x32_bf16 v[44:47], a[236:239], a[132:135], v[44:47]\n" \
  "ds_read_b64_tr_b16 a[120:121], v31 offset:0\n" \
  "ds_read_b64_tr_b16 a[122:123], v31 offset:1024\n" \
  "v_cvt_pk_bf16_f32 v198, v92, v93\n" "v_cvt_pk_bf16_f32 v199, v94, v95\n" \
  "v_mfma_f32_16x16x32_bf16 v[44:47], a[232:235], a[128:131], v[44:47]\n" \
  "ds_read_b64_tr_b16 a[124:125], v31 offset:8192\n" \
  "ds_read_b64_tr_b16 a[126:127], v31 offset:9216\n" \
  "v_cvt_pk_bf16_f32 v200, v120, v121\n" "v_cvt_pk_bf16_f32 v201, v122, v123\n" \
  "s_waitcnt lgkmcnt(8)\n" \
  "v_mfma_f32_16x16x32_bf16 v[48:51], a[96:99], a[128:131], v[48:51]\n" \
  "ds_read_b64_tr_b16 a[208:209], v24 offset:16384\n" \
  "ds_read_b64_tr_b16 a[210:211], v24 offset:17408\n" \
  "buffer_store_dwordx4 v[192:195], %[po3], %[srdp], 0 offen offset:2048 sc1\n" \
  "v_mfma_f32_16x16x32_bf16 v[48:51], a[100:103], a[132:135], v[48:51]\n" \
  "ds_read_b64_tr_b16 a[212:213], v24 offset:24576\n" \
  "ds_read_b64_tr_b16 a[214:215], v24 offset:25600\n" \
  "v_cvt_pk_bf16_f32 v202, v124, v125\n" "v_cvt_pk_bf16_f32 v203, v126, v127\n" \
  "v_mfma_f32_16x16x32_bf16 v[52:55], a[108:111], a[132:135], v[52:55]\n" \
  "ds_read_b64_tr_b16 a[216:217], v25 offset:16384\n" \
  "ds_read_b64_tr_b16 a[218:219], v25 offset:17408\n" \
  "v_cvt_pk_bf16_f32 v204, v152, v153\n" "v_cvt_pk_bf16_f32 v205, v154, v155\n" \
  "v_mfma_f32_16x16x32_bf16 v[52:55], a[104:107], a[128:131], v[52:55]\n" \
  "ds_read_b64_tr_b16 a[220:221], v25 offset:24576\n" \
  "ds_read_b64_tr_b16 a[222:223], v25 offset:25600\n" \
  "v_cvt_pk_bf16_f32 v206, v156, v157\n" "v_cvt_pk_bf16_f32 v207, v158, v159\n" \
  "s_waitcnt lgkmcnt(8)\n" \
  "v_mfma_f32_16x16x32_bf16 v[56:59], a[112:115], a[128:131], v[56:59]\n" \
  "ds_read_b64_tr_b16 a[224:225], v26 offset:16384\n" \
  "ds_read_b64_tr_b16 a[226:227], v26 offset:17408\n" \
  "buffer_store_dwordx4 v[196:199], %[po1], %[srdp], 0 offen offset:3072 sc1\n" \
  "v_mfma_f32_16x16x32_bf16 v[56:59], a[116:119], a[132:135], v[56:59]\n" \
  "ds_read_b64_tr_b16 a[228:229], v26 offset:24576\n" \
  "ds_read_b64_tr_b16 a[230:231], v26 offset:25600\n" \
  "v_mfma_f32_16x16x32_bf16 v[60:63], a[124:127], a[132:135], v[60:63]\n" \
  "ds_read_b64_tr_b16 a[232:233], v27 offset:16384\n" \
  "ds_read_b64_tr_b16 a[234:235], v27 offset:17408\n" \
  "v_mfma_f32_16x16x32_bf16 v[60:63], a[120:123], a[128:131], v[60:63]\n" \
  "ds_read_b64_tr_b16 a[236:237], v27 offset:24576\n" \
  "ds_read_b64_tr_b16 a[238:239], v27 offset:25600\n" \
  "buffer_store_dwordx4 v[200:203], %[po2], %[srdp], 0 offen offset:3072 sc1\n" \
  "s_waitcnt lgkmcnt(8)\n" \
  "v_mfma_f32_16x16x32_bf16 v[32:35], a[208:211], a[136:139], v[32:35]\n" \
  "ds_read_b64_tr_b16 a[96:97], v28 offset:16384\n" \
  "ds_read_b64_tr_b16 a[98:99], v28 offset:17408\n" \
  "v_mfma_f32_16x16x32_bf16 v[32:35], a[212:215], a[140:143], v[32:35]\n" \
  "ds_read_b64_tr_b16 a[100:101], v28 offset:24576\n" \
  "ds_read_b64_tr_b16 a[102:103], v28 offset:25600\n" \
  "v_mfma_f32_16x16x32_bf16 v[36:39], a[220:223], a[140:143], v[36:39]\n" \
  "ds_read_b64_tr_b16 a[104:105], v29 offset:16384\n" \
  "ds_read_b64_tr_b16 a[106:107], v29 offset:17408\n" \
  "buffer_store_dwordx4 v[204:207], %[po3], %[srdp], 0 offen offset:3072 sc1\n" \
  "v_mfma_f32_16x16x32_bf16 v[36:39], a[216:219], a[136:139], v[36:39]\n" \
  "ds_read_b64_tr_b16 a[108:109], v29 offset:24576\n" \
  "ds_read_b64_tr_b16 a[110:111], v29 offset:25600\n" \
  "s_waitcnt lgkmcnt(8)\n" \
  "v_mfma_f32_16x16x32_bf16 v[40:43], a[224:227], a[136:139], v[40:43]\n" \
  "ds_read_b64_tr_b16 a[112:113], v30 offset:16384\n" \
  "ds_read_b64_tr_b16 a[114:115], v30 offset:17408\n" \
  "v_mfma_f32_16x16x32_bf16 v[40:43], a[228:231], a[140:143], v[40:43]\n" \
  "ds_read_b64_tr_b16 a[116:117], v30 offset:24576\n" \
  "ds_read_b64_tr_b16 a[118:119], v30 offset:25600\n" \
  "v_mfma_f32_16x16x32_bf16 v[44:47], a[236:239], a[140:143], v[44:47]\n" \
  "ds_read_b64_tr_b16 a[120:121], v31 offset:16384\n" \
  "ds_read_b64_tr_b16 a[122:123], v31 offset:17408\n" \
  "v_mfma_f32_16x16x32_bf16 v[44:47], a[232:235], a[136:139], v[44:47]\n" \
  "ds_read_b64_tr_b16 a[124:125], v31 offset:24576\n" \
  "ds_read_b64_tr_b16 a[126:127], v31 offset:25600\n" \
  "s_waitcnt lgkmcnt(8)\n" \
  "v_mfma_f32_16x16x32_bf16 v[48:51], a[96:99], a[136:139], v[48:51]\n" \
  "ds_read_b64_tr_b16 a[208:209], v24 offset:32768\n" \
  "ds_read_b64_tr_b16 a[210:211], v24 offset:33792\n" \
  "v_mfma_f32_16x16x32_bf16 v[48:51], a[100:103], a[140:143], v[48:51]\n" \
  "ds_read_b64_tr_b16 a[212:213], v24 offset:40960\n" \
  "ds_read_b64_tr_b16 a[214:215], v24 offset:41984\n" \
  "v_mfma_f32_16x16x32_bf16 v[52:55], a[108:111], a[140:143], v[52:55]\n" \
  "ds_read_b64_tr_b16 a[216:217], v25 offset:32768\n" \
  "ds_read_b64_tr_b16 a[218:219], v25 offset:33792\n" \
  "v_mfma_f32_16x16x32_bf16 v[52:55], a[104:107], a[136:139], v[52:55]\n" \
  "ds_read_b64_tr_b16 a[220:221], v25 offset:40960\n" \
  "ds_read_b64_tr_b16 a[222:223], v25 offset:41984\n" \
  "s_waitcnt lgkmcnt(8)\n" \
  "v_mfma_f32_16x16x32_bf16 v[56:59], a[112:115], a[136:139], v[56:59]\n" \
  "ds_read_b64_tr_b16 a[224:225], v26 offset:32768\n" \
  "ds_read_b64_tr_b16 a[226:227], v26 offset:33792\n" \
  "v_mfma_f32_16x16x32_bf16 v[56:59], a[116:119], a[140:143], v[56:59]\n" \
  "ds_read_b64_tr_b16 a[228:229], v26 offset:40960\n" \
  "ds_read_b64_tr_b16 a[230:231], v26 offset:41984\n" \
  "v_mfma_f32_16x16x32_bf16 v[60:63], a[124:127], a[140:143], v[60:63]\n" \
  "ds_read_b64_tr_b16 a[232:233], v27 offset:32768\n" \
  "ds_read_b64_tr_b16 a[234:235], v27 offset:33792\n" \
  "v_mfma_f32_16x16x32_bf16 v[60:63], a[120:123], a[136:139], v[60:63]\n" \
  "ds_read_b64_tr_b16 a[236:237], v27 offset:40960\n" \
  "ds_read_b64_tr_b16 a[238:239], v27 offset:41984\n" \
  "s_waitcnt lgkmcnt(8)\n" \
  "v_mfma_f32_16x16x32_bf16 v[32:35], a[208:211], a[144:147], v[32:35]\n" \
  "ds_read_b64_tr_b16 a[96:97], v28 offset:32768\n" \
  "ds_read_b64_tr_b16 a[98:99], v28 offset:33792\n" \
  "v_mfma_f32_16x16x32_bf16 v[32:35], a[212:215], a[148:151], v[32:35]\n" \
  "ds_read_b64_tr_b16 a[100:101], v28 offset:40960\n" \
  "ds_read_b64_tr_b16 a[102:103], v28 offset:41984\n" \
  "v_mfma_f32_16x16x32_bf16 v[36:39], a[220:223], a[148:151], v[36:39]\n" \
  "ds_read_b64_tr_b16 a[104:105], v29 offset:32768\n" \
  "ds_read_b64_tr_b16 a[106:107], v29 offset:33792\n" \
  "v_mfma_f32_16x16x32_bf16 v[36:39], a[216:219], a[144:147], v[36:39]\n" \
  "ds_read_b64_tr_b16 a[108:109], v29 offset:40960\n" \
  "ds_read_b64_tr_b16 a[110:111], v29 offset:41984\n" \
  "s_waitcnt lgkmcnt(8)\n" \
  "v_mfma_f32_16x16x32_bf16 v[40:43], a[224:227], a[144:147], v[40:43]\n" \
  "ds_read_b64_tr_b16 a[112:113], v30 offset:32768\n" \
  "ds_read_b64_tr_b16 a[114:115], v30 offset:33792\n" \
  "v_mfma_f32_16x16x32_bf16 v[40:43], a[228:231], a[148:151], v[40:43]\n" \
  "ds_read_b64_tr_b16 a[116:117], v30 offset:40960\n" \
  "ds_read_b64_tr_b16 a[118:119], v30 offset:41984\n" \
  "v_mfma_f32_16x16x32_bf16 v[44:47], a[236:239], a[148:151], v[44:47]\n" \
  "ds_read_b64_tr_b16 a[120:121], v31 offset:32768\n" \
  "ds_read_b64_tr_b16 a[122:123], v31 offset:33792\n" \
  "v_mfma_f32_16x16x32_bf16 v[44:47], a[232:235], a[144:147], v[44:47]\n" \
  "ds_read_b64_tr_b16 a[124:125], v31 offset:40960\n" \
  "ds_read_b64_tr_b16 a[126:127], v31 offset:41984\n" \
  "s_waitcnt lgkmcnt(8)\n" \
  "v_mfma_f32_16x16x32_bf16 v[48:51], a[96:99], a[144:147], v[48:51]\n" \
  "ds_read_b64_tr_b16 a[208:209], v24 offset:49152\n" \
  "ds_read_b64_tr_b16 a[210:211], v24 offset:50176\n" \
  "v_mfma_f32_16x16x32_bf16 v[48:51], a[100:103], a[148:151], v[48:51]\n" \
  "ds_read_b64_tr_b16 a[212:213], v24 offset:57344\n" \
  "ds_read_b64_tr_b16 a[214:215], v24 offset:58368\n" \
  "v_mfma_f32_16x16x32_bf16 v[52:55], a[108:111], a[148:151], v[52:55]\n" \
  "ds_read_b64_tr_b16 a[216:217], v25 offset:49152\n" \
  "ds_read_b64_tr_b16 a[218:219], v25 offset:50176\n" \
  "v_mfma_f32_16x16x32_bf16 v[52:55], a[104:107], a[144:147], v[52:55]\n" \
  "ds_read_b64_tr_b16 a[220:221], v25 offset:57344\n" \
  "ds_read_b64_tr_b16 a[222:223], v25 offset:58368\n" \
  "s_waitcnt lgkmcnt(8)\n" \
  "v_mfma_f32_16x16x32_bf16 v[56:59], a[112:115], a[144:147], v[56:59]\n" \
  "ds_read_b64_tr_b16 a[224:225], v26 offset:49152\n" \
  "ds_read_b64_tr_b16 a[226:227], v26 offset:50176\n" \
  "v_mfma_f32_16x16x32_bf16 v[56:59], a[116:119], a[148:151], v[56:59]\n" \
  "ds_read_b64_tr_b16 a[228:229], v26 offset:57344\n" \
  "ds_read_b64_tr_b16 a[230:231], v26 offset:58368\n" \
  "v_mfma_f32_16x16x32_bf16 v[60:63], a[124:127], a[148:151], v[60:63]\n" \
  "ds_read_b64_tr_b16 a[232:233], v27 offset:49152\n" \
  "ds_read_b64_tr_b16 a[234:235], v27 offset:50176\n" \
  "v_mfma_f32_16x16x32_bf16 v[60:63], a[120:123], a[144:147], v[60:63]\n" \
  "ds_read_b64_tr_b16 a[236:237], v27 offset:57344\n" \
  "ds_read_b64_tr_b16 a[238:239], v27 offset:58368\n" \
  "s_waitcnt lgkmcnt(8)\n" \
  "v_mfma_f32_16x16x32_bf16 v[32:35], a[208:211], a[152:155], v[32:35]\n" \
  "ds_read_b64_tr_b16 a[96:97], v28 offset:49152\n" \
  "ds_read_b64_tr_b16 a[98:99], v28 offset:50176\n" \
  "v_mfma_f32_16x16x32_bf16 v[32:35], a[212:215], a[156:159], v[32:35]\n" \
  "ds_read_b64_tr_b16 a[100:101], v28 offset:57344\n" \
  "ds_read_b64_tr_b16 a[102:103], v28 offset:58368\n" \
  "v_mfma_f32_16x16x32_bf16 v[36:39], a[220:223], a[156:159], v[36:39]\n" \
  "ds_read_b64_tr_b16 a[104:105], v29 offset:49152\n" \
  "ds_read_b64_tr_b16 a[106:107], v29 offset:50176\n" \
  "v_add_u32 v24, 131072, %[vl0]\n" \
  "v_mfma_f32_16x16x32_bf16 v[36:39], a[216:219], a[152:155], v[36:39]\n" \
  "ds_read_b64_tr_b16 a[108:109], v29 offset:57344\n" \
  "ds_read_b64_tr_b16 a[110:111], v29 offset:58368\n" \
  "v_add_u32 v25, 131072, %[vl1]\n" \
  "s_waitcnt lgkmcnt(8)\n" \
  "v_mfma_f32_16x16x32_bf16 v[40:43], a[224:227], a[152:155], v[40:43]\n" \
  "ds_read_b64_tr_b16 a[112:113], v30 offset:49152\n" \
  "ds_read_b64_tr_b16 a[114:115], v30 offset:50176\n" \
  "v_mfma_f32_16x16x32_bf16 v[40:43], a[228:231], a[156:159], v[40:43]\n" \
  "ds_read_b64_tr_b16 a[116:117], v30 offset:57344\n" \
  "ds_read_b64_tr_b16 a[118:119], v30 offset:58368\n" \
  "v_mfma_f32_16x16x32_bf16 v[44:47], a[236:239], a[156:159], v[44:47]\n" \
  "ds_read_b64_tr_b16 a[120:121], v31 offset:49152\n" \
  "ds_read_b64_tr_b16 a[122:123], v31 offset:50176\n" \
  "v_add_u32 v26, 131072, %[vl2]\n" \
  "v_mfma_f32_16x16x32_bf16 v[44:47], a[232:235], a[152:155], v[44:47]\n" \
  "ds_read_b64_tr_b16 a[124:125], v31 offset:57344\n" \
  "ds_read_b64_tr_b16 a[126:127], v31 offset:58368\n" \
  "v_add_u32 v27, 131072, %[vl3]\n" \
  "s_waitcnt lgkmcnt(8)\n" \
  "v_mfma_f32_16x16x32_bf16 v[48:51], a[96:99], a[152:155], v[48:51]\n" \
  "ds_read_b64_tr_b16 a[208:209], v24 offset:0\n" \
  "ds_read_b64_tr_b16 a[210:211], v24 offset:1024\n" \
  "v_mfma_f32_16x16x32_bf16 v[48:51], a[100:103], a[156:159], v[48:51]\n" \
  "ds_read_b64_tr_b16 a[212:213], v24 offset:8192\n" \
  "ds_read_b64_tr_b16 a[214:215], v24 offset:9216\n" \
  "v_mfma_f32_16x16x32_bf16 v[52:55], a[108:111], a[156:159], v[52:55]\n" \
  "ds_read_b64_tr_b16 a[216:217], v25 offset:0\n" \
  "ds_read_b64_tr_b16 a[218:219], v25 offset:1024\n" \
  "v_add_u32 v28, 131072, %[vl4]\n" \
  "v_mfma_f32_16x16x32_bf16 v[52:55], a[104:107], a[152:155], v[52:55]\n" \
  "ds_read_b64_tr_b16 a[220:221], v25 offset:8192\n" \
  "ds_read_b64_tr_b16 a[222:223], v25 offset:9216\n" \
  "v_add_u32 v29, 131072, %[vl5]\n" \
  "s_waitcnt lgkmcnt(8)\n" \
  "v_mfma_f32_16x16x32_bf16 v[56:59], a[112:115], a[152:155], v[56:59]\n" \
  "ds_read_b64_tr_b16 a[224:225], v26 offset:0\n" \
  "ds_read_b64_tr_b16 a[226:227], v26 offset:1024\n" \
  "v_mfma_f32_16x16x32_bf16 v[56:59], a[116:119], a[156:159], v[56:59]\n" \
  "ds_read_b64_tr_b16 a[228:229], v26 offset:8192\n" \
  "ds_read_b64_tr_b16 a[230:231], v26 offset:9216\n" \
  "v_mfma_f32_16x16x32_bf16 v[60:63], a[124:127], a[156:159], v[60:63]\n" \
  "ds_read_b64_tr_b16 a[232:233], v27 offset:0\n" \
  "ds_read_b64_tr_b16 a[234:235], v27 offset:1024\n" \
  "v_add_u32 v30, 131072, %[vl6]\n" \
  "v_mfma_f32_16x16x32_bf16 v[60:63], a[120:123], a[152:155], v[60:63]\n" \
  "ds_read_b64_tr_b16 a[236:237], v27 offset:8192\n" \
  "ds_read_b64_tr_b16 a[238:239], v27 offset:9216\n" \
  "v_add_u32 v31, 131072, %[vl7]\n" \
  "s_waitcnt vmcnt(0) lgkmcnt(8)\n" \
  "buffer_store_dwordx2 %[tagv], %[fo], %[srdw], 0 offen sc1\n" \
  "v_mfma_f32_16x16x32_bf16 v[32:35], a[208:211], a[160:163], v[32:35]\n" \
  "ds_read_b64_tr_b16 a[96:97], v28 offset:0\n" \
  "ds_read_b64_tr_b16 a[98:99], v28 offset:1024\n" \
  "v_mfma_f32_16x16x32_bf16 v[32:35], a[212:215], a[164:167], v[32:35]\n" \
  "ds_read_b64_tr_b16 a[100:101], v28 offset:8192\n" \
  "ds_read_b64_tr_b16 a[102:103], v28 offset:9216\n" \
  "v_mfma_f32_16x16x32_bf16 v[36:39], a[220:223], a[164:167], v[36:39]\n" \
  "ds_read_b64_tr_b16 a[104:105], v29 offset:0\n" \
  "ds_read_b64_tr_b16 a[106:107], v29 offset:1024\n" \
  "v_mfma_f32_16x16x32_bf16 v[36:39], a[216:219], a[160:163], v[36:39]\n" \
  "ds_read_b64_tr_b16 a[108:109], v29 offset:8192\n" \
  "ds_read_b64_tr_b16 a[110:111], v29 offset:9216\n" \
  "s_waitcnt lgkmcnt(8)\n" \
  "v_mfma_f32_16x16x32_bf16 v[40:43], a[224:227], a[160:163], v[40:43]\n" \
  "ds_read_b64_tr_b16 a[112:113], v30 offset:0\n" \
  "ds_read_b64_tr_b16 a[114:115], v30 offset:1024\n" \
  "v_mfma_f32_16x16x32_bf16 v[40:43], a[228:231], a[164:167], v[40:43]\n" \
  "ds_read_b64_tr_b16 a[116:117], v30 offset:8192\n" \
  "ds_read_b64_tr_b16 a[118:119], v30 offset:9216\n" \
  "v_mfma_f32_16x16x32_bf16 v[44:47], a[236:239], a[164:167], v[44:47]\n" \
  "ds_read_b64_tr_b16 a[120:121], v31 offset:0\n" \
  "ds_read_b64_tr_b16 a[122:123], v31 offset:1024\n" \
  "v_mfma_f32_16x16x32_bf16 v[44:47], a[232:235], a[160:163], v[44:47]\n" \
  "ds_read_b64_tr_b16 a[124:125], v31 offset:8192\n" \
  "ds_read_b64_tr_b16 a[126:127], v31 offset:9216\n" \
  "s_waitcnt lgkmcnt(8)\n" \
  "v_mfma_f32_16x16x32_bf16 v[48:51], a[96:99], a[160:163], v[48:51]\n" \
  "ds_read_b64_tr_b16 a[208:209], v24 offset:16384\n" \
  "ds_read_b64_tr_b16 a[210:211], v24 offset:17408\n" \
  "v_mfma_f32_16x16x32_bf16 v[48:51], a[100:103], a[164:167], v[48:51]\n" \
  "ds_read_b64_tr_b16 a[212:213], v24 offset:24576\n" \
  "ds_read_b64_tr_b16 a[214:215], v24 offset:25600\n" \
  "v_mfma_f32_16x16x32_bf16 v[52:55], a[108:111], a[164:167], v[52:55]\n" \
  "ds_read_b64_tr_b16 a[216:217], v25 offset:16384\n" \
  "ds_read_b64_tr_b16 a[218:219], v25 offset:17408\n" \
  "v_mfma_f32_16x16x32_bf16 v[52:55], a[104:107], a[160:163], v[52:55]\n" \
  "ds_read_b64_tr_b16 a[220:221], v25 offset:24576\n" \
  "ds_read_b64_tr_b16 a[222:223], v25 offset:25600\n" \
  "s_waitcnt lgkmcnt(8)\n" \
  "v_mfma_f32_16x16x32_bf16 v[56:59], a[112:115], a[160:163], v[56:59]\n" \
  "ds_read_b64_tr_b16 a[224:225], v26 offset:16384\n" \
  "ds_read_b64_tr_b16 a[226:227], v26 offset:17408\n" \
  "v_mfma_f32_16x16x32_bf16 v[56:59], a[116:119], a[164:167], v[56:59]\n" \
  "ds_read_b64_tr_b16 a[228:229], v26 offset:24576\n" \
  "ds_read_b64_tr_b16 a[230:231], v26 offset:25600\n" \
  "v_mfma_f32_16x16x32_bf16 v[60:63], a[124:127], a[164:167], v[60:63]\n" \
  "ds_read_b64_tr_b16 a[232:233], v27 offset:16384\n" \
  "ds_read_b64_tr_b16 a[234:235], v27 offset:17408\n" \
  "v_mfma_f32_16x16x32_bf16 v[60:63], a[120:123], a[160:163], v[60:63]\n" \
  "ds_read_b64_tr_b16 a[236:237], v27 offset:24576\n" \
  "ds_read_b64_tr_b16 a[238:239], v27 offset:25600\n" \
  "s_waitcnt lgkmcnt(8)\n" \
  "v_mfma_f32_16x16x32_bf16 v[32:35], a[208:211], a[168:171], v[32:35]\n" \
  "ds_read_b64_tr_b16 a[96:97], v28 offset:16384\n" \
  "ds_read_b64_tr_b16 a[98:99], v28 offset:17408\n" \
  "v_mfma_f32_16x16x32_bf16 v[32:35], a[212:215], a[172:175], v[32:35]\n" \
  "ds_read_b64_tr_b16 a[100:101], v28 offset:24576\n" \
  "ds_read_b64_tr_b16 a[102:103], v28 offset:25600\n" \
  "v_mfma_f32_16x16x32_bf16 v[36:39], a[220:223], a[172:175], v[36:39]\n" \
  "ds_read_b64_tr_b16 a[104:105], v29 offset:16384\n" \
  "ds_read_b64_tr_b16 a[106:107], v29 offset:17408\n" \
  "v_mov_b32 v24, %[vl0]\n" \
  "v_mfma_f32_16x16x32_bf16 v[36:39], a[216:219], a[168:171], v[36:39]\n" \
  "ds_read_b64_tr_b16 a[108:109], v29 offset:24576\n" \
  "ds_read_b64_tr_b16 a[110:111], v29 offset:25600\n" \
  "v_mov_b32 v25, %[vl1]\n" \
  "s_waitcnt lgkmcnt(8)\n" \
  "v_mfma_f32_16x16x32_bf16 v[40:43], a[224:227], a[168:171], v[40:43]\n" \
  "ds_read_b64_tr_b16 a[112:113], v30 offset:16384\n" \
  "ds_read_b64_tr_b16 a[114:115], v30 offset:17408\n" \
  "v_mfma_f32_16x16x32_bf16 v[40:43], a[228:231], a[172:175], v[40:43]\n" \
  "ds_read_b64_tr_b16 a[116:117], v30 offset:24576\n" \
  "ds_read_b64_tr_b16 a[118:119], v30 offset:25600\n" \
  "v_mfma_f32_16x16x32_bf16 v[44:47], a[236:239], a[172:175], v[44:47]\n" \
  "ds_read_b64_tr_b16 a[120:121], v31 offset:16384\n" \
  "ds_read_b64_tr_b16 a[122:123], v31 offset:17408\n" \
  "v_mov_b32 v26, %[vl2]\n" \
  "v_mfma_f32_16x16x32_bf16 v[44:47], a[232:235], a[168:171], v[44:47]\n" \
  "ds_read_b64_tr_b16 a[124:125], v31 offset:24576\n" \
  "ds_read_b64_tr_b16 a[126:127], v31 offset:25600\n" \
  "v_mov_b32 v27, %[vl3]\n" \
  "s_waitcnt lgkmcnt(8)\n" \
  "v_mfma_f32_16x16x32_bf16 v[48:51], a[96:99], a[168:171], v[48:51]\n" \
  "ds_read_b64_tr_b16 a[208:209], v24 offset:0\n" \
  "ds_read_b64_tr_b16 a[210:211], v24 offset:1024\n" \
  "v_mfma_f32_16x16x32_bf16 v[48:51], a[100:103], a[172:175], v[48:51]\n" \
  "ds_read_b64_tr_b16 a[212:213], v24 offset:8192\n" \
  "ds_read_b64_tr_b16 a[214:215], v24 offset:9216\n" \
  "v_mfma_f32_16x16x32_bf16 v[52:55], a[108:111], a[172:175], v[52:55]\n" \
  "ds_read_b64_tr_b16 a[216:217], v25 offset:0\n" \
  "ds_read_b64_tr_b16 a[218:219], v25 offset:1024\n" \
  "v_mov_b32 v28, %[vl4]\n" \
  "v_mfma_f32_16x16x32_bf16 v[52:55], a[104:107], a[168:171], v[52:55]\n" \
  "ds_read_b64_tr_b16 a[220:221], v25 offset:8192\n" \
  "ds_read_b64_tr_b16 a[222:223], v25 offset:9216\n" \
  "v_mov_b32 v29, %[vl5]\n" \
  "s_waitcnt lgkmcnt(8)\n" \
  "v_mfma_f32_16x16x32_bf16 v[56:59], a[112:115], a[168:171], v[56:59]\n" \
  "ds_read_b64_tr_b16 a[224:225], v26 offset:0\n" \
  "ds_read_b64_tr_b16 a[226:227], v26 offset:1024\n" \
  "v_mfma_f32_16x16x32_bf16 v[56:59], a[116:119], a[172:175], v[56:59]\n" \
  "ds_read_b64_tr_b16 a[228:229], v26 offset:8192\n" \
  "ds_read_b64_tr_b16 a[230:231], v26 offset:9216\n" \
  "v_mfma_f32_16x16x32_bf16 v[60:63], a[124:127], a[172:175], v[60:63]\n" \
  "ds_read_b64_tr_b16 a[232:233], v27 offset:0\n" \
  "ds_read_b64_tr_b16 a[234:235], v27 offset:1024\n" \
  "v_mov_b32 v30, %[vl6]\n" \
  "v_mfma_f32_16x16x32_bf16 v[60:63], a[120:123], a[168:171], v[60:63]\n" \
  "ds_read_b64_tr_b16 a[236:237], v27 offset:8192\n" \
  "ds_read_b64_tr_b16 a[238:239], v27 offset:9216\n" \
  "v_mov_b32 v31, %[vl7]\n" \
  "s_waitcnt lgkmcnt(8)\n" \
  "global_load_dwordx2 %[ft], %[fp], off sc1\n" \
  "v_mfma_f32_16x16x32_bf16 v[32:35], a[208:211], a[176:179], v[32:35]\n" \
  "ds_read_b64_tr_b16 a[96:97], v28 offset:0\n" \
  "ds_read_b64_tr_b16 a[98:99], v28 offset:1024\n" \
  "v_mfma_f32_16x16x32_bf16 v[32:35], a[212:215], a[180:183], v[32:35]\n" \
  "ds_read_b64_tr_b16 a[100:101], v28 offset:8192\n" \
  "ds_read_b64_tr_b16 a[102:103], v28 offset:9216\n" \
  "v_mfma_f32_16x16x32_bf16 v[36:39], a[220:223], a[180:183], v[36:39]\n" \
  "ds_read_b64_tr_b16 a[104:105], v29 offset:0\n" \
  "ds_read_b64_tr_b16 a[106:107], v29 offset:1024\n" \
  "v_mfma_f32_16x16x32_bf16 v[36:39], a[216:219], a[176:179], v[36:39]\n" \
  "ds_read_b64_tr_b16 a[108:109], v29 offset:8192\n" \
  "ds_read_b64_tr_b16 a[110:111], v29 offset:9216\n" \
  "s_waitcnt lgkmcnt(8)\n" \
  "v_mfma_f32_16x16x32_bf16 v[40:43], a[224:227], a[176:179], v[40:43]\n" \
  "ds_read_b64_tr_b16 a[112:113], v30 offset:0\n" \
  "ds_read_b64_tr_b16 a[114:115], v30 offset:1024\n" \
  "v_mfma_f32_16x16x32_bf16 v[40:43], a[228:231], a[180:183], v[40:43]\n" \
  "ds_read_b64_tr_b16 a[116:117], v30 offset:8192\n" \
  "ds_read_b64_tr_b16 a[118:119], v30 offset:9216\n" \
  "v_mfma_f32_16x16x32_bf16 v[44:47], a[236:239], a[180:183], v[44:47]\n" \
  "ds_read_b64_tr_b16 a[120:121], v31 offset:0\n" \
  "ds_read_b64_tr_b16 a[122:123], v31 offset:1024\n" \
  "v_mfma_f32_16x16x32_bf16 v[44:47], a[232:235], a[176:179], v[44:47]\n" \
  "ds_read_b64_tr_b16 a[124:125], v31 offset:8192\n" \
  "ds_read_b64_tr_b16 a[126:127], v31 offset:9216\n" \
  "s_waitcnt lgkmcnt(8)\n" \
  "v_mfma_f32_16x16x32_bf16 v[48:51], a[96:99], a[176:179], v[48:51]\n" \
  "ds_read_b64_tr_b16 a[208:209], v24 offset:16384\n" \
  "ds_read_b64_tr_b16 a[210:211], v24 offset:17408\n" \
  "v_mfma_f32_16x16x32_bf16 v[48:51], a[100:103], a[180:183], v[48:51]\n" \
  "ds_read_b64_tr_b16 a[212:213], v24 offset:24576\n" \
  "ds_read_b64_tr_b16 a[214:215], v24 offset:25600\n" \
  "v_mfma_f32_16x16x32_bf16 v[52:55], a[108:111], a[180:183], v[52:55]\n" \
  "ds_read_b64_tr_b16 a[216:217], v25 offset:16384\n" \
  "ds_read_b64_tr_b16 a[218:219], v25 offset:17408\n" \
  "v_mfma_f32_16x16x32_bf16 v[52:55], a[104:107], a[176:179], v[52:55]\n" \
  "ds_read_b64_tr_b16 a[220:221], v25 offset:24576\n" \
  "ds_read_b64_tr_b16 a[222:223], v25 offset:25600\n" \
  "s_waitcnt lgkmcnt(8)\n" \
  "v_mfma_f32_16x16x32_bf16 v[56:59], a[112:115], a[176:179], v[56:59]\n" \
  "ds_read_b64_tr_b16 a[224:225], v26 offset:16384\n" \
  "ds_read_b64_tr_b16 a[226:227], v26 offset:17408\n" \
  "v_mfma_f32_16x16x32_bf16 v[56:59], a[116:119], a[180:183], v[56:59]\n" \
  "ds_read_b64_tr_b16 a[228:229], v26 offset:24576\n" \
  "ds_read_b64_tr_b16 a[230:231], v26 offset:25600\n" \
  "v_mfma_f32_16x16x32_bf16 v[60:63], a[124:127], a[180:183], v[60:63]\n" \
  "ds_read_b64_tr_b16 a[232:233], v27 offset:16384\n" \
  "ds_read_b64_tr_b16 a[234:235], v27 offset:17408\n" \
  "v_mfma_f32_16x16x32_bf16 v[60:63], a[120:123], a[176:179], v[60:63]\n" \
  "ds_read_b64_tr_b16 a[236:237], v27 offset:24576\n" \
  "ds_read_b64_tr_b16 a[238:239], v27 offset:25600\n" \
  "s_waitcnt lgkmcnt(8)\n" \
  "v_mfma_f32_16x16x32_bf16 v[32:35], a[208:211], a[184:187], v[32:35]\n" \
  "ds_read_b64_tr_b16 a[96:97], v28 offset:16384\n" \
  "ds_read_b64_tr_b16 a[98:99], v28 offset:17408\n" \
  "v_mfma_f32_16x16x32_bf16 v[32:35], a[212:215], a[188:191], v[32:35]\n" \
  "ds_read_b64_tr_b16 a[100:101], v28 offset:24576\n" \
  "ds_read_b64_tr_b16 a[102:103], v28 offset:25600\n" \
  "v_mfma_f32_16x16x32_bf16 v[36:39], a[220:223], a[188:191], v[36:39]\n" \
  "ds_read_b64_tr_b16 a[104:105], v29 offset:16384\n" \
  "ds_read_b64_tr_b16 a[106:107], v29 offset:17408\n" \
  "v_mfma_f32_16x16x32_bf16 v[36:39], a[216:219], a[184:187], v[36:39]\n" \
  "ds_read_b64_tr_b16 a[108:109], v29 offset:24576\n" \
  "ds_read_b64_tr_b16 a[110:111], v29 offset:25600\n" \
  "s_waitcnt lgkmcnt(8)\n" \
  "v_mfma_f32_16x16x32_bf16 v[40:43], a[224:227], a[184:187], v[40:43]\n" \
  "ds_read_b64_tr_b16 a[112:113], v30 offset:16384\n" \
  "ds_read_b64_tr_b16 a[114:115], v30 offset:17408\n" \
  "v_mfma_f32_16x16x32_bf16 v[40:43], a[228:231], a[188:191], v[40:43]\n" \
  "ds_read_b64_tr_b16 a[116:117], v30 offset:24576\n" \
  "ds_read_b64_tr_b16 a[118:119], v30 offset:25600\n" \
  "v_mfma_f32_16x16x32_bf16 v[44:47], a[236:239], a[188:191], v[44:47]\n" \
  "ds_read_b64_tr_b16 a[120:121], v31 offset:16384\n" \
  "ds_read_b64_tr_b16 a[122:123], v31 offset:17408\n" \
  "v_mfma_f32_16x16x32_bf16 v[44:47], a[232:235], a[184:187], v[44:47]\n" \
  "ds_read_b64_tr_b16 a[124:125], v31 offset:24576\n" \
  "ds_read_b64_tr_b16 a[126:127], v31 offset:25600\n" \
  "s_waitcnt lgkmcnt(8)\n" \
  "v_mfma_f32_16x16x32_bf16 v[48:51], a[96:99], a[184:187], v[48:51]\n" \
  "ds_read_b64_tr_b16 a[208:209], v24 offset:32768\n" \
  "ds_read_b64_tr_b16 a[210:211], v24 offset:33792\n" \
  "v_mfma_f32_16x16x32_bf16 v[48:51], a[100:103], a[188:191], v[48:51]\n" \
  "ds_read_b64_tr_b16 a[212:213], v24 offset:40960\n" \
  "ds_read_b64_tr_b16 a[214:215], v24 offset:41984\n" \
  "v_mfma_f32_16x16x32_bf16 v[52:55], a[108:111], a[188:191], v[52:55]\n" \
  "ds_read_b64_tr_b16 a[216:217], v25 offset:32768\n" \
  "ds_read_b64_tr_b16 a[218:219], v25 offset:33792\n" \
  "v_mfma_f32_16x16x32_bf16 v[52:55], a[104:107], a[184:187], v[52:55]\n" \
  "ds_read_b64_tr_b16 a[220:221], v25 offset:40960\n" \
  "ds_read_b64_tr_b16 a[222:223], v25 offset:41984\n" \
  "s_waitcnt lgkmcnt(8)\n" \
  "v_mfma_f32_16x16x32_bf16 v[56:59], a[112:115], a[184:187], v[56:59]\n" \
  "ds_read_b64_tr_b16 a[224:225], v26 offset:32768\n" \
  "ds_read_b64_tr_b16 a[226:227], v26 offset:33792\n" \
  "v_mfma_f32_16x16x32_bf16 v[56:59], a[116:119], a[188:191], v[56:59]\n" \
  "ds_read_b64_tr_b16 a[228:229], v26 offset:40960\n" \
  "ds_read_b64_tr_b16 a[230:231], v26 offset:41984\n" \
  "v_mfma_f32_16x16x32_bf16 v[60:63], a[124:127], a[188:191], v[60:63]\n" \
  "ds_read_b64_tr_b16 a[232:233], v27 offset:32768\n" \
  "ds_read_b64_tr_b16 a[234:235], v27 offset:33792\n" \
  "v_mfma_f32_16x16x32_bf16 v[60:63], a[120:123], a[184:187], v[60:63]\n" \
  "ds_read_b64_tr_b16 a[236:237], v27 offset:40960\n" \
  "ds_read_b64_tr_b16 a[238:239], v27 offset:41984\n" \
  "s_waitcnt vmcnt(0) lgkmcnt(8)\n" \
  "v_mfma_f32_16x16x32_bf16 v[32:35], a[208:211], a[192:195], v[32:35]\n" \
  "ds_read_b64_tr_b16 a[96:97], v28 offset:32768\n" \
  "ds_read_b64_tr_b16 a[98:99], v28 offset:33792\n" \
  "v_cmp_eq_u64 vcc, %[tag], %[ft]\n" \
  "v_mfma_f32_16x16x32_bf16 v[32:35], a[212:215], a[196:199], v[32:35]\n" \
  "ds_read_b64_tr_b16 a[100:101], v28 offset:40960\n" \
  "ds_read_b64_tr_b16 a[102:103], v28 offset:41984\n" \
  "v_mfma_f32_16x16x32_bf16 v[36:39], a[220:223], a[196:199], v[36:39]\n" \
  "ds_read_b64_tr_b16 a[104:105], v29 offset:32768\n" \
  "ds_read_b64_tr_b16 a[106:107], v29 offset:33792\n" \
  "s_and_b32 s58, vcc_lo, 7\n" \
  "v_mfma_f32_16x16x32_bf16 v[36:39], a[216:219], a[192:195], v[36:39]\n" \
  "ds_read_b64_tr_b16 a[108:109], v29 offset:40960\n" \
  "ds_read_b64_tr_b16 a[110:111], v29 offset:41984\n" \
  "s_bitcmp1_b32 s58, 0\n" "s_cselect_b32 s62, %[wsz], 0\n" \
  "s_waitcnt lgkmcnt(8)\n" \
  "v_mfma_f32_16x16x32_bf16 v[40:43], a[224:227], a[192:195], v[40:43]\n" \
  "ds_read_b64_tr_b16 a[112:113], v30 offset:32768\n" \
  "ds_read_b64_tr_b16 a[114:115], v30 offset:33792\n" \
  "s_bitcmp1_b32 s58, 1\n" "s_cselect_b32 s70, %[wsz], 0\n" \
  "v_mfma_f32_16x16x32_bf16 v[40:43], a[228:231], a[196:199], v[40:43]\n" \
  "ds_read_b64_tr_b16 a[116:117], v30 offset:40960\n" \
  "ds_read_b64_tr_b16 a[118:119], v30 offset:41984\n" \
  "s_bitcmp1_b32 s58, 2\n" "s_cselect_b32 s74, %[wsz], 0\n" \
  "v_mfma_f32_16x16x32_bf16 v[44:47], a[236:239], a[196:199], v[44:47]\n" \
  "ds_read_b64_tr_b16 a[120:121], v31 offset:32768\n" \
  "ds_read_b64_tr_b16 a[122:123], v31 offset:33792\n" \
  "s_mov_b32 %[got], s58\n" \
  "v_mfma_f32_16x16x32_bf16 v[44:47], a[232:235], a[192:195], v[44:47]\n" \
  "ds_read_b64_tr_b16 a[124:125], v31 offset:40960\n" \
  "ds_read_b64_tr_b16 a[126:127], v31 offset:41984\n" \
  "buffer_load_dwordx4 v[160:163], %[po1], s[60:63], %[ps0] offen offset:0 sc1\n" \
  "s_waitcnt lgkmcnt(8)\n" \
  "v_mfma_f32_16x16x32_bf16 v[48:51], a[96:99], a[192:195], v[48:51]\n" \
  "ds_read_b64_tr_b16 a[208:209], v24 offset:49152\n" \
  "ds_read_b64_tr_b16 a[210:211], v24 offset:50176\n" \
  "buffer_load_dwordx4 v[172:175], %[po1], s[60:63], %[ps0] offen offset:1024 sc1\n" \
  "v_mfma_f32_16x16x32_bf16 v[48:51], a[100:103], a[196:199], v[48:51]\n" \
  "ds_read_b64_tr_b16 a[212:213], v24 offset:57344\n" \
  "ds_read_b64_tr_b16 a[214:215], v24 offset:58368\n" \
  "buffer_load_dwordx4 v[184:187], %[po1], s[60:63], %[ps0] offen offset:2048 sc1\n" \
  "v_mfma_f32_16x16x32_bf16 v[52:55], a[108:111], a[196:199], v[52:55]\n" \
  "ds_read_b64_tr_b16 a[216:217], v25 offset:49152\n" \
  "ds_read_b64_tr_b16 a[218:219], v25 offset:50176\n" \
  "buffer_load_dwordx4 v[196:199], %[po1], s[60:63], %[ps0] offen offset:3072 sc1\n" \
  "v_mfma_f32_16x16x32_bf16 v[52:55], a[104:107], a[192:195], v[52:55]\n" \
  "ds_read_b64_tr_b16 a[220:221], v25 offset:57344\n" \
  "ds_read_b64_tr_b16 a[222:223], v25 offset:58368\n" \
  "buffer_load_dwordx4 v[164:167], %[po1], s[68:71], %[ps1] offen offset:0 sc1\n" \
  "s_waitcnt lgkmcnt(8)\n" \
  "v_mfma_f32_16x16x32_bf16 v[56:59], a[112:115], a[192:195], v[56:59]\n" \
  "ds_read_b64_tr_b16 a[224:225], v26 offset:49152\n" \
  "ds_read_b64_tr_b16 a[226:227], v26 offset:50176\n" \
  "buffer_load_dwordx4 v[176:179], %[po1], s[68:71], %[ps1] offen offset:1024 sc1\n" \
  "v_mfma_f32_16x16x32_bf16 v[56:59], a[116:119], a[196:199], v[56:59]\n" \
  "ds_read_b64_tr_b16 a[228:229], v26 offset:57344\n" \
  "ds_read_b64_tr_b16 a[230:231], v26 offset:58368\n" \
  "buffer_load_dwordx4 v[188:191], %[po1], s[68:71], %[ps1] offen offset:2048 sc1\n" \
  "v_mfma_f32_16x16x32_bf16 v[60:63], a[124:127], a[196:199], v[60:63]\n" \
  "ds_read_b64_tr_b16 a[232:233], v27 offset:49152\n" \
  "ds_read_b64_tr_b16 a[234:235], v27 offset:50176\n" \
  "buffer_load_dwordx4 v[200:203], %[po1], s[68:71], %[ps1] offen offset:3072 sc1\n" \
  "v_mfma_f32_16x16x32_bf16 v[60:63], a[120:123], a[192:195], v[60:63]\n" \
  "ds_read_b64_tr_b16 a[236:237], v27 offset:57344\n" \
  "ds_read_b64_tr_b16 a[238:239], v27 offset:58368\n" \
  "buffer_load_dwordx4 v[168:171], %[po1], s[72:75], %[ps2] offen offset:0 sc1\n" \
  "s_waitcnt lgkmcnt(8)\n" \
  "v_mfma_f32_16x16x32_bf16 v[32:35], a[208:211], a[200:203], v[32:35]\n" \
  "ds_read_b64_tr_b16 a[96:97], v28 offset:49152\n" \
  "ds_read_b64_tr_b16 a[98:99], v28 offset:50176\n" \
  "buffer_load_dwordx4 v[180:183], %[po1], s[72:75], %[ps2] offen offset:1024 sc1\n" \
  "v_mfma_f32_16x16x32_bf16 v[32:35], a[212:215], a[204:207], v[32:35]\n" \
  "ds_read_b64_tr_b16 a[100:101], v28 offset:57344\n" \
  "ds_read_b64_tr_b16 a[102:103], v28 offset:58368\n" \
  "buffer_load_dwordx4 v[192:195], %[po1], s[72:75], %[ps2] offen offset:2048 sc1\n" \
  "v_mfma_f32_16x16x32_bf16 v[36:39], a[220:223], a[204:207], v[36:39]\n" \
  "ds_read_b64_tr_b16 a[104:105], v29 offset:49152\n" \
  "ds_read_b64_tr_b16 a[106:107], v29 offset:50176\n" \
  "buffer_load_dwordx4 v[204:207], %[po1], s[72:75], %[ps2] offen offset:3072 sc1\n" \
  "v_mfma_f32_16x16x32_bf16 v[36:39], a[216:219], a[200:203], v[36:39]\n" \
  "ds_read_b64_tr_b16 a[108:109], v29 offset:57344\n" \
  "ds_read_b64_tr_b16 a[110:111], v29 offset:58368\n" \
  "s_waitcnt lgkmcnt(8)\n" \
  "v_mfma_f32_16x16x32_bf16 v[40:43], a[224:227], a[200:203], v[40:43]\n" \
  "ds_read_b64_tr_b16 a[112:113], v30 offset:49152\n" \
  "ds_read_b64_tr_b16 a[114:115], v30 offset:50176\n" \
  "v_mfma_f32_16x16x32_bf16 v[40:43], a[228:231], a[204:207], v[40:43]\n" \
  "ds_read_b64_tr_b16 a[116:117], v30 offset:57344\n" \
  "ds_read_b64_tr_b16 a[118:119], v30 offset:58368\n" \
  "v_mfma_f32_16x16x32_bf16 v[44:47], a[236:239], a[204:207], v[44:47]\n" \
  "ds_read_b64_tr_b16 a[120:121], v31 offset:49152\n" \
  "ds_read_b64_tr_b16 a[122:123], v31 offset:50176\n" \
  "v_mfma_f32_16x16x32_bf16 v[44:47], a[232:235], a[200:203], v[44:47]\n" \
  "ds_read_b64_tr_b16 a[124:125], v31 offset:57344\n" \
  "ds_read_b64_tr_b16 a[126:127], v31 offset:58368\n" \
  "s_waitcnt lgkmcnt(8)\n" \
  "v_mfma_f32_16x16x32_bf16 v[48:51], a[96:99], a[200:203], v[48:51]\n" \
  "v_mfma_f32_16x16x32_bf16 v[48:51], a[100:103], a[204:207], v[48:51]\n" \
  "v_mfma_f32_16x16x32_bf16 v[52:55], a[108:111], a[204:207], v[52:55]\n" \
  "v_mfma_f32_16x16x32_bf16 v[52:55], a[104:107], a[200:203], v[52:55]\n" \
  "s_waitcnt lgkmcnt(0)\n" \
  "v_mfma_f32_16x16x32_bf16 v[56:59], a[112:115], a[200:203], v[56:59]\n" \
  "v_mfma_f32_16x16x32_bf16 v[56:59], a[116:119], a[204:207], v[56:59]\n" \
  "v_mfma_f32_16x16x32_bf16 v[60:63], a[124:127], a[204:207], v[60:63]\n" \
  "v_mfma_f32_16x16x32_bf16 v[60:63], a[120:123], a[200:203], v[60:63]\n" \
  "s_nop 7\n"
#define G2_CLOB "v24","v25","v26","v27","v28","v29","v30","v31","a0","a1","a2","a3","a4","a5","a6","a7","a8","a9","a10","a11","a12","a13","a14","a15","a16","a17","a18","a19","a20","a21","a22","a23","a24","a25","a26","a27","a28","a29","a30","a31","a32","a33","a34","a35","a36","a37","a38","a39","a40","a41","a42","a43","a44","a45","a46","a47","a48","a49","a50","a51","a52","a53","a54","a55","a56","a57","a58","a59","a60","a61","a62","a63","a64","a65","a66","a67","a68","a69","a70","a71","a72","a73","a74","a75","a76","a77","a78","a79","a80","a81","a82","a83","a84","a85","a86","a87","a88","a89","a90","a91","a92","a93","a94","a95","a96","a97","a98","a99","a100","a101","a102","a103","a104","a105","a106","a107","a108","a109","a110","a111","a112","a113","a114","a115","a116","a117","a118","a119","a120","a121","a122","a123","a124","a125","a126","a127","a128","a129","a130","a131","a132","a133","a134","a135","a136","a137","a138","a139","a140","a141","a142","a143","a144","a145","a146","a147","a148","a149","a150","a151","a152","a153","a154","a155","a156","a157","a158","a159","a160","a161","a162","a163","a164","a165","a166","a167","a168","a169","a170","a171","a172","a173","a174","a175","a176","a177","a178","a179","a180","a181","a182","a183","a184","a185","a186","a187","a188","a189","a190","a191","a192","a193","a194","a195","a196","a197","a198","a199","a200","a201","a202","a203","a204","a205","a206","a207","a208","a209","a210","a211","a212","a213","a214","a215","a216","a217","a218","a219","a220","a221","a222","a223","a224","a225","a226","a227","a228","a229","a230","a231","a232","a233","a234","a235","a236","a237","a238","a239","vcc","s40","s41","s42","s43","s44","s45","s46","s47","s48","s49","s50","s51","s52","s53","s54","s55","s56","s57","s58","s59","s60","s61","s62","s63","s64","s65","s66","s67","s68","s69","s70","s71","s72","s73","s74","s75","s76","s77","scc","memory"
#define EPI_ASM \
  "s_mov_b32 s60, 0xbfb8aa3b\n" \
  "s_mov_b32 s61, 0xbfb8aa3b\n" \
  "v_pk_mul_f32 v[36:37], v[32:33], v[36:37]\n" \
  "v_pk_mul_f32 v[38:39], v[34:35], v[38:39]\n" \
  "v_pk_mul_f32 v[44:45], v[40:41], v[44:45]\n" \
  "v_pk_mul_f32 v[46:47], v[42:43], v[46:47]\n" \
  "v_pk_mul_f32 v[32:33], v[32:33], s[60:61]\n" \
  "v_pk_mul_f32 v[34:35], v[34:35], s[60:61]\n" \
  "v_pk_mul_f32 v[40:41], v[40:41], s[60:61]\n" \
  "v_pk_mul_f32 v[42:43], v[42:43], s[60:61]\n" \
  "s_add_u32 m0, %[ldsw], 0\n" \
  "s_mov_b32 s62, 0\n" \
  "buffer_load_dwordx4 %[wv0], %[srd2], s62 offen lds\n" \
  "v_exp_f32 v32, v32\n" \
  "v_exp_f32 v33, v33\n" \
  "v_exp_f32 v34, v34\n" \
  "v_exp_f32 v35, v35\n" \
  "v_exp_f32 v40, v40\n" \
  "v_exp_f32 v41, v41\n" \
  "v_exp_f32 v42, v42\n" \
  "v_exp_f32 v43, v43\n" \
  "v_pk_add_f32 v[32:33], v[32:33], 1.0 op_sel_hi:[1,0]\n" \
  "v_pk_add_f32 v[34:35], v[34:35], 1.0 op_sel_hi:[1,0]\n" \
  "v_pk_add_f32 v[40:41], v[40:41], 1.0 op_sel_hi:[1,0]\n" \
  "v_pk_add_f32 v[42:43], v[42:43], 1.0 op_sel_hi:[1,0]\n" \
  "s_add_u32 m0, %[ldsw], 1024\n" \
  "s_mov_b32 s62, 8192\n" \
  "buffer_load_dwordx4 %[wv0], %[srd2], s62 offen lds\n" \
  "v_rcp_f32 v32, v32\n" \
  "v_rcp_f32 v33, v33\n" \
  "v_rcp_f32 v34, v34\n" \
  "v_rcp_f32 v35, v35\n" \
  "v_rcp_f32 v40, v40\n" \
  "v_rcp_f32 v41, v41\n" \
  "v_rcp_f32 v42, v42\n" \
  "v_rcp_f32 v43, v43\n" \
  "v_pk_mul_f32 v[36:37], v[36:37], v[32:33]\n" \
  "v_pk_mul_f32 v[38:39], v[38:39], v[34:35]\n" \
  "v_pk_mul_f32 v[44:45], v[44:45], v[40:41]\n" \
  "v_pk_mul_f32 v[46:47], v[46:47], v[42:43]\n" \
  "v_cvt_pk_bf16_f32 v32, v36, v37\n" \
  "v_cvt_pk_bf16_f32 v34, v44, v45\n" \
  "v_cvt_pk_bf16_f32 v33, v38, v39\n" \
  "v_cvt_pk_bf16_f32 v35, v46, v47\n" \
  "v_pk_mul_f32 v[52:53], v[48:49], v[52:53]\n" \
  "v_pk_mul_f32 v[54:55], v[50:51], v[54:55]\n" \
  "v_pk_mul_f32 v[60:61], v[56:57], v[60:61]\n" \
  "v_pk_mul_f32 v[62:63], v[58:59], v[62:63]\n" \
  "v_permlane16_swap_b32 v32, v34\n" \
  "v_permlane16_swap_b32 v33, v35\n" \
  "buffer_store_dwordx4 v[32:35], %[o01], %[srdw], 0 offen offset:0 sc1\n" \
  "v_pk_mul_f32 v[48:49], v[48:49], s[60:61]\n" \
  "v_pk_mul_f32 v[50:51], v[50:51], s[60:61]\n" \
  "v_pk_mul_f32 v[56:57], v[56:57], s[60:61]\n" \
  "v_pk_mul_f32 v[58:59], v[58:59], s[60:61]\n" \
  "s_add_u32 m0, %[ldsw], 2048\n" \
  "s_mov_b32 s62, 16384\n" \
  "buffer_load_dwordx4 %[wv1], %[srd2], s62 offen lds\n" \
  "v_exp_f32 v48, v48\n" \
  "v_exp_f32 v49, v49\n" \
  "v_exp_f32 v50, v50\n" \
  "v_exp_f32 v51, v51\n" \
  "v_exp_f32 v56, v56\n" \
  "v_exp_f32 v57, v57\n" \
  "v_exp_f32 v58, v58\n" \
  "v_exp_f32 v59, v59\n" \
  "v_pk_add_f32 v[48:49], v[48:49], 1.0 op_sel_hi:[1,0]\n" \
  "v_pk_add_f32 v[50:51], v[50:51], 1.0 op_sel_hi:[1,0]\n" \
  "v_pk_add_f32 v[56:57], v[56:57], 1.0 op_sel_hi:[1,0]\n" \
  "v_pk_add_f32 v[58:59], v[58:59], 1.0 op_sel_hi:[1,0]\n" \
  "s_add_u32 m0, %[ldsw], 3072\n" \
  "s_mov_b32 s62, 24576\n" \
  "buffer_load_dwordx4 %[wv1], %[srd2], s62 offen lds\n" \
  "v_rcp_f32 v48, v48\n" \
  "v_rcp_f32 v49, v49\n" \
  "v_rcp_f32 v50, v50\n" \
  "v_rcp_f32 v51, v51\n" \
  "v_rcp_f32 v56, v56\n" \
  "v_rcp_f32 v57, v57\n" \
  "v_rcp_f32 v58, v58\n" \
  "v_rcp_f32 v59, v59\n" \
  "v_pk_mul_f32 v[52:53], v[52:53], v[48:49]\n" \
  "v_pk_mul_f32 v[54:55], v[54:55], v[50:51]\n" \
  "v_pk_mul_f32 v[60:61], v[60:61], v[56:57]\n" \
  "v_pk_mul_f32 v[62:63], v[62:63], v[58:59]\n" \
  "v_cvt_pk_bf16_f32 v48, v52, v53\n" \
  "v_cvt_pk_bf16_f32 v50, v60, v61\n" \
  "v_cvt_pk_bf16_f32 v49, v54, v55\n" \
  "v_cvt_pk_bf16_f32 v51, v62, v63\n" \
  "v_pk_mul_f32 v[92:93], v[88:89], v[92:93]\n" \
  "v_pk_mul_f32 v[94:95], v[90:91], v[94:95]\n" \
  "v_pk_mul_f32 v[100:101], v[96:97], v[100:101]\n" \
  "v_pk_mul_f32 v[102:103], v[98:99], v[102:103]\n" \
  "v_permlane16_swap_b32 v48, v50\n" \
  "v_permlane16_swap_b32 v49, v51\n" \
  "buffer_store_dwordx4 v[48:51], %[o01], %[srdw], 0 offen offset:1024 sc1\n" \
  "v_add_u32 %[o01], 114688, %[o01]\n" \
  "v_pk_mul_f32 v[88:89], v[88:89], s[60:61]\n" \
  "v_pk_mul_f32 v[90:91], v[90:91], s[60:61]\n" \
  "v_pk_mul_f32 v[96:97], v[96:97], s[60:61]\n" \
  "v_pk_mul_f32 v[98:99], v[98:99], s[60:61]\n" \
  "s_add_u32 m0, %[ldsw], 16384\n" \
  "s_mov_b32 s62, 229376\n" \
  "buffer_load_dwordx4 %[wv0], %[srd2], s62 offen lds\n" \
  "v_exp_f32 v88, v88\n" \
  "v_exp_f32 v89, v89\n" \
  "v_exp_f32 v90, v90\n" \
  "v_exp_f32 v91, v91\n" \
  "v_exp_f32 v96, v96\n" \
  "v_exp_f32 v97, v97\n" \
  "v_exp_f32 v98, v98\n" \
  "v_exp_f32 v99, v99\n" \
  "v_pk_add_f32 v[88:89], v[88:89], 1.0 op_sel_hi:[1,0]\n" \
  "v_pk_add_f32 v[90:91], v[90:91], 1.0 op_sel_hi:[1,0]\n" \
  "v_pk_add_f32 v[96:97], v[96:97], 1.0 op_sel_hi:[1,0]\n" \
  "v_pk_add_f32 v[98:99], v[98:99], 1.0 op_sel_hi:[1,0]\n" \
  "s_add_u32 m0, %[ldsw], 17408\n" \
  "s_mov_b32 s62, 237568\n" \
  "buffer_load_dwordx4 %[wv0], %[srd2], s62 offen lds\n" \
  "v_rcp_f32 v88, v88\n" \
  "v_rcp_f32 v89, v89\n" \
  "v_rcp_f32 v90, v90\n" \
  "v_rcp_f32 v91, v91\n" \
  "v_rcp_f32 v96, v96\n" \
  "v_rcp_f32 v97, v97\n" \
  "v_rcp_f32 v98, v98\n" \
  "v_rcp_f32 v99, v99\n" \
  "v_pk_mul_f32 v[92:93], v[92:93], v[88:89]\n" \
  "v_pk_mul_f32 v[94:95], v[94:95], v[90:91]\n" \
  "v_pk_mul_f32 v[100:101], v[100:101], v[96:97]\n" \
  "v_pk_mul_f32 v[102:103], v[102:103], v[98:99]\n" \
  "v_cvt_pk_bf16_f32 v88, v92, v93\n" \
  "v_cvt_pk_bf16_f32 v90, v100, v101\n" \
  "v_cvt_pk_bf16_f32 v89, v94, v95\n" \
  "v_cvt_pk_bf16_f32 v91, v102, v103\n" \
  "v_pk_mul_f32 v[108:109], v[104:105], v[108:109]\n" \
  "v_pk_mul_f32 v[110:111], v[106:107], v[110:111]\n" \
  "v_pk_mul_f32 v[116:117], v[112:113], v[116:117]\n" \
  "v_pk_mul_f32 v[118:119], v[114:115], v[118:119]\n" \
  "v_permlane16_swap_b32 v88, v90\n" \
  "v_permlane16_swap_b32 v89, v91\n" \
  "buffer_store_dwordx4 v[88:91], %[o01], %[srdw], 0 offen offset:0 sc1\n" \
  "v_pk_mul_f32 v[104:105], v[104:105], s[60:61]\n" \
  "v_pk_mul_f32 v[106:107], v[106:107], s[60:61]\n" \
  "v_pk_mul_f32 v[112:113], v[112:113], s[60:61]\n" \
  "v_pk_mul_f32 v[114:115], v[114:115], s[60:61]\n" \
  "s_add_u32 m0, %[ldsw], 18432\n" \
  "s_mov_b32 s62, 245760\n" \
  "buffer_load_dwordx4 %[wv1], %[srd2], s62 offen lds\n" \
  "v_exp_f32 v104, v104\n" \
  "v_exp_f32 v105, v105\n" \
  "v_exp_f32 v106, v106\n" \
  "v_exp_f32 v107, v107\n" \
  "v_exp_f32 v112, v112\n" \
  "v_exp_f32 v113, v113\n" \
  "v_exp_f32 v114, v114\n" \
  "v_exp_f32 v115, v115\n" \
  "v_pk_add_f32 v[104:105], v[104:105], 1.0 op_sel_hi:[1,0]\n" \
  "v_pk_add_f32 v[106:107], v[106:107], 1.0 op_sel_hi:[1,0]\n" \
  "v_pk_add_f32 v[112:113], v[112:113], 1.0 op_sel_hi:[1,0]\n" \
  "v_pk_add_f32 v[114:115], v[114:115], 1.0 op_sel_hi:[1,0]\n" \
  "s_add_u32 m0, %[ldsw], 19456\n" \
  "s_mov_b32 s62, 253952\n" \
  "buffer_load_dwordx4 %[wv1], %[srd2], s62 offen lds\n" \
  "v_rcp_f32 v104, v104\n" \
  "v_rcp_f32 v105, v105\n" \
  "v_rcp_f32 v106, v106\n" \
  "v_rcp_f32 v107, v107\n" \
  "v_rcp_f32 v112, v112\n" \
  "v_rcp_f32 v113, v113\n" \
  "v_rcp_f32 v114, v114\n" \
  "v_rcp_f32 v115, v115\n" \
  "v_pk_mul_f32 v[108:109], v[108:109], v[104:105]\n" \
  "v_pk_mul_f32 v[110:111], v[110:111], v[106:107]\n" \
  "v_pk_mul_f32 v[116:117], v[116:117], v[112:113]\n" \
  "v_pk_mul_f32 v[118:119], v[118:119], v[114:115]\n" \
  "v_cvt_pk_bf16_f32 v104, v108, v109\n" \
  "v_cvt_pk_bf16_f32 v106, v116, v117\n" \
  "v_cvt_pk_bf16_f32 v105, v110, v111\n" \
  "v_cvt_pk_bf16_f32 v107, v118, v119\n" \
  "v_pk_mul_f32 v[148:149], v[144:145], v[148:149]\n" \
  "v_pk_mul_f32 v[150:151], v[146:147], v[150:151]\n" \
  "v_pk_mul_f32 v[156:157], v[152:153], v[156:157]\n" \
  "v_pk_mul_f32 v[158:159], v[154:155], v[158:159]\n" \
  "v_permlane16_swap_b32 v104, v106\n" \
  "v_permlane16_swap_b32 v105, v107\n" \
  "buffer_store_dwordx4 v[104:107], %[o01], %[srdw], 0 offen offset:1024 sc1\n" \
  "v_add_u32 %[o01], 114688, %[o01]\n" \
  "v_pk_mul_f32 v[144:145], v[144:145], s[60:61]\n" \
  "v_pk_mul_f32 v[146:147], v[146:147], s[60:61]\n" \
  "v_pk_mul_f32 v[152:153], v[152:153], s[60:61]\n" \
  "v_pk_mul_f32 v[154:155], v[154:155], s[60:61]\n" \
  "s_add_u32 m0, %[ldsw], 32768\n" \
  "s_mov_b32 s62, 458752\n" \
  "buffer_load_dwordx4 %[wv0], %[srd2], s62 offen lds\n" \
  "v_exp_f32 v144, v144\n" \
  "v_exp_f32 v145, v145\n" \
  "v_exp_f32 v146, v146\n" \
  "v_exp_f32 v147, v147\n" \
  "v_exp_f32 v152, v152\n" \
  "v_exp_f32 v153, v153\n" \
  "v_exp_f32 v154, v154\n" \
  "v_exp_f32 v155, v155\n" \
  "v_pk_add_f32 v[144:145], v[144:145], 1.0 op_sel_hi:[1,0]\n" \
  "v_pk_add_f32 v[146:147], v[146:147], 1.0 op_sel_hi:[1,0]\n" \
  "v_pk_add_f32 v[152:153], v[152:153], 1.0 op_sel_hi:[1,0]\n" \
  "v_pk_add_f32 v[154:155], v[154:155], 1.0 op_sel_hi:[1,0]\n" \
  "s_add_u32 m0, %[ldsw], 33792\n" \
  "s_mov_b32 s62, 466944\n" \
  "buffer_load_dwordx4 %[wv0], %[srd2], s62 offen lds\n" \
  "v_rcp_f32 v144, v144\n" \
  "v_rcp_f32 v145, v145\n" \
  "v_rcp_f32 v146, v146\n" \
  "v_rcp_f32 v147, v147\n" \
  "v_rcp_f32 v152, v152\n" \
  "v_rcp_f32 v153, v153\n" \
  "v_rcp_f32 v154, v154\n" \
  "v_rcp_f32 v155, v155\n" \
  "v_pk_mul_f32 v[148:149], v[148:149], v[144:145]\n" \
  "v_pk_mul_f32 v[150:151], v[150:151], v[146:147]\n" \
  "v_pk_mul_f32 v[156:157], v[156:157], v[152:153]\n" \
  "v_pk_mul_f32 v[158:159], v[158:159], v[154:155]\n" \
  "v_cvt_pk_bf16_f32 v144, v148, v149\n" \
  "v_cvt_pk_bf16_f32 v146, v156, v157\n" \
  "v_cvt_pk_bf16_f32 v145, v150, v151\n" \
  "v_cvt_pk_bf16_f32 v147, v158, v159\n" \
  "v_pk_mul_f32 v[164:165], v[160:161], v[164:165]\n" \
  "v_pk_mul_f32 v[166:167], v[162:163], v[166:167]\n" \
  "v_pk_mul_f32 v[172:173], v[168:169], v[172:173]\n" \
  "v_pk_mul_f32 v[174:175], v[170:171], v[174:175]\n" \
  "v_permlane16_swap_b32 v144, v146\n" \
  "v_permlane16_swap_b32 v145, v147\n" \
  "buffer_store_dwordx4 v[144:147], %[o01], %[srdw], 0 offen offset:0 sc1\n" \
  "v_pk_mul_f32 v[160:161], v[160:161], s[60:61]\n" \
  "v_pk_mul_f32 v[162:163], v[162:163], s[60:61]\n" \
  "v_pk_mul_f32 v[168:169], v[168:169], s[60:61]\n" \
  "v_pk_mul_f32 v[170:171], v[170:171], s[60:61]\n" \
  "s_add_u32 m0, %[ldsw], 34816\n" \
  "s_mov_b32 s62, 475136\n" \
  "buffer_load_dwordx4 %[wv1], %[srd2], s62 offen lds\n" \
  "v_exp_f32 v160, v160\n" \
  "v_exp_f32 v161, v161\n" \
  "v_exp_f32 v162, v162\n" \
  "v_exp_f32 v163, v163\n" \
  "v_exp_f32 v168, v168\n" \
  "v_exp_f32 v169, v169\n" \
  "v_exp_f32 v170, v170\n" \
  "v_exp_f32 v171, v171\n" \
  "v_pk_add_f32 v[160:161], v[160:161], 1.0 op_sel_hi:[1,0]\n" \
  "v_pk_add_f32 v[162:163], v[162:163], 1.0 op_sel_hi:[1,0]\n" \
  "v_pk_add_f32 v[168:169], v[168:169], 1.0 op_sel_hi:[1,0]\n" \
  "v_pk_add_f32 v[170:171], v[170:171], 1.0 op_sel_hi:[1,0]\n" \
  "s_add_u32 m0, %[ldsw], 35840\n" \
  "s_mov_b32 s62, 483328\n" \
  "buffer_load_dwordx4 %[wv1], %[srd2], s62 offen lds\n" \
  "v_rcp_f32 v160, v160\n" \
  "v_rcp_f32 v161, v161\n" \
  "v_rcp_f32 v162, v162\n" \
  "v_rcp_f32 v163, v163\n" \
  "v_rcp_f32 v168, v168\n" \
  "v_rcp_f32 v169, v169\n" \
  "v_rcp_f32 v170, v170\n" \
  "v_rcp_f32 v171, v171\n" \
  "v_pk_mul_f32 v[164:165], v[164:165], v[160:161]\n" \
  "v_pk_mul_f32 v[166:167], v[166:167], v[162:163]\n" \
  "v_pk_mul_f32 v[172:173], v[172:173], v[168:169]\n" \
  "v_pk_mul_f32 v[174:175], v[174:175], v[170:171]\n" \
  "v_cvt_pk_bf16_f32 v160, v164, v165\n" \
  "v_cvt_pk_bf16_f32 v162, v172, v173\n" \
  "v_cvt_pk_bf16_f32 v161, v166, v167\n" \
  "v_cvt_pk_bf16_f32 v163, v174, v175\n" \
  "v_pk_mul_f32 v[204:205], v[200:201], v[204:205]\n" \
  "v_pk_mul_f32 v[206:207], v[202:203], v[206:207]\n" \
  "v_pk_mul_f32 v[212:213], v[208:209], v[212:213]\n" \
  "v_pk_mul_f32 v[214:215], v[210:211], v[214:215]\n" \
  "v_permlane16_swap_b32 v160, v162\n" \
  "v_permlane16_swap_b32 v161, v163\n" \
  "buffer_store_dwordx4 v[160:163], %[o01], %[srdw], 0 offen offset:1024 sc1\n" \
  "v_add_u32 %[o01], 114688, %[o01]\n" \
  "v_pk_mul_f32 v[200:201], v[200:201], s[60:61]\n" \
  "v_pk_mul_f32 v[202:203], v[202:203], s[60:61]\n" \
  "v_pk_mul_f32 v[208:209], v[208:209], s[60:61]\n" \
  "v_pk_mul_f32 v[210:211], v[210:211], s[60:61]\n" \
  "v_exp_f32 v200, v200\n" \
  "v_exp_f32 v201, v201\n" \
  "v_exp_f32 v202, v202\n" \
  "v_exp_f32 v203, v203\n" \
  "v_exp_f32 v208, v208\n" \
  "v_exp_f32 v209, v209\n" \
  "v_exp_f32 v210, v210\n" \
  "v_exp_f32 v211, v211\n" \
  "v_pk_add_f32 v[200:201], v[200:201], 1.0 op_sel_hi:[1,0]\n" \
  "v_pk_add_f32 v[202:203], v[202:203], 1.0 op_sel_hi:[1,0]\n" \
  "v_pk_add_f32 v[208:209], v[208:209], 1.0 op_sel_hi:[1,0]\n" \
  "v_pk_add_f32 v[210:211], v[210:211], 1.0 op_sel_hi:[1,0]\n" \
  "v_rcp_f32 v200, v200\n" \
  "v_rcp_f32 v201, v201\n" \
  "v_rcp_f32 v202, v202\n" \
  "v_rcp_f32 v203, v203\n" \
  "v_rcp_f32 v208, v208\n" \
  "v_rcp_f32 v209, v209\n" \
  "v_rcp_f32 v210, v210\n" \
  "v_rcp_f32 v211, v211\n" \
  "v_pk_mul_f32 v[204:205], v[204:205], v[200:201]\n" \
  "v_pk_mul_f32 v[206:207], v[206:207], v[202:203]\n" \
  "v_pk_mul_f32 v[212:213], v[212:213], v[208:209]\n" \
  "v_pk_mul_f32 v[214:215], v[214:215], v[210:211]\n" \
  "v_cvt_pk_bf16_f32 v200, v204, v205\n" \
  "v_cvt_pk_bf16_f32 v202, v212, v213\n" \
  "v_cvt_pk_bf16_f32 v201, v206, v207\n" \
  "v_cvt_pk_bf16_f32 v203, v214, v215\n" \
  "v_pk_mul_f32 v[220:221], v[216:217], v[220:221]\n" \
  "v_pk_mul_f32 v[222:223], v[218:219], v[222:223]\n" \
  "v_pk_mul_f32 v[228:229], v[224:225], v[228:229]\n" \
  "v_pk_mul_f32 v[230:231], v[226:227], v[230:231]\n" \
  "v_permlane16_swap_b32 v200, v202\n" \
  "v_permlane16_swap_b32 v201, v203\n" \
  "buffer_store_dwordx4 v[200:203], %[o01], %[srdw], 0 offen offset:0 sc1\n" \
  "v_pk_mul_f32 v[216:217], v[216:217], s[60:61]\n" \
  "v_pk_mul_f32 v[218:219], v[218:219], s[60:61]\n" \
  "v_pk_mul_f32 v[224:225], v[224:225], s[60:61]\n" \
  "v_pk_mul_f32 v[226:227], v[226:227], s[60:61]\n" \
  "v_exp_f32 v216, v216\n" \
  "v_exp_f32 v217, v217\n" \
  "v_exp_f32 v218, v218\n" \
  "v_exp_f32 v219, v219\n" \
  "v_exp_f32 v224, v224\n" \
  "v_exp_f32 v225, v225\n" \
  "v_exp_f32 v226, v226\n" \
  "v_exp_f32 v227, v227\n" \
  "v_pk_add_f32 v[216:217], v[216:217], 1.0 op_sel_hi:[1,0]\n" \
  "v_pk_add_f32 v[218:219], v[218:219], 1.0 op_sel_hi:[1,0]\n" \
  "v_pk_add_f32 v[224:225], v[224:225], 1.0 op_sel_hi:[1,0]\n" \
  "v_pk_add_f32 v[226:227], v[226:227], 1.0 op_sel_hi:[1,0]\n" \
  "v_rcp_f32 v216, v216\n" \
  "v_rcp_f32 v217, v217\n" \
  "v_rcp_f32 v218, v218\n" \
  "v_rcp_f32 v219, v219\n" \
  "v_rcp_f32 v224, v224\n" \
  "v_rcp_f32 v225, v225\n" \
  "v_rcp_f32 v226, v226\n" \
  "v_rcp_f32 v227, v227\n" \
  "v_pk_mul_f32 v[220:221], v[220:221], v[216:217]\n" \
  "v_pk_mul_f32 v[222:223], v[222:223], v[218:219]\n" \
  "v_pk_mul_f32 v[228:229], v[228:229], v[224:225]\n" \
  "v_pk_mul_f32 v[230:231], v[230:231], v[226:227]\n" \
  "v_cvt_pk_bf16_f32 v216, v220, v221\n" \
  "v_cvt_pk_bf16_f32 v218, v228, v229\n" \
  "v_cvt_pk_bf16_f32 v217, v222, v223\n" \
  "v_cvt_pk_bf16_f32 v219, v230, v231\n" \
  "v_pk_mul_f32 v[68:69], v[64:65], v[68:69]\n" \
  "v_pk_mul_f32 v[70:71], v[66:67], v[70:71]\n" \
  "v_pk_mul_f32 v[76:77], v[72:73], v[76:77]\n" \
  "v_pk_mul_f32 v[78:79], v[74:75], v[78:79]\n" \
  "v_permlane16_swap_b32 v216, v218\n" \
  "v_permlane16_swap_b32 v217, v219\n" \
  "buffer_store_dwordx4 v[216:219], %[o01], %[srdw], 0 offen offset:1024 sc1\n" \
  "v_pk_mul_f32 v[64:65], v[64:65], s[60:61]\n" \
  "v_pk_mul_f32 v[66:67], v[66:67], s[60:61]\n" \
  "v_pk_mul_f32 v[72:73], v[72:73], s[60:61]\n" \
  "v_pk_mul_f32 v[74:75], v[74:75], s[60:61]\n" \
  "v_exp_f32 v64, v64\n" \
  "v_exp_f32 v65, v65\n" \
  "v_exp_f32 v66, v66\n" \
  "v_exp_f32 v67, v67\n" \
  "v_exp_f32 v72, v72\n" \
  "v_exp_f32 v73, v73\n" \
  "v_exp_f32 v74, v74\n" \
  "v_exp_f32 v75, v75\n" \
  "v_pk_add_f32 v[64:65], v[64:65], 1.0 op_sel_hi:[1,0]\n" \
  "v_pk_add_f32 v[66:67], v[66:67], 1.0 op_sel_hi:[1,0]\n" \
  "v_pk_add_f32 v[72:73], v[72:73], 1.0 op_sel_hi:[1,0]\n" \
  "v_pk_add_f32 v[74:75], v[74:75], 1.0 op_sel_hi:[1,0]\n" \
  "v_rcp_f32 v64, v64\n" \
  "v_rcp_f32 v65, v65\n" \
  "v_rcp_f32 v66, v66\n" \
  "v_rcp_f32 v67, v67\n" \
  "v_rcp_f32 v72, v72\n" \
  "v_rcp_f32 v73, v73\n" \
  "v_rcp_f32 v74, v74\n" \
  "v_rcp_f32 v75, v75\n" \
  "v_pk_mul_f32 v[68:69], v[68:69], v[64:65]\n" \
  "v_pk_mul_f32 v[70:71], v[70:71], v[66:67]\n" \
  "v_pk_mul_f32 v[76:77], v[76:77], v[72:73]\n" \
  "v_pk_mul_f32 v[78:79], v[78:79], v[74:75]\n" \
  "v_cvt_pk_bf16_f32 v64, v68, v69\n" \
  "v_cvt_pk_bf16_f32 v66, v76, v77\n" \
  "v_cvt_pk_bf16_f32 v65, v70, v71\n" \
  "v_cvt_pk_bf16_f32 v67, v78, v79\n" \
  "v_pk_mul_f32 v[124:125], v[120:121], v[124:125]\n" \
  "v_pk_mul_f32 v[126:127], v[122:123], v[126:127]\n" \
  "v_pk_mul_f32 v[132:133], v[128:129], v[132:133]\n" \
  "v_pk_mul_f32 v[134:135], v[130:131], v[134:135]\n" \
  "v_permlane16_swap_b32 v64, v66\n" \
  "v_permlane16_swap_b32 v65, v67\n" \
  "v_pk_mul_f32 v[120:121], v[120:121], s[60:61]\n" \
  "v_pk_mul_f32 v[122:123], v[122:123], s[60:61]\n" \
  "v_pk_mul_f32 v[128:129], v[128:129], s[60:61]\n" \
  "v_pk_mul_f32 v[130:131], v[130:131], s[60:61]\n" \
  "v_exp_f32 v120, v120\n" \
  "v_exp_f32 v121, v121\n" \
  "v_exp_f32 v122, v122\n" \
  "v_exp_f32 v123, v123\n" \
  "v_exp_f32 v128, v128\n" \
  "v_exp_f32 v129, v129\n" \
  "v_exp_f32 v130, v130\n" \
  "v_exp_f32 v131, v131\n" \
  "v_pk_add_f32 v[120:121], v[120:121], 1.0 op_sel_hi:[1,0]\n" \
  "v_pk_add_f32 v[122:123], v[122:123], 1.0 op_sel_hi:[1,0]\n" \
  "v_pk_add_f32 v[128:129], v[128:129], 1.0 op_sel_hi:[1,0]\n" \
  "v_pk_add_f32 v[130:131], v[130:131], 1.0 op_sel_hi:[1,0]\n" \
  "v_rcp_f32 v120, v120\n" \
  "v_rcp_f32 v121, v121\n" \
  "v_rcp_f32 v122, v122\n" \
  "v_rcp_f32 v123, v123\n" \
  "v_rcp_f32 v128, v128\n" \
  "v_rcp_f32 v129, v129\n" \
  "v_rcp_f32 v130, v130\n" \
  "v_rcp_f32 v131, v131\n" \
  "v_pk_mul_f32 v[124:125], v[124:125], v[120:121]\n" \
  "v_pk_mul_f32 v[126:127], v[126:127], v[122:123]\n" \
  "v_pk_mul_f32 v[132:133], v[132:133], v[128:129]\n" \
  "v_pk_mul_f32 v[134:135], v[134:135], v[130:131]\n" \
  "v_cvt_pk_bf16_f32 v120, v124, v125\n" \
  "v_cvt_pk_bf16_f32 v122, v132, v133\n" \
  "v_cvt_pk_bf16_f32 v121, v126, v127\n" \
  "v_cvt_pk_bf16_f32 v123, v134, v135\n" \
  "v_pk_mul_f32 v[180:181], v[176:177], v[180:181]\n" \
  "v_pk_mul_f32 v[182:183], v[178:179], v[182:183]\n" \
  "v_pk_mul_f32 v[188:189], v[184:185], v[188:189]\n" \
  "v_pk_mul_f32 v[190:191], v[186:187], v[190:191]\n" \
  "v_permlane16_swap_b32 v120, v122\n" \
  "v_permlane16_swap_b32 v121, v123\n" \
  "s_waitcnt vmcnt(0)\n" \
  "s_barrier\n" \
  "buffer_store_dwordx2 %[tagv], %[fo4], %[srdw], 0 offen sc1\n" \
  "buffer_store_dwordx4 v[64:67], %[ho0], %[srdw], 0 offen offset:0 sc1\n" \
  "v_add_u32 %[ho0], 114688, %[ho0]\n" \
  "buffer_store_dwordx4 v[120:123], %[ho0], %[srdw], 0 offen offset:0 sc1\n" \
  "v_add_u32 %[ho0], 114688, %[ho0]\n" \
  "v_pk_mul_f32 v[176:177], v[176:177], s[60:61]\n" \
  "v_pk_mul_f32 v[178:179], v[178:179], s[60:61]\n" \
  "v_pk_mul_f32 v[184:185], v[184:185], s[60:61]\n" \
  "v_pk_mul_f32 v[186:187], v[186:187], s[60:61]\n" \
  "v_exp_f32 v176, v176\n" \
  "v_exp_f32 v177, v177\n" \
  "v_exp_f32 v178, v178\n" \
  "v_exp_f32 v179, v179\n" \
  "v_exp_f32 v184, v184\n" \
  "v_exp_f32 v185, v185\n" \
  "v_exp_f32 v186, v186\n" \
  "v_exp_f32 v187, v187\n" \
  "v_pk_add_f32 v[176:177], v[176:177], 1.0 op_sel_hi:[1,0]\n" \
  "v_pk_add_f32 v[178:179], v[178:179], 1.0 op_sel_hi:[1,0]\n" \
  "v_pk_add_f32 v[184:185], v[184:185], 1.0 op_sel_hi:[1,0]\n" \
  "v_pk_add_f32 v[186:187], v[186:187], 1.0 op_sel_hi:[1,0]\n" \
  "v_rcp_f32 v176, v176\n" \
  "v_rcp_f32 v177, v177\n" \
  "v_rcp_f32 v178, v178\n" \
  "v_rcp_f32 v179, v179\n" \
  "v_rcp_f32 v184, v184\n" \
  "v_rcp_f32 v185, v185\n" \
  "v_rcp_f32 v186, v186\n" \
  "v_rcp_f32 v187, v187\n" \
  "v_pk_mul_f32 v[180:181], v[180:181], v[176:177]\n" \
  "v_pk_mul_f32 v[182:183], v[182:183], v[178:179]\n" \
  "v_pk_mul_f32 v[188:189], v[188:189], v[184:185]\n" \
  "v_pk_mul_f32 v[190:191], v[190:191], v[186:187]\n" \
  "v_cvt_pk_bf16_f32 v176, v180, v181\n" \
  "v_cvt_pk_bf16_f32 v178, v188, v189\n" \
  "v_cvt_pk_bf16_f32 v177, v182, v183\n" \
  "v_cvt_pk_bf16_f32 v179, v190, v191\n" \
  "v_pk_mul_f32 v[236:237], v[232:233], v[236:237]\n" \
  "v_pk_mul_f32 v[238:239], v[234:235], v[238:239]\n" \
  "v_pk_mul_f32 v[244:245], v[240:241], v[244:245]\n" \
  "v_pk_mul_f32 v[246:247], v[242:243], v[246:247]\n" \
  "v_permlane16_swap_b32 v176, v178\n" \
  "v_permlane16_swap_b32 v177, v179\n" \
  "buffer_store_dwordx4 v[176:179], %[ho0], %[srdw], 0 offen offset:0 sc1\n" \
  "v_add_u32 %[ho0], 114688, %[ho0]\n" \
  "v_pk_mul_f32 v[232:233], v[232:233], s[60:61]\n" \
  "v_pk_mul_f32 v[234:235], v[234:235], s[60:61]\n" \
  "v_pk_mul_f32 v[240:241], v[240:241], s[60:61]\n" \
  "v_pk_mul_f32 v[242:243], v[242:243], s[60:61]\n" \
  "v_exp_f32 v232, v232\n" \
  "v_exp_f32 v233, v233\n" \
  "v_exp_f32 v234, v234\n" \
  "v_exp_f32 v235, v235\n" \
  "v_exp_f32 v240, v240\n" \
  "v_exp_f32 v241, v241\n" \
  "v_exp_f32 v242, v242\n" \
  "v_exp_f32 v243, v243\n" \
  "v_pk_add_f32 v[232:233], v[232:233], 1.0 op_sel_hi:[1,0]\n" \
  "v_pk_add_f32 v[234:235], v[234:235], 1.0 op_sel_hi:[1,0]\n" \
  "v_pk_add_f32 v[240:241], v[240:241], 1.0 op_sel_hi:[1,0]\n" \
  "v_pk_add_f32 v[242:243], v[242:243], 1.0 op_sel_hi:[1,0]\n" \
  "v_rcp_f32 v232, v232\n" \
  "v_rcp_f32 v233, v233\n" \
  "v_rcp_f32 v234, v234\n" \
  "v_rcp_f32 v235, v235\n" \
  "v_rcp_f32 v240, v240\n" \
  "v_rcp_f32 v241, v241\n" \
  "v_rcp_f32 v242, v242\n" \
  "v_rcp_f32 v243, v243\n" \
  "v_pk_mul_f32 v[236:237], v[236:237], v[232:233]\n" \
  "v_pk_mul_f32 v[238:239], v[238:239], v[234:235]\n" \
  "v_pk_mul_f32 v[244:245], v[244:245], v[240:241]\n" \
  "v_pk_mul_f32 v[246:247], v[246:247], v[242:243]\n" \
  "v_cvt_pk_bf16_f32 v232, v236, v237\n" \
  "v_cvt_pk_bf16_f32 v234, v244, v245\n" \
  "v_cvt_pk_bf16_f32 v233, v238, v239\n" \
  "v_cvt_pk_bf16_f32 v235, v246, v247\n" \
  "v_pk_mul_f32 v[84:85], v[80:81], v[84:85]\n" \
  "v_pk_mul_f32 v[86:87], v[82:83], v[86:87]\n" \
  "v_pk_mul_f32 v[140:141], v[136:137], v[140:141]\n" \
  "v_pk_mul_f32 v[142:143], v[138:139], v[142:143]\n" \
  "v_permlane16_swap_b32 v232, v234\n" \
  "v_permlane16_swap_b32 v233, v235\n" \
  "buffer_store_dwordx4 v[232:235], %[ho0], %[srdw], 0 offen offset:0 sc1\n" \
  "v_pk_mul_f32 v[80:81], v[80:81], s[60:61]\n" \
  "v_pk_mul_f32 v[82:83], v[82:83], s[60:61]\n" \
  "v_pk_mul_f32 v[136:137], v[136:137], s[60:61]\n" \
  "v_pk_mul_f32 v[138:139], v[138:139], s[60:61]\n" \
  "v_exp_f32 v80, v80\n" \
  "v_exp_f32 v81, v81\n" \
  "v_exp_f32 v82, v82\n" \
  "v_exp_f32 v83, v83\n" \
  "v_exp_f32 v136, v136\n" \
  "v_exp_f32 v137, v137\n" \
  "v_exp_f32 v138, v138\n" \
  "v_exp_f32 v139, v139\n" \
  "v_pk_add_f32 v[80:81], v[80:81], 1.0 op_sel_hi:[1,0]\n" \
  "v_pk_add_f32 v[82:83], v[82:83], 1.0 op_sel_hi:[1,0]\n" \
  "v_pk_add_f32 v[136:137], v[136:137], 1.0 op_sel_hi:[1,0]\n" \
  "v_pk_add_f32 v[138:139], v[138:139], 1.0 op_sel_hi:[1,0]\n" \
  "v_rcp_f32 v80, v80\n" \
  "v_rcp_f32 v81, v81\n" \
  "v_rcp_f32 v82, v82\n" \
  "v_rcp_f32 v83, v83\n" \
  "v_rcp_f32 v136, v136\n" \
  "v_rcp_f32 v137, v137\n" \
  "v_rcp_f32 v138, v138\n" \
  "v_rcp_f32 v139, v139\n" \
  "v_pk_mul_f32 v[84:85], v[84:85], v[80:81]\n" \
  "v_pk_mul_f32 v[86:87], v[86:87], v[82:83]\n" \
  "v_pk_mul_f32 v[140:141], v[140:141], v[136:137]\n" \
  "v_pk_mul_f32 v[142:143], v[142:143], v[138:139]\n" \
  "v_cvt_pk_bf16_f32 v80, v84, v85\n" \
  "v_cvt_pk_bf16_f32 v82, v140, v141\n" \
  "v_cvt_pk_bf16_f32 v81, v86, v87\n" \
  "v_cvt_pk_bf16_f32 v83, v142, v143\n" \
  "v_pk_mul_f32 v[196:197], v[192:193], v[196:197]\n" \
  "v_pk_mul_f32 v[198:199], v[194:195], v[198:199]\n" \
  "v_pk_mul_f32 v[252:253], v[248:249], v[252:253]\n" \
  "v_pk_mul_f32 v[254:255], v[250:251], v[254:255]\n" \
  "v_permlane16_swap_b32 v80, v82\n" \
  "v_permlane16_swap_b32 v81, v83\n" \
  "buffer_store_dwordx4 v[80:83], %[ho1], %[srdw], 0 offen offset:0 sc1\n" \
  "v_add_u32 %[ho1], 229376, %[ho1]\n" \
  "global_load_dwordx2 %[ft], %[fap], off sc1\n" \
  "v_pk_mul_f32 v[192:193], v[192:193], s[60:61]\n" \
  "v_pk_mul_f32 v[194:195], v[194:195], s[60:61]\n" \
  "v_pk_mul_f32 v[248:249], v[248:249], s[60:61]\n" \
  "v_pk_mul_f32 v[250:251], v[250:251], s[60:61]\n" \
  "v_exp_f32 v192, v192\n" \
  "v_exp_f32 v193, v193\n" \
  "v_exp_f32 v194, v194\n" \
  "v_exp_f32 v195, v195\n" \
  "v_exp_f32 v248, v248\n" \
  "v_exp_f32 v249, v249\n" \
  "v_exp_f32 v250, v250\n" \
  "v_exp_f32 v251, v251\n" \
  "v_pk_add_f32 v[192:193], v[192:193], 1.0 op_sel_hi:[1,0]\n" \
  "v_pk_add_f32 v[194:195], v[194:195], 1.0 op_sel_hi:[1,0]\n" \
  "v_pk_add_f32 v[248:249], v[248:249], 1.0 op_sel_hi:[1,0]\n" \
  "v_pk_add_f32 v[250:251], v[250:251], 1.0 op_sel_hi:[1,0]\n" \
  "v_rcp_f32 v192, v192\n" \
  "v_rcp_f32 v193, v193\n" \
  "v_rcp_f32 v194, v194\n" \
  "v_rcp_f32 v195, v195\n" \
  "v_rcp_f32 v248, v248\n" \
  "v_rcp_f32 v249, v249\n" \
  "v_rcp_f32 v250, v250\n" \
  "v_rcp_f32 v251, v251\n" \
  "v_pk_mul_f32 v[196:197], v[196:197], v[192:193]\n" \
  "v_pk_mul_f32 v[198:199], v[198:199], v[194:195]\n" \
  "v_pk_mul_f32 v[252:253], v[252:253], v[248:249]\n" \
  "v_pk_mul_f32 v[254:255], v[254:255], v[250:251]\n" \
  "v_cvt_pk_bf16_f32 v192, v196, v197\n" \
  "v_cvt_pk_bf16_f32 v194, v252, v253\n" \
  "v_cvt_pk_bf16_f32 v193, v198, v199\n" \
  "v_cvt_pk_bf16_f32 v195, v254, v255\n" \
  "s_nop 1\n" \
  "v_permlane16_swap_b32 v192, v194\n" \
  "v_permlane16_swap_b32 v193, v195\n" \
  "buffer_store_dwordx4 v[192:195], %[ho1], %[srdw], 0 offen offset:0 sc1\n" \
  "s_nop 1\n"
#define CONV_ASM \
  "ds_read2_b32 a[0:1], %[ab0] offset0:0 offset1:1\n" \
  "ds_read2_b32 a[2:3], %[ab0] offset0:2 offset1:3\n" \
  "ds_read_b128 a[64:67], %[bb] offset:3584\n" \
  "ds_read2_b32 a[4:5], %[ab0] offset0:128 offset1:129\n" \
  "ds_read2_b32 a[6:7], %[ab0] offset0:130 offset1:131\n" \
  "ds_read_b128 a[68:71], %[bb] offset:3072\n" \
  "ds_read2_b32 a[8:9], %[ab1] offset0:0 offset1:1\n" \
  "ds_read2_b32 a[10:11], %[ab1] offset0:2 offset1:3\n" \
  "ds_read_b128 a[72:75], %[bb] offset:2560\n" \
  "ds_read2_b32 a[12:13], %[ab1] offset0:128 offset1:129\n" \
  "ds_read2_b32 a[14:15], %[ab1] offset0:130 offset1:131\n" \
  "ds_read_b128 a[76:79], %[bb] offset:2048\n" \
  "ds_read2_b32 a[16:17], %[ab2] offset0:0 offset1:1\n" \
  "ds_read2_b32 a[18:19], %[ab2] offset0:2 offset1:3\n" \
  "ds_read_b128 a[80:83], %[bb] offset:1536\n" \
  "ds_read2_b32 a[20:21], %[ab2] offset0:128 offset1:129\n" \
  "ds_read2_b32 a[22:23], %[ab2] offset0:130 offset1:131\n" \
  "ds_read_b128 a[84:87], %[bb] offset:1024\n" \
  "ds_read2_b32 a[24:25], %[ab3] offset0:0 offset1:1\n" \
  "ds_read2_b32 a[26:27], %[ab3] offset0:2 offset1:3\n" \
  "ds_read_b128 a[88:91], %[bb] offset:512\n" \
  "ds_read2_b32 a[28:29], %[ab3] offset0:128 offset1:129\n" \
  "ds_read2_b32 a[30:31], %[ab3] offset0:130 offset1:131\n" \
  "ds_read_b128 a[92:95], %[bb] offset:0\n" \
  "s_waitcnt lgkmcnt(15)\n" \
  "v_mfma_f32_16x16x32_bf16 v[32:35], a[0:3], a[64:67], 0\n" \
  "v_mfma_f32_16x16x32_bf16 v[32:35], %[dg], a[64:67], v[32:35]\n" \
  "s_waitcnt lgkmcnt(15)\n" \
  "v_mfma_f32_16x16x32_bf16 v[36:39], a[4:7], a[64:67], 0\n" \
  "v_mfma_f32_16x16x32_bf16 v[36:39], a[0:3], a[68:71], v[36:39]\n" \
  "v_mfma_f32_16x16x32_bf16 v[36:39], %[dg], a[68:71], v[36:39]\n" \
  "s_waitcnt lgkmcnt(15)\n" \
  "v_mfma_f32_16x16x32_bf16 v[40:43], a[0:3], a[72:75], 0\n" \
  "v_mfma_f32_16x16x32_bf16 v[40:43], %[dg], a[72:75], v[40:43]\n" \
  "v_mfma_f32_16x16x32_bf16 v[40:43], a[4:7], a[68:71], v[40:43]\n" \
  "v_mfma_f32_16x16x32_bf16 v[40:43], a[8:11], a[64:67], v[40:43]\n" \
  "s_waitcnt lgkmcnt(12)\n" \
  "v_mfma_f32_16x16x32_bf16 v[44:47], a[12:15], a[64:67], 0\n" \
  "ds_read2_b32 a[32:33], %[ab0] offset0:16 offset1:17\n" \
  "v_mfma_f32_16x16x32_bf16 v[44:47], a[8:11], a[68:71], v[44:47]\n" \
  "ds_read2_b32 a[34:35], %[ab0] offset0:18 offset1:19\n" \
  "v_mfma_f32_16x16x32_bf16 v[44:47], a[4:7], a[72:75], v[44:47]\n" \
  "ds_read_b128 a[96:99], %[bb] offset:3648\n" \
  "v_mfma_f32_16x16x32_bf16 v[44:47], a[0:3], a[76:79], v[44:47]\n" \
  "v_mfma_f32_16x16x32_bf16 v[44:47], %[dg], a[76:79], v[44:47]\n" \
  "ds_read2_b32 a[36:37], %[ab0] offset0:144 offset1:145\n" \
  "s_waitcnt lgkmcnt(13)\n" \
  "v_mfma_f32_16x16x32_bf16 v[48:51], a[0:3], a[80:83], 0\n" \
  "v_mfma_f32_16x16x32_bf16 v[48:51], %[dg], a[80:83], v[48:51]\n" \
  "ds_read2_b32 a[38:39], %[ab0] offset0:146 offset1:147\n" \
  "v_mfma_f32_16x16x32_bf16 v[48:51], a[4:7], a[76:79], v[48:51]\n" \
  "ds_read_b128 a[100:103], %[bb] offset:3136\n" \
  "v_mfma_f32_16x16x32_bf16 v[48:51], a[8:11], a[72:75], v[48:51]\n" \
  "ds_read2_b32 a[40:41], %[ab1] offset0:16 offset1:17\n" \
  "v_mfma_f32_16x16x32_bf16 v[48:51], a[12:15], a[68:71], v[48:51]\n" \
  "ds_read2_b32 a[42:43], %[ab1] offset0:18 offset1:19\n" \
  "v_mfma_f32_16x16x32_bf16 v[48:51], a[16:19], a[64:67], v[48:51]\n" \
  "ds_read_b128 a[104:107], %[bb] offset:2624\n" \
  "s_waitcnt lgkmcnt(15)\n" \
  "v_mfma_f32_16x16x32_bf16 v[52:55], a[20:23], a[64:67], 0\n" \
  "ds_read2_b32 a[44:45], %[ab1] offset0:144 offset1:145\n" \
  "v_mfma_f32_16x16x32_bf16 v[52:55], a[16:19], a[68:71], v[52:55]\n" \
  "ds_read2_b32 a[46:47], %[ab1] offset0:146 offset1:147\n" \
  "v_mfma_f32_16x16x32_bf16 v[52:55], a[12:15], a[72:75], v[52:55]\n" \
  "ds_read_b128 a[108:111], %[bb] offset:2112\n" \
  "v_mfma_f32_16x16x32_bf16 v[52:55], a[8:11], a[76:79], v[52:55]\n" \
  "ds_read2_b32 a[48:49], %[ab2] offset0:16 offset1:17\n" \
  "v_mfma_f32_16x16x32_bf16 v[52:55], a[4:7], a[80:83], v[52:55]\n" \
  "ds_read2_b32 a[50:51], %[ab2] offset0:18 offset1:19\n" \
  "v_mfma_f32_16x16x32_bf16 v[52:55], a[0:3], a[84:87], v[52:55]\n" \
  "v_mfma_f32_16x16x32_bf16 v[52:55], %[dg], a[84:87], v[52:55]\n" \
  "ds_read_b128 a[112:115], %[bb] offset:1600\n" \
  "s_waitcnt lgkmcnt(15)\n" \
  "v_mfma_f32_16x16x32_bf16 v[56:59], a[0:3], a[88:91], 0\n" \
  "v_mfma_f32_16x16x32_bf16 v[56:59], %[dg], a[88:91], v[56:59]\n" \
  "ds_read2_b32 a[52:53], %[ab2] offset0:144 offset1:145\n" \
  "v_mfma_f32_16x16x32_bf16 v[56:59], a[4:7], a[84:87], v[56:59]\n" \
  "ds_read2_b32 a[54:55], %[ab2] offset0:146 offset1:147\n" \
  "v_mfma_f32_16x16x32_bf16 v[56:59], a[8:11], a[80:83], v[56:59]\n" \
  "ds_read_b128 a[116:119], %[bb] offset:1088\n" \
  "v_mfma_f32_16x16x32_bf16 v[56:59], a[12:15], a[76:79], v[56:59]\n" \
  "ds_read2_b32 a[56:57], %[ab3] offset0:16 offset1:17\n" \
  "v_mfma_f32_16x16x32_bf16 v[56:59], a[16:19], a[72:75], v[56:59]\n" \
  "ds_read2_b32 a[58:59], %[ab3] offset0:18 offset1:19\n" \
  "v_mfma_f32_16x16x32_bf16 v[56:59], a[20:23], a[68:71], v[56:59]\n" \
  "ds_read_b128 a[120:123], %[bb] offset:576\n" \
  "v_mfma_f32_16x16x32_bf16 v[56:59], a[24:27], a[64:67], v[56:59]\n" \
  "ds_read2_b32 a[60:61], %[ab3] offset0:144 offset1:145\n" \
  "s_waitcnt lgkmcnt(15)\n" \
  "v_mfma_f32_16x16x32_bf16 v[60:63], a[28:31], a[64:67], 0\n" \
  "ds_read2_b32 a[62:63], %[ab3] offset0:146 offset1:147\n" \
  "v_mfma_f32_16x16x32_bf16 v[60:63], a[24:27], a[68:71], v[60:63]\n" \
  "ds_read_b128 a[124:127], %[bb] offset:64\n" \
  "v_mfma_f32_16x16x32_bf16 v[60:63], a[20:23], a[72:75], v[60:63]\n" \
  "v_mfma_f32_16x16x32_bf16 v[60:63], a[16:19], a[76:79], v[60:63]\n" \
  "v_mfma_f32_16x16x32_bf16 v[60:63], a[12:15], a[80:83], v[60:63]\n" \
  "v_mfma_f32_16x16x32_bf16 v[60:63], a[8:11], a[84:87], v[60:63]\n" \
  "v_mfma_f32_16x16x32_bf16 v[60:63], a[4:7], a[88:91], v[60:63]\n" \
  "v_mfma_f32_16x16x32_bf16 v[60:63], a[0:3], a[92:95], v[60:63]\n" \
  "v_mfma_f32_16x16x32_bf16 v[60:63], %[dg], a[92:95], v[60:63]\n" \
  "s_waitcnt lgkmcnt(0)\n" \
  "v_mfma_f32_16x16x32_bf16 v[32:35], a[32:35], a[96:99], v[32:35]\n" \
  "ds_read2_b32 a[0:1], %[ab0] offset0:32 offset1:33\n" \
  "v_mfma_f32_16x16x32_bf16 v[36:39], a[36:39], a[96:99], v[36:39]\n" \
  "ds_read2_b32 a[2:3], %[ab0] offset0:34 offset1:35\n" \
  "v_mfma_f32_16x16x32_bf16 v[36:39], a[32:35], a[100:103], v[36:39]\n" \
  "ds_read_b128 a[64:67], %[bb] offset:3712\n" \
  "v_mfma_f32_16x16x32_bf16 v[40:43], a[32:35], a[104:107], v[40:43]\n" \
  "ds_read2_b32 a[4:5], %[ab0] offset0:160 offset1:161\n" \
  "v_mfma_f32_16x16x32_bf16 v[40:43], a[36:39], a[100:103], v[40:43]\n" \
  "ds_read2_b32 a[6:7], %[ab0] offset0:162 offset1:163\n" \
  "v_mfma_f32_16x16x32_bf16 v[40:43], a[40:43], a[96:99], v[40:43]\n" \
  "ds_read_b128 a[68:71], %[bb] offset:3200\n" \
  "v_mfma_f32_16x16x32_bf16 v[44:47], a[44:47], a[96:99], v[44:47]\n" \
  "ds_read2_b32 a[8:9], %[ab1] offset0:32 offset1:33\n" \
  "v_mfma_f32_16x16x32_bf16 v[44:47], a[40:43], a[100:103], v[44:47]\n" \
  "ds_read2_b32 a[10:11], %[ab1] offset0:34 offset1:35\n" \
  "v_mfma_f32_16x16x32_bf16 v[44:47], a[36:39], a[104:107], v[44:47]\n" \
  "ds_read_b128 a[72:75], %[bb] offset:2688\n" \
  "v_mfma_f32_16x16x32_bf16 v[44:47], a[32:35], a[108:111], v[44:47]\n" \
  "ds_read2_b32 a[12:13], %[ab1] offset0:160 offset1:161\n" \
  "v_mfma_f32_16x16x32_bf16 v[48:51], a[32:35], a[112:115], v[48:51]\n" \
  "ds_read2_b32 a[14:15], %[ab1] offset0:162 offset1:163\n" \
  "v_mfma_f32_16x16x32_bf16 v[48:51], a[36:39], a[108:111], v[48:51]\n" \
  "ds_read_b128 a[76:79], %[bb] offset:2176\n" \
  "v_mfma_f32_16x16x32_bf16 v[48:51], a[40:43], a[104:107], v[48:51]\n" \
  "ds_read2_b32 a[16:17], %[ab2] offset0:32 offset1:33\n" \
  "v_mfma_f32_16x16x32_bf16 v[48:51], a[44:47], a[100:103], v[48:51]\n" \
  "ds_read2_b32 a[18:19], %[ab2] offset0:34 offset1:35\n" \
  "v_mfma_f32_16x16x32_bf16 v[48:51], a[48:51], a[96:99], v[48:51]\n" \
  "ds_read_b128 a[80:83], %[bb] offset:1664\n" \
  "v_mfma_f32_16x16x32_bf16 v[52:55], a[52:55], a[96:99], v[52:55]\n" \
  "ds_read2_b32 a[20:21], %[ab2] offset0:160 offset1:161\n" \
  "v_mfma_f32_16x16x32_bf16 v[52:55], a[48:51], a[100:103], v[52:55]\n" \
  "ds_read2_b32 a[22:23], %[ab2] offset0:162 offset1:163\n" \
  "v_mfma_f32_16x16x32_bf16 v[52:55], a[44:47], a[104:107], v[52:55]\n" \
  "ds_read_b128 a[84:87], %[bb] offset:1152\n" \
  "v_mfma_f32_16x16x32_bf16 v[52:55], a[40:43], a[108:111], v[52:55]\n" \
  "ds_read2_b32 a[24:25], %[ab3] offset0:32 offset1:33\n" \
  "v_mfma_f32_16x16x32_bf16 v[52:55], a[36:39], a[112:115], v[52:55]\n" \
  "ds_read2_b32 a[26:27], %[ab3] offset0:34 offset1:35\n" \
  "v_mfma_f32_16x16x32_bf16 v[52:55], a[32:35], a[116:119], v[52:55]\n" \
  "ds_read_b128 a[88:91], %[bb] offset:640\n" \
  "v_mfma_f32_16x16x32_bf16 v[56:59], a[32:35], a[120:123], v[56:59]\n" \
  "ds_read2_b32 a[28:29], %[ab3] offset0:160 offset1:161\n" \
  "v_mfma_f32_16x16x32_bf16 v[56:59], a[36:39], a[116:119], v[56:59]\n" \
  "ds_read2_b32 a[30:31], %[ab3] offset0:162 offset1:163\n" \
  "v_mfma_f32_16x16x32_bf16 v[56:59], a[40:43], a[112:115], v[56:59]\n" \
  "ds_read_b128 a[92:95], %[bb] offset:128\n" \
  "v_mfma_f32_16x16x32_bf16 v[56:59], a[44:47], a[108:111], v[56:59]\n" \
  "v_mfma_f32_16x16x32_bf16 v[56:59], a[48:51], a[104:107], v[56:59]\n" \
  "v_mfma_f32_16x16x32_bf16 v[56:59], a[52:55], a[100:103], v[56:59]\n" \
  "v_mfma_f32_16x16x32_bf16 v[56:59], a[56:59], a[96:99], v[56:59]\n" \
  "v_mfma_f32_16x16x32_bf16 v[60:63], a[60:63], a[96:99], v[60:63]\n" \
  "v_mfma_f32_16x16x32_bf16 v[60:63], a[56:59], a[100:103], v[60:63]\n" \
  "v_mfma_f32_16x16x32_bf16 v[60:63], a[52:55], a[104:107], v[60:63]\n" \
  "v_mfma_f32_16x16x32_bf16 v[60:63], a[48:51], a[108:111], v[60:63]\n" \
  "v_mfma_f32_16x16x32_bf16 v[60:63], a[44:47], a[112:115], v[60:63]\n" \
  "v_mfma_f32_16x16x32_bf16 v[60:63], a[40:43], a[116:119], v[60:63]\n" \
  "v_mfma_f32_16x16x32_bf16 v[60:63], a[36:39], a[120:123], v[60:63]\n" \
  "v_mfma_f32_16x16x32_bf16 v[60:63], a[32:35], a[124:127], v[60:63]\n" \
  "s_waitcnt lgkmcnt(0)\n" \
  "v_mfma_f32_16x16x32_bf16 v[32:35], a[0:3], a[64:67], v[32:35]\n" \
  "ds_read2_b32 a[32:33], %[ab0] offset0:48 offset1:49\n" \
  "v_mfma_f32_16x16x32_bf16 v[36:39], a[4:7], a[64:67], v[36:39]\n" \
  "ds_read2_b32 a[34:35], %[ab0] offset0:50 offset1:51\n" \
  "v_mfma_f32_16x16x32_bf16 v[36:39], a[0:3], a[68:71], v[36:39]\n" \
  "ds_read_b128 a[96:99], %[bb] offset:3776\n" \
  "v_mfma_f32_16x16x32_bf16 v[40:43], a[0:3], a[72:75], v[40:43]\n" \
  "ds_read2_b32 a[36:37], %[ab0] offset0:176 offset1:177\n" \
  "v_mfma_f32_16x16x32_bf16 v[40:43], a[4:7], a[68:71], v[40:43]\n" \
  "ds_read2_b32 a[38:39], %[ab0] offset0:178 offset1:179\n" \
  "v_mfma_f32_16x16x32_bf16 v[40:43], a[8:11], a[64:67], v[40:43]\n" \
  "ds_read_b128 a[100:103], %[bb] offset:3264\n" \
  "v_mfma_f32_16x16x32_bf16 v[44:47], a[12:15], a[64:67], v[44:47]\n" \
  "ds_read2_b32 a[40:41], %[ab1] offset0:48 offset1:49\n" \
  "v_mfma_f32_16x16x32_bf16 v[44:47], a[8:11], a[68:71], v[44:47]\n" \
  "ds_read2_b32 a[42:43], %[ab1] offset0:50 offset1:51\n" \
  "v_mfma_f32_16x16x32_bf16 v[44:47], a[4:7], a[72:75], v[44:47]\n" \
  "ds_read_b128 a[104:107], %[bb] offset:2752\n" \
  "v_mfma_f32_16x16x32_bf16 v[44:47], a[0:3], a[76:79], v[44:47]\n" \
  "ds_read2_b32 a[44:45], %[ab1] offset0:176 offset1:177\n" \
  "v_mfma_f32_16x16x32_bf16 v[48:51], a[0:3], a[80:83], v[48:51]\n" \
  "ds_read2_b32 a[46:47], %[ab1] offset0:178 offset1:179\n" \
  "v_mfma_f32_16x16x32_bf16 v[48:51], a[4:7], a[76:79], v[48:51]\n" \
  "ds_read_b128 a[108:111], %[bb] offset:2240\n" \
  "v_mfma_f32_16x16x32_bf16 v[48:51], a[8:11], a[72:75], v[48:51]\n" \
  "ds_read2_b32 a[48:49], %[ab2] offset0:48 offset1:49\n" \
  "v_mfma_f32_16x16x32_bf16 v[48:51], a[12:15], a[68:71], v[48:51]\n" \
  "ds_read2_b32 a[50:51], %[ab2] offset0:50 offset1:51\n" \
  "v_mfma_f32_16x16x32_bf16 v[48:51], a[16:19], a[64:67], v[48:51]\n" \
  "ds_read_b128 a[112:115], %[bb] offset:1728\n" \
  "v_mfma_f32_16x16x32_bf16 v[52:55], a[20:23], a[64:67], v[52:55]\n" \
  "ds_read2_b32 a[52:53], %[ab2] offset0:176 offset1:177\n" \
  "v_mfma_f32_16x16x32_bf16 v[52:55], a[16:19], a[68:71], v[52:55]\n" \
  "ds_read2_b32 a[54:55], %[ab2] offset0:178 offset1:179\n" \
  "v_mfma_f32_16x16x32_bf16 v[52:55], a[12:15], a[72:75], v[52:55]\n" \
  "ds_read_b128 a[116:119], %[bb] offset:1216\n" \
  "v_mfma_f32_16x16x32_bf16 v[52:55], a[8:11], a[76:79], v[52:55]\n" \
  "ds_read2_b32 a[56:57], %[ab3] offset0:48 offset1:49\n" \
  "v_mfma_f32_16x16x32_bf16 v[52:55], a[4:7], a[80:83], v[52:55]\n" \
  "ds_read2_b32 a[58:59], %[ab3] offset0:50 offset1:51\n" \
  "v_mfma_f32_16x16x32_bf16 v[52:55], a[0:3], a[84:87], v[52:55]\n" \
  "ds_read_b128 a[120:123], %[bb] offset:704\n" \
  "v_mfma_f32_16x16x32_bf16 v[56:59], a[0:3], a[88:91], v[56:59]\n" \
  "ds_read2_b32 a[60:61], %[ab3] offset0:176 offset1:177\n" \
  "v_mfma_f32_16x16x32_bf16 v[56:59], a[4:7], a[84:87], v[56:59]\n" \
  "ds_read2_b32 a[62:63], %[ab3] offset0:178 offset1:179\n" \
  "v_mfma_f32_16x16x32_bf16 v[56:59], a[8:11], a[80:83], v[56:59]\n" \
  "ds_read_b128 a[124:127], %[bb] offset:192\n" \
  "v_mfma_f32_16x16x32_bf16 v[56:59], a[12:15], a[76:79], v[56:59]\n" \
  "v_mfma_f32_16x16x32_bf16 v[56:59], a[16:19], a[72:75], v[56:59]\n" \
  "v_mfma_f32_16x16x32_bf16 v[56:59], a[20:23], a[68:71], v[56:59]\n" \
  "v_mfma_f32_16x16x32_bf16 v[56:59], a[24:27], a[64:67], v[56:59]\n" \
  "v_mfma_f32_16x16x32_bf16 v[60:63], a[28:31], a[64:67], v[60:63]\n" \
  "v_mfma_f32_16x16x32_bf16 v[60:63], a[24:27], a[68:71], v[60:63]\n" \
  "v_mfma_f32_16x16x32_bf16 v[60:63], a[20:23], a[72:75], v[60:63]\n" \
  "v_mfma_f32_16x16x32_bf16 v[60:63], a[16:19], a[76:79], v[60:63]\n" \
  "v_mfma_f32_16x16x32_bf16 v[60:63], a[12:15], a[80:83], v[60:63]\n" \
  "v_mfma_f32_16x16x32_bf16 v[60:63], a[8:11], a[84:87], v[60:63]\n" \
  "v_mfma_f32_16x16x32_bf16 v[60:63], a[4:7], a[88:91], v[60:63]\n" \
  "v_mfma_f32_16x16x32_bf16 v[60:63], a[0:3], a[92:95], v[60:63]\n" \
  "s_waitcnt lgkmcnt(0)\n" \
  "v_mfma_f32_16x16x32_bf16 v[32:35], a[32:35], a[96:99], v[32:35]\n" \
  "ds_read2_b32 a[0:1], %[ab0] offset0:64 offset1:65\n" \
  "v_mfma_f32_16x16x32_bf16 v[36:39], a[36:39], a[96:99], v[36:39]\n" \
  "ds_read2_b32 a[2:3], %[ab0] offset0:66 offset1:67\n" \
  "v_mfma_f32_16x16x32_bf16 v[36:39], a[32:35], a[100:103], v[36:39]\n" \
  "ds_read_b128 a[64:67], %[bb] offset:3840\n" \
  "v_mfma_f32_16x16x32_bf16 v[40:43], a[32:35], a[104:107], v[40:43]\n" \
  "ds_read2_b32 a[4:5], %[ab0] offset0:192 offset1:193\n" \
  "v_mfma_f32_16x16x32_bf16 v[40:43], a[36:39], a[100:103], v[40:43]\n" \
  "ds_read2_b32 a[6:7], %[ab0] offset0:194 offset1:195\n" \
  "v_mfma_f32_16x16x32_bf16 v[40:43], a[40:43], a[96:99], v[40:43]\n" \
  "ds_read_b128 a[68:71], %[bb] offset:3328\n" \
  "v_mfma_f32_16x16x32_bf16 v[44:47], a[44:47], a[96:99], v[44:47]\n" \
  "ds_read2_b32 a[8:9], %[ab1] offset0:64 offset1:65\n" \
  "v_mfma_f32_16x16x32_bf16 v[44:47], a[40:43], a[100:103], v[44:47]\n" \
  "ds_read2_b32 a[10:11], %[ab1] offset0:66 offset1:67\n" \
  "v_mfma_f32_16x16x32_bf16 v[44:47], a[36:39], a[104:107], v[44:47]\n" \
  "ds_read_b128 a[72:75], %[bb] offset:2816\n" \
  "v_mfma_f32_16x16x32_bf16 v[44:47], a[32:35], a[108:111], v[44:47]\n" \
  "ds_read2_b32 a[12:13], %[ab1] offset0:192 offset1:193\n" \
  "v_mfma_f32_16x16x32_bf16 v[48:51], a[32:35], a[112:115], v[48:51]\n" \
  "ds_read2_b32 a[14:15], %[ab1] offset0:194 offset1:195\n" \
  "v_mfma_f32_16x16x32_bf16 v[48:51], a[36:39], a[108:111], v[48:51]\n" \
  "ds_read_b128 a[76:79], %[bb] offset:2304\n" \
  "v_mfma_f32_16x16x32_bf16 v[48:51], a[40:43], a[104:107], v[48:51]\n" \
  "ds_read2_b32 a[16:17], %[ab2] offset0:64 offset1:65\n" \
  "v_mfma_f32_16x16x32_bf16 v[48:51], a[44:47], a[100:103], v[48:51]\n" \
  "ds_read2_b32 a[18:19], %[ab2] offset0:66 offset1:67\n" \
  "v_mfma_f32_16x16x32_bf16 v[48:51], a[48:51], a[96:99], v[48:51]\n" \
  "ds_read_b128 a[80:83], %[bb] offset:1792\n" \
  "v_mfma_f32_16x16x32_bf16 v[52:55], a[52:55], a[96:99], v[52:55]\n" \
  "ds_read2_b32 a[20:21], %[ab2] offset0:192 offset1:193\n" \
  "v_mfma_f32_16x16x32_bf16 v[52:55], a[48:51], a[100:103], v[52:55]\n" \
  "ds_read2_b32 a[22:23], %[ab2] offset0:194 offset1:195\n" \
  "v_mfma_f32_16x16x32_bf16 v[52:55], a[44:47], a[104:107], v[52:55]\n" \
  "ds_read_b128 a[84:87], %[bb] offset:1280\n" \
  "v_mfma_f32_16x16x32_bf16 v[52:55], a[40:43], a[108:111], v[52:55]\n" \
  "ds_read2_b32 a[24:25], %[ab3] offset0:64 offset1:65\n" \
  "v_mfma_f32_16x16x32_bf16 v[52:55], a[36:39], a[112:115], v[52:55]\n" \
  "ds_read2_b32 a[26:27], %[ab3] offset0:66 offset1:67\n" \
  "v_mfma_f32_16x16x32_bf16 v[52:55], a[32:35], a[116:119], v[52:55]\n" \
  "ds_read_b128 a[88:91], %[bb] offset:768\n" \
  "v_mfma_f32_16x16x32_bf16 v[56:59], a[32:35], a[120:123], v[56:59]\n" \
  "ds_read2_b32 a[28:29], %[ab3] offset0:192 offset1:193\n" \
  "v_mfma_f32_16x16x32_bf16 v[56:59], a[36:39], a[116:119], v[56:59]\n" \
  "ds_read2_b32 a[30:31], %[ab3] offset0:194 offset1:195\n" \
  "v_mfma_f32_16x16x32_bf16 v[56:59], a[40:43], a[112:115], v[56:59]\n" \
  "ds_read_b128 a[92:95], %[bb] offset:256\n" \
  "v_mfma_f32_16x16x32_bf16 v[56:59], a[44:47], a[108:111], v[56:59]\n" \
  "v_mfma_f32_16x16x32_bf16 v[56:59], a[48:51], a[104:107], v[56:59]\n" \
  "v_mfma_f32_16x16x32_bf16 v[56:59], a[52:55], a[100:103], v[56:59]\n" \
  "v_mfma_f32_16x16x32_bf16 v[56:59], a[56:59], a[96:99], v[56:59]\n" \
  "v_mfma_f32_16x16x32_bf16 v[60:63], a[60:63], a[96:99], v[60:63]\n" \
  "v_mfma_f32_16x16x32_bf16 v[60:63], a[56:59], a[100:103], v[60:63]\n" \
  "v_mfma_f32_16x16x32_bf16 v[60:63], a[52:55], a[104:107], v[60:63]\n" \
  "v_mfma_f32_16x16x32_bf16 v[60:63], a[48:51], a[108:111], v[60:63]\n" \
  "v_mfma_f32_16x16x32_bf16 v[60:63], a[44:47], a[112:115], v[60:63]\n" \
  "v_mfma_f32_16x16x32_bf16 v[60:63], a[40:43], a[116:119], v[60:63]\n" \
  "v_mfma_f32_16x16x32_bf16 v[60:63], a[36:39], a[120:123], v[60:63]\n" \
  "v_mfma_f32_16x16x32_bf16 v[60:63], a[32:35], a[124:127], v[60:63]\n" \
  "s_waitcnt lgkmcnt(0)\n" \
  "v_mfma_f32_16x16x32_bf16 v[32:35], a[0:3], a[64:67], v[32:35]\n" \
  "ds_read2_b32 a[32:33], %[ab0] offset0:80 offset1:81\n" \
  "s_mov_b32 s44, %[pw00]\n" \
  "v_mfma_f32_16x16x32_bf16 v[36:39], a[4:7], a[64:67], v[36:39]\n" \
  "ds_read2_b32 a[34:35], %[ab0] offset0:82 offset1:83\n" \
  "s_mov_b32 s45, %[pw01]\n" \
  "v_mfma_f32_16x16x32_bf16 v[36:39], a[0:3], a[68:71], v[36:39]\n" \
  "ds_read_b128 a[96:99], %[bb] offset:3904\n" \
  "s_mov_b32 s46, %[szw]\n" \
  "v_mfma_f32_16x16x32_bf16 v[40:43], a[0:3], a[72:75], v[40:43]\n" \
  "ds_read2_b32 a[36:37], %[ab0] offset0:208 offset1:209\n" \
  "s_mov_b32 s47, 0x20000\n" \
  "v_mfma_f32_16x16x32_bf16 v[40:43], a[4:7], a[68:71], v[40:43]\n" \
  "ds_read2_b32 a[38:39], %[ab0] offset0:210 offset1:211\n" \
  "s_mov_b32 s48, %[pw10]\n" \
  "v_mfma_f32_16x16x32_bf16 v[40:43], a[8:11], a[64:67], v[40:43]\n" \
  "ds_read_b128 a[100:103], %[bb] offset:3392\n" \
  "s_mov_b32 s49, %[pw11]\n" \
  "v_mfma_f32_16x16x32_bf16 v[44:47], a[12:15], a[64:67], v[44:47]\n" \
  "ds_read2_b32 a[40:41], %[ab1] offset0:80 offset1:81\n" \
  "s_mov_b32 s50, %[szw]\n" \
  "v_mfma_f32_16x16x32_bf16 v[44:47], a[8:11], a[68:71], v[44:47]\n" \
  "ds_read2_b32 a[42:43], %[ab1] offset0:82 offset1:83\n" \
  "s_mov_b32 s51, 0x20000\n" \
  "v_mfma_f32_16x16x32_bf16 v[44:47], a[4:7], a[72:75], v[44:47]\n" \
  "ds_read_b128 a[104:107], %[bb] offset:2880\n" \
  "s_mov_b32 s56, 0\n" \
  "v_mfma_f32_16x16x32_bf16 v[44:47], a[0:3], a[76:79], v[44:47]\n" \
  "ds_read2_b32 a[44:45], %[ab1] offset0:208 offset1:209\n" \
  "s_mov_b32 s57, 28672\n" \
  "v_mfma_f32_16x16x32_bf16 v[48:51], a[0:3], a[80:83], v[48:51]\n" \
  "ds_read2_b32 a[46:47], %[ab1] offset0:210 offset1:211\n" \
  "s_mov_b32 s58, 57344\n" \
  "v_mfma_f32_16x16x32_bf16 v[48:51], a[4:7], a[76:79], v[48:51]\n" \
  "ds_read_b128 a[108:111], %[bb] offset:2368\n" \
  "s_mov_b32 s59, 86016\n" \
  "v_mfma_f32_16x16x32_bf16 v[48:51], a[8:11], a[72:75], v[48:51]\n" \
  "ds_read2_b32 a[48:49], %[ab2] offset0:80 offset1:81\n" \
  "s_add_u32 s61, %[ldsw], 0\n" \
  "v_mfma_f32_16x16x32_bf16 v[48:51], a[12:15], a[68:71], v[48:51]\n" \
  "ds_read2_b32 a[50:51], %[ab2] offset0:82 offset1:83\n" \
  "s_add_u32 m0, s61, 0\n" \
  "v_mfma_f32_16x16x32_bf16 v[48:51], a[16:19], a[64:67], v[48:51]\n" \
  "ds_read_b128 a[112:115], %[bb] offset:1856\n" \
  "buffer_load_dwordx4 %[vow0], s[44:47], s56 offen lds\n" \
  "v_mfma_f32_16x16x32_bf16 v[52:55], a[20:23], a[64:67], v[52:55]\n" \
  "ds_read2_b32 a[52:53], %[ab2] offset0:208 offset1:209\n" \
  "s_add_u32 m0, s61, 1024\n" \
  "v_mfma_f32_16x16x32_bf16 v[52:55], a[16:19], a[68:71], v[52:55]\n" \
  "ds_read2_b32 a[54:55], %[ab2] offset0:210 offset1:211\n" \
  "v_mfma_f32_16x16x32_bf16 v[52:55], a[12:15], a[72:75], v[52:55]\n" \
  "ds_read_b128 a[116:119], %[bb] offset:1344\n" \
  "v_mfma_f32_16x16x32_bf16 v[52:55], a[8:11], a[76:79], v[52:55]\n" \
  "ds_read2_b32 a[56:57], %[ab3] offset0:80 offset1:81\n" \
  "v_mfma_f32_16x16x32_bf16 v[52:55], a[4:7], a[80:83], v[52:55]\n" \
  "ds_read2_b32 a[58:59], %[ab3] offset0:82 offset1:83\n" \
  "v_mfma_f32_16x16x32_bf16 v[52:55], a[0:3], a[84:87], v[52:55]\n" \
  "ds_read_b128 a[120:123], %[bb] offset:832\n" \
  "v_mfma_f32_16x16x32_bf16 v[56:59], a[0:3], a[88:91], v[56:59]\n" \
  "ds_read2_b32 a[60:61], %[ab3] offset0:208 offset1:209\n" \
  "v_mfma_f32_16x16x32_bf16 v[56:59], a[4:7], a[84:87], v[56:59]\n" \
  "ds_read2_b32 a[62:63], %[ab3] offset0:210 offset1:211\n" \
  "buffer_load_dwordx4 %[vow0], s[44:47], s57 offen lds\n" \
  "v_mfma_f32_16x16x32_bf16 v[56:59], a[8:11], a[80:83], v[56:59]\n" \
  "ds_read_b128 a[124:127], %[bb] offset:320\n" \
  "s_add_u32 m0, s61, 2048\n" \
  "v_mfma_f32_16x16x32_bf16 v[56:59], a[12:15], a[76:79], v[56:59]\n" \
  "v_mfma_f32_16x16x32_bf16 v[56:59], a[16:19], a[72:75], v[56:59]\n" \
  "v_mfma_f32_16x16x32_bf16 v[56:59], a[20:23], a[68:71], v[56:59]\n" \
  "v_mfma_f32_16x16x32_bf16 v[56:59], a[24:27], a[64:67], v[56:59]\n" \
  "v_mfma_f32_16x16x32_bf16 v[60:63], a[28:31], a[64:67], v[60:63]\n" \
  "v_mfma_f32_16x16x32_bf16 v[60:63], a[24:27], a[68:71], v[60:63]\n" \
  "v_mfma_f32_16x16x32_bf16 v[60:63], a[20:23], a[72:75], v[60:63]\n" \
  "buffer_load_dwordx4 %[vow1], s[44:47], s58 offen lds\n" \
  "s_add_u32 m0, s61, 3072\n" \
  "v_mfma_f32_16x16x32_bf16 v[60:63], a[16:19], a[76:79], v[60:63]\n" \
  "v_mfma_f32_16x16x32_bf16 v[60:63], a[12:15], a[80:83], v[60:63]\n" \
  "v_mfma_f32_16x16x32_bf16 v[60:63], a[8:11], a[84:87], v[60:63]\n" \
  "v_mfma_f32_16x16x32_bf16 v[60:63], a[4:7], a[88:91], v[60:63]\n" \
  "v_mfma_f32_16x16x32_bf16 v[60:63], a[0:3], a[92:95], v[60:63]\n" \
  "s_waitcnt lgkmcnt(0)\n" \
  "v_mfma_f32_16x16x32_bf16 v[32:35], a[32:35], a[96:99], v[32:35]\n" \
  "ds_read2_b32 a[0:1], %[ab0] offset0:96 offset1:97\n" \
  "v_mfma_f32_16x16x32_bf16 v[36:39], a[36:39], a[96:99], v[36:39]\n" \
  "ds_read2_b32 a[2:3], %[ab0] offset0:98 offset1:99\n" \
  "v_mfma_f32_16x16x32_bf16 v[36:39], a[32:35], a[100:103], v[36:39]\n" \
  "ds_read_b128 a[64:67], %[bb] offset:3968\n" \
  "buffer_load_dwordx4 %[vow1], s[44:47], s59 offen lds\n" \
  "v_mfma_f32_16x16x32_bf16 v[40:43], a[32:35], a[104:107], v[40:43]\n" \
  "ds_read2_b32 a[4:5], %[ab0] offset0:224 offset1:225\n" \
  "s_add_u32 m0, s61, 16384\n" \
  "v_mfma_f32_16x16x32_bf16 v[40:43], a[36:39], a[100:103], v[40:43]\n" \
  "ds_read2_b32 a[6:7], %[ab0] offset0:226 offset1:227\n" \
  "v_mfma_f32_16x16x32_bf16 v[40:43], a[40:43], a[96:99], v[40:43]\n" \
  "ds_read_b128 a[68:71], %[bb] offset:3456\n" \
  "v_mfma_f32_16x16x32_bf16 v[44:47], a[44:47], a[96:99], v[44:47]\n" \
  "ds_read2_b32 a[8:9], %[ab1] offset0:96 offset1:97\n" \
  "v_mfma_f32_16x16x32_bf16 v[44:47], a[40:43], a[100:103], v[44:47]\n" \
  "ds_read2_b32 a[10:11], %[ab1] offset0:98 offset1:99\n" \
  "v_mfma_f32_16x16x32_bf16 v[44:47], a[36:39], a[104:107], v[44:47]\n" \
  "ds_read_b128 a[72:75], %[bb] offset:2944\n" \
  "v_mfma_f32_16x16x32_bf16 v[44:47], a[32:35], a[108:111], v[44:47]\n" \
  "ds_read2_b32 a[12:13], %[ab1] offset0:224 offset1:225\n" \
  "v_mfma_f32_16x16x32_bf16 v[48:51], a[32:35], a[112:115], v[48:51]\n" \
  "ds_read2_b32 a[14:15], %[ab1] offset0:226 offset1:227\n" \
  "buffer_load_dwordx4 %[vow0], s[48:51], s56 offen lds\n" \
  "v_mfma_f32_16x16x32_bf16 v[48:51], a[36:39], a[108:111], v[48:51]\n" \
  "ds_read_b128 a[76:79], %[bb] offset:2432\n" \
  "s_add_u32 s56, s56, 458752\n" \
  "v_mfma_f32_16x16x32_bf16 v[48:51], a[40:43], a[104:107], v[48:51]\n" \
  "ds_read2_b32 a[16:17], %[ab2] offset0:96 offset1:97\n" \
  "s_add_u32 m0, s61, 17408\n" \
  "v_mfma_f32_16x16x32_bf16 v[48:51], a[44:47], a[100:103], v[48:51]\n" \
  "ds_read2_b32 a[18:19], %[ab2] offset0:98 offset1:99\n" \
  "v_mfma_f32_16x16x32_bf16 v[48:51], a[48:51], a[96:99], v[48:51]\n" \
  "ds_read_b128 a[80:83], %[bb] offset:1920\n" \
  "v_mfma_f32_16x16x32_bf16 v[52:55], a[52:55], a[96:99], v[52:55]\n" \
  "ds_read2_b32 a[20:21], %[ab2] offset0:224 offset1:225\n" \
  "v_mfma_f32_16x16x32_bf16 v[52:55], a[48:51], a[100:103], v[52:55]\n" \
  "ds_read2_b32 a[22:23], %[ab2] offset0:226 offset1:227\n" \
  "v_mfma_f32_16x16x32_bf16 v[52:55], a[44:47], a[104:107], v[52:55]\n" \
  "ds_read_b128 a[84:87], %[bb] offset:1408\n" \
  "v_mfma_f32_16x16x32_bf16 v[52:55], a[40:43], a[108:111], v[52:55]\n" \
  "ds_read2_b32 a[24:25], %[ab3] offset0:96 offset1:97\n" \
  "buffer_load_dwordx4 %[vow0], s[48:51], s57 offen lds\n" \
  "v_mfma_f32_16x16x32_bf16 v[52:55], a[36:39], a[112:115], v[52:55]\n" \
  "ds_read2_b32 a[26:27], %[ab3] offset0:98 offset1:99\n" \
  "s_add_u32 s57, s57, 458752\n" \
  "v_mfma_f32_16x16x32_bf16 v[52:55], a[32:35], a[116:119], v[52:55]\n" \
  "ds_read_b128 a[88:91], %[bb] offset:896\n" \
  "s_add_u32 m0, s61, 18432\n" \
  "v_mfma_f32_16x16x32_bf16 v[56:59], a[32:35], a[120:123], v[56:59]\n" \
  "ds_read2_b32 a[28:29], %[ab3] offset0:224 offset1:225\n" \
  "v_mfma_f32_16x16x32_bf16 v[56:59], a[36:39], a[116:119], v[56:59]\n" \
  "ds_read2_b32 a[30:31], %[ab3] offset0:226 offset1:227\n" \
  "v_mfma_f32_16x16x32_bf16 v[56:59], a[40:43], a[112:115], v[56:59]\n" \
  "ds_read_b128 a[92:95], %[bb] offset:384\n" \
  "v_mfma_f32_16x16x32_bf16 v[56:59], a[44:47], a[108:111], v[56:59]\n" \
  "v_mfma_f32_16x16x32_bf16 v[56:59], a[48:51], a[104:107], v[56:59]\n" \
  "v_mfma_f32_16x16x32_bf16 v[56:59], a[52:55], a[100:103], v[56:59]\n" \
  "buffer_load_dwordx4 %[vow1], s[48:51], s58 offen lds\n" \
  "s_add_u32 s58, s58, 458752\n" \
  "v_mfma_f32_16x16x32_bf16 v[56:59], a[56:59], a[96:99], v[56:59]\n" \
  "s_add_u32 m0, s61, 19456\n" \
  "v_mfma_f32_16x16x32_bf16 v[60:63], a[60:63], a[96:99], v[60:63]\n" \
  "v_mfma_f32_16x16x32_bf16 v[60:63], a[56:59], a[100:103], v[60:63]\n" \
  "v_mfma_f32_16x16x32_bf16 v[60:63], a[52:55], a[104:107], v[60:63]\n" \
  "v_mfma_f32_16x16x32_bf16 v[60:63], a[48:51], a[108:111], v[60:63]\n" \
  "v_mfma_f32_16x16x32_bf16 v[60:63], a[44:47], a[112:115], v[60:63]\n" \
  "v_mfma_f32_16x16x32_bf16 v[60:63], a[40:43], a[116:119], v[60:63]\n" \
  "v_mfma_f32_16x16x32_bf16 v[60:63], a[36:39], a[120:123], v[60:63]\n" \
  "buffer_load_dwordx4 %[vow1], s[48:51], s59 offen lds\n" \
  "s_add_u32 s59, s59, 458752\n" \
  "v_mfma_f32_16x16x32_bf16 v[60:63], a[32:35], a[124:127], v[60:63]\n" \
  "s_add_u32 s61, s61, 32768\n" \
  "s_and_b32 s61, s61, 131071\n" \
  "s_waitcnt lgkmcnt(0)\n" \
  "v_mfma_f32_16x16x32_bf16 v[32:35], a[0:3], a[64:67], v[32:35]\n" \
  "ds_read2_b32 a[32:33], %[ab0] offset0:112 offset1:113\n" \
  "s_add_u32 m0, s61, 0\n" \
  "v_mfma_f32_16x16x32_bf16 v[36:39], a[4:7], a[64:67], v[36:39]\n" \
  "ds_read2_b32 a[34:35], %[ab0] offset0:114 offset1:115\n" \
  "v_mfma_f32_16x16x32_bf16 v[36:39], a[0:3], a[68:71], v[36:39]\n" \
  "ds_read_b128 a[96:99], %[bb] offset:4032\n" \
  "v_mfma_f32_16x16x32_bf16 v[40:43], a[0:3], a[72:75], v[40:43]\n" \
  "ds_read2_b32 a[36:37], %[ab0] offset0:240 offset1:241\n" \
  "v_mfma_f32_16x16x32_bf16 v[40:43], a[4:7], a[68:71], v[40:43]\n" \
  "ds_read2_b32 a[38:39], %[ab0] offset0:242 offset1:243\n" \
  "v_mfma_f32_16x16x32_bf16 v[40:43], a[8:11], a[64:67], v[40:43]\n" \
  "ds_read_b128 a[100:103], %[bb] offset:3520\n" \
  "v_mfma_f32_16x16x32_bf16 v[44:47], a[12:15], a[64:67], v[44:47]\n" \
  "ds_read2_b32 a[40:41], %[ab1] offset0:112 offset1:113\n" \
  "buffer_load_dwordx4 %[vow0], s[44:47], s56 offen lds\n" \
  "v_mfma_f32_16x16x32_bf16 v[44:47], a[8:11], a[68:71], v[44:47]\n" \
  "ds_read2_b32 a[42:43], %[ab1] offset0:114 offset1:115\n" \
  "s_add_u32 m0, s61, 1024\n" \
  "v_mfma_f32_16x16x32_bf16 v[44:47], a[4:7], a[72:75], v[44:47]\n" \
  "ds_read_b128 a[104:107], %[bb] offset:3008\n" \
  "v_mfma_f32_16x16x32_bf16 v[44:47], a[0:3], a[76:79], v[44:47]\n" \
  "ds_read2_b32 a[44:45], %[ab1] offset0:240 offset1:241\n" \
  "v_mfma_f32_16x16x32_bf16 v[48:51], a[0:3], a[80:83], v[48:51]\n" \
  "ds_read2_b32 a[46:47], %[ab1] offset0:242 offset1:243\n" \
  "v_mfma_f32_16x16x32_bf16 v[48:51], a[4:7], a[76:79], v[48:51]\n" \
  "ds_read_b128 a[108:111], %[bb] offset:2496\n" \
  "v_mfma_f32_16x16x32_bf16 v[48:51], a[8:11], a[72:75], v[48:51]\n" \
  "ds_read2_b32 a[48:49], %[ab2] offset0:112 offset1:113\n" \
  "v_mfma_f32_16x16x32_bf16 v[48:51], a[12:15], a[68:71], v[48:51]\n" \
  "ds_read2_b32 a[50:51], %[ab2] offset0:114 offset1:115\n" \
  "v_mfma_f32_16x16x32_bf16 v[48:51], a[16:19], a[64:67], v[48:51]\n" \
  "ds_read_b128 a[112:115], %[bb] offset:1984\n" \
  "buffer_load_dwordx4 %[vow0], s[44:47], s57 offen lds\n" \
  "v_mfma_f32_16x16x32_bf16 v[52:55], a[20:23], a[64:67], v[52:55]\n" \
  "ds_read2_b32 a[52:53], %[ab2] offset0:240 offset1:241\n" \
  "s_add_u32 m0, s61, 2048\n" \
  "v_mfma_f32_16x16x32_bf16 v[52:55], a[16:19], a[68:71], v[52:55]\n" \
  "ds_read2_b32 a[54:55], %[ab2] offset0:242 offset1:243\n" \
  "v_mfma_f32_16x16x32_bf16 v[52:55], a[12:15], a[72:75], v[52:55]\n" \
  "ds_read_b128 a[116:119], %[bb] offset:1472\n" \
  "v_mfma_f32_16x16x32_bf16 v[52:55], a[8:11], a[76:79], v[52:55]\n" \
  "ds_read2_b32 a[56:57], %[ab3] offset0:112 offset1:113\n" \
  "v_mfma_f32_16x16x32_bf16 v[52:55], a[4:7], a[80:83], v[52:55]\n" \
  "ds_read2_b32 a[58:59], %[ab3] offset0:114 offset1:115\n" \
  "v_mfma_f32_16x16x32_bf16 v[52:55], a[0:3], a[84:87], v[52:55]\n" \
  "ds_read_b128 a[120:123], %[bb] offset:960\n" \
  "v_mfma_f32_16x16x32_bf16 v[56:59], a[0:3], a[88:91], v[56:59]\n" \
  "ds_read2_b32 a[60:61], %[ab3] offset0:240 offset1:241\n" \
  "v_mfma_f32_16x16x32_bf16 v[56:59], a[4:7], a[84:87], v[56:59]\n" \
  "ds_read2_b32 a[62:63], %[ab3] offset0:242 offset1:243\n" \
  "buffer_load_dwordx4 %[vow1], s[44:47], s58 offen lds\n" \
  "v_mfma_f32_16x16x32_bf16 v[56:59], a[8:11], a[80:83], v[56:59]\n" \
  "ds_read_b128 a[124:127], %[bb] offset:448\n" \
  "s_add_u32 m0, s61, 3072\n" \
  "v_mfma_f32_16x16x32_bf16 v[56:59], a[12:15], a[76:79], v[56:59]\n" \
  "v_mfma_f32_16x16x32_bf16 v[56:59], a[16:19], a[72:75], v[56:59]\n" \
  "v_mfma_f32_16x16x32_bf16 v[56:59], a[20:23], a[68:71], v[56:59]\n" \
  "v_mfma_f32_16x16x32_bf16 v[56:59], a[24:27], a[64:67], v[56:59]\n" \
  "v_mfma_f32_16x16x32_bf16 v[60:63], a[28:31], a[64:67], v[60:63]\n" \
  "v_mfma_f32_16x16x32_bf16 v[60:63], a[24:27], a[68:71], v[60:63]\n" \
  "v_mfma_f32_16x16x32_bf16 v[60:63], a[20:23], a[72:75], v[60:63]\n" \
  "buffer_load_dwordx4 %[vow1], s[44:47], s59 offen lds\n" \
  "s_add_u32 m0, s61, 16384\n" \
  "v_mfma_f32_16x16x32_bf16 v[60:63], a[16:19], a[76:79], v[60:63]\n" \
  "v_mfma_f32_16x16x32_bf16 v[60:63], a[12:15], a[80:83], v[60:63]\n" \
  "v_mfma_f32_16x16x32_bf16 v[60:63], a[8:11], a[84:87], v[60:63]\n" \
  "v_mfma_f32_16x16x32_bf16 v[60:63], a[4:7], a[88:91], v[60:63]\n" \
  "v_mfma_f32_16x16x32_bf16 v[60:63], a[0:3], a[92:95], v[60:63]\n" \
  "s_waitcnt lgkmcnt(0)\n" \
  "v_mfma_f32_16x16x32_bf16 v[32:35], a[32:35], a[96:99], v[32:35]\n" \
  "v_mfma_f32_16x16x32_bf16 v[36:39], a[36:39], a[96:99], v[36:39]\n" \
  "v_mfma_f32_16x16x32_bf16 v[36:39], a[32:35], a[100:103], v[36:39]\n" \
  "buffer_load_dwordx4 %[vow0], s[48:51], s56 offen lds\n" \
  "s_add_u32 s56, s56, 458752\n" \
  "v_mfma_f32_16x16x32_bf16 v[40:43], a[32:35], a[104:107], v[40:43]\n" \
  "s_add_u32 m0, s61, 17408\n" \
  "v_mfma_f32_16x16x32_bf16 v[40:43], a[36:39], a[100:103], v[40:43]\n" \
  "v_mfma_f32_16x16x32_bf16 v[40:43], a[40:43], a[96:99], v[40:43]\n" \
  "v_mfma_f32_16x16x32_bf16 v[44:47], a[44:47], a[96:99], v[44:47]\n" \
  "v_mfma_f32_16x16x32_bf16 v[44:47], a[40:43], a[100:103], v[44:47]\n" \
  "v_mfma_f32_16x16x32_bf16 v[44:47], a[36:39], a[104:107], v[44:47]\n" \
  "v_mfma_f32_16x16x32_bf16 v[44:47], a[32:35], a[108:111], v[44:47]\n" \
  "v_mfma_f32_16x16x32_bf16 v[48:51], a[32:35], a[112:115], v[48:51]\n" \
  "buffer_load_dwordx4 %[vow0], s[48:51], s57 offen lds\n" \
  "s_add_u32 s57, s57, 458752\n" \
  "v_mfma_f32_16x16x32_bf16 v[48:51], a[36:39], a[108:111], v[48:51]\n" \
  "s_add_u32 m0, s61, 18432\n" \
  "v_mfma_f32_16x16x32_bf16 v[48:51], a[40:43], a[104:107], v[48:51]\n" \
  "v_mfma_f32_16x16x32_bf16 v[48:51], a[44:47], a[100:103], v[48:51]\n" \
  "v_mfma_f32_16x16x32_bf16 v[48:51], a[48:51], a[96:99], v[48:51]\n" \
  "v_mfma_f32_16x16x32_bf16 v[52:55], a[52:55], a[96:99], v[52:55]\n" \
  "v_mfma_f32_16x16x32_bf16 v[52:55], a[48:51], a[100:103], v[52:55]\n" \
  "v_mfma_f32_16x16x32_bf16 v[52:55], a[44:47], a[104:107], v[52:55]\n" \
  "v_mfma_f32_16x16x32_bf16 v[52:55], a[40:43], a[108:111], v[52:55]\n" \
  "buffer_load_dwordx4 %[vow1], s[48:51], s58 offen lds\n" \
  "s_add_u32 s58, s58, 458752\n" \
  "v_mfma_f32_16x16x32_bf16 v[52:55], a[36:39], a[112:115], v[52:55]\n" \
  "s_add_u32 m0, s61, 19456\n" \
  "v_mfma_f32_16x16x32_bf16 v[52:55], a[32:35], a[116:119], v[52:55]\n" \
  "v_mfma_f32_16x16x32_bf16 v[56:59], a[32:35], a[120:123], v[56:59]\n" \
  "v_mfma_f32_16x16x32_bf16 v[56:59], a[36:39], a[116:119], v[56:59]\n" \
  "v_mfma_f32_16x16x32_bf16 v[56:59], a[40:43], a[112:115], v[56:59]\n" \
  "v_mfma_f32_16x16x32_bf16 v[56:59], a[44:47], a[108:111], v[56:59]\n" \
  "v_mfma_f32_16x16x32_bf16 v[56:59], a[48:51], a[104:107], v[56:59]\n" \
  "v_mfma_f32_16x16x32_bf16 v[56:59], a[52:55], a[100:103], v[56:59]\n" \
  "buffer_load_dwordx4 %[vow1], s[48:51], s59 offen lds\n" \
  "s_add_u32 s59, s59, 458752\n" \
  "v_mfma_f32_16x16x32_bf16 v[56:59], a[56:59], a[96:99], v[56:59]\n" \
  "s_add_u32 s61, s61, 32768\n" \
  "s_and_b32 s61, s61, 131071\n" \
  "v_mfma_f32_16x16x32_bf16 v[60:63], a[60:63], a[96:99], v[60:63]\n" \
  "v_mfma_f32_16x16x32_bf16 v[60:63], a[56:59], a[100:103], v[60:63]\n" \
  "v_mfma_f32_16x16x32_bf16 v[60:63], a[52:55], a[104:107], v[60:63]\n" \
  "v_mfma_f32_16x16x32_bf16 v[60:63], a[48:51], a[108:111], v[60:63]\n" \
  "v_mfma_f32_16x16x32_bf16 v[60:63], a[44:47], a[112:115], v[60:63]\n" \
  "v_mfma_f32_16x16x32_bf16 v[60:63], a[40:43], a[116:119], v[60:63]\n" \
  "v_mfma_f32_16x16x32_bf16 v[60:63], a[36:39], a[120:123], v[60:63]\n" \
  "v_mfma_f32_16x16x32_bf16 v[60:63], a[32:35], a[124:127], v[60:63]\n" \
  "s_nop 7\n"
#define CONV_CLOB "a0","a1","a2","a3","a4","a5","a6","a7","a8","a9","a10","a11","a12","a13","a14","a15","a16","a17","a18","a19","a20","a21","a22","a23","a24","a25","a26","a27","a28","a29","a30","a31","a32","a33","a34","a35","a36","a37","a38","a39","a40","a41","a42","a43","a44","a45","a46","a47","a48","a49","a50","a51","a52","a53","a54","a55","a56","a57","a58","a59","a60","a61","a62","a63","a64","a65","a66","a67","a68","a69","a70","a71","a72","a73","a74","a75","a76","a77","a78","a79","a80","a81","a82","a83","a84","a85","a86","a87","a88","a89","a90","a91","a92","a93","a94","a95","a96","a97","a98","a99","a100","a101","a102","a103","a104","a105","a106","a107","a108","a109","a110","a111","a112","a113","a114","a115","a116","a117","a118","a119","a120","a121","a122","a123","a124","a125","a126","a127","s40","s41","s42","s43","s44","s45","s46","s47","s48","s49","s50","s51","s52","s53","s54","s55","s56","s57","s58","s59","s60","s61","s62","s63","s64","s65","s66","s67","s68","s69","s70","s71","scc","memory"


#define DI __device__ __forceinline__
DI unsigned cvtpk(float lo, float hi) { f32x2 v = {lo, hi}; bf16x2_t b = __builtin_convertvector(v, bf16x2_t); return __builtin_bit_cast(unsigned, b); }
DI float blo(unsigned u) { return __uint_as_float(u << 16); }
DI float bhi(unsigned u) { unsigned m; asm("s_mov_b32 %0, 0xffff0000" : "=s"(m)); return __uint_as_float(u & m); }
DI unsigned rfl(unsigned x) { return __builtin_amdgcn_readfirstlane(x); }
DI void lds_bar() { asm volatile("s_waitcnt lgkmcnt(0)\n\ts_barrier" ::: "memory"); }
DI void drain_bar() { asm volatile("s_waitcnt vmcnt(0) lgkmcnt(0)\n\ts_barrier" ::: "memory"); }
template <class T> DI T lds_ld(unsigned a) { return *(const LAS T*)(size_t)a; }
template <class T> DI void lds_st(unsigned a, T v) { *(LAS T*)(size_t)a = v; }
DI void put_flag(char* ws, unsigned off, u64 tag) { __hip_atomic_store((GAS u64*)(ws + off), tag, __ATOMIC_RELAXED, __HIP_MEMORY_SCOPE_AGENT); }
DI void wait_each(char* ws, unsigned off, u64 tag) {
  const GAS u64* p = (const GAS u64*)(ws + off);
#pragma unroll 1
  for (unsigned spins = 0; spins < SPIN_LIMIT; ++spins) {
    const u64 v = __hip_atomic_load(p, __ATOMIC_RELAXED, __HIP_MEMORY_SCOPE_AGENT);
    if (__all(v == tag)) break;
  }
  asm volatile("" ::: "memory");
}
template <bool SC1> DI void touch(const void* base, unsigned bytes, unsigned off) {
  const size_t p = (size_t)base;
  const u32x4 srd = {(unsigned)p, (unsigned)(p >> 32), bytes, 0x20000u};
  if (SC1) asm volatile("s_mov_b32 m0, %2\n\ts_nop 0\n\tbuffer_load_dword %0, %1, 0 offen sc1 lds" :: "v"(off), "s"(srd), "s"(LDS_BYTES - 256) : "memory");
  else asm volatile("s_mov_b32 m0, %2\n\ts_nop 0\n\tbuffer_load_dword %0, %1, 0 offen lds" :: "v"(off), "s"(srd), "s"(LDS_BYTES - 256) : "memory");
}
DI float red8(float v) {
  v += __uint_as_float(__builtin_amdgcn_update_dpp(0u, __float_as_uint(v), 0x128  , 0xf, 0xf, false));
  auto a = __builtin_amdgcn_permlane16_swap(__float_as_uint(v), __float_as_uint(v), false, false); v = __uint_as_float(a[0]) + __uint_as_float(a[1]);
  auto c = __builtin_amdgcn_permlane32_swap(__float_as_uint(v), __float_as_uint(v), false, false); return __uint_as_float(c[0]) + __uint_as_float(c[1]);
}
DI f32x2 unpk(unsigned u) { return (f32x2){blo(u), bhi(u)}; }
DI unsigned pk2(f32x2 v) { return cvtpk(v.x, v.y); }
DI void stats_put(const f32x2 (&p)[16], unsigned base, unsigned w, int lane) {
  f32x2 a = p[0], a2 = p[1];
#pragma unroll
  for (int i = 2; i < 16; i += 2) { a += p[i]; a2 += p[i + 1]; }
  a += a2;
  const float mw = red8(a.x + a.y) * (1.f / 256.f);
  f32x2 q = {0.f, 0.f}, q2 = {0.f, 0.f};
#pragma unroll
  for (int i = 0; i < 16; i += 2) { const f32x2 d = p[i] - mw, e = p[i + 1] - mw; q = __builtin_elementwise_fma(d, d, q); q2 = __builtin_elementwise_fma(e, e, q2); }
  q += q2;
  const float m2 = red8(q.x + q.y);
  if (lane < 8) lds_st<f32x2>(base + lane * 32 + w * 8, (f32x2){mw, m2});
}
DI void stats_get(unsigned base, int s, float& mean, float& rstd) {
  const f32x4 a = lds_ld<f32x4>(base + s * 32), c = lds_ld<f32x4>(base + s * 32 + 16);
  mean = ((a[0] + a[2]) + (c[0] + c[2])) * 0.25f;
  const float d0 = a[0] - mean, d1 = a[2] - mean, d2 = c[0] - mean, d3 = c[2] - mean;
  const float q = ((a[1] + a[3]) + (c[1] + c[3])) + 256.f * ((d0 * d0 + d1 * d1) + (d2 * d2 + d3 * d3));
  rstd = __builtin_amdgcn_rsqf(q * (1.f / 1024.f) + 1e-5f);
}
DI rsrc_t mkrs(const void* p, unsigned bytes) { return __builtin_amdgcn_make_buffer_rsrc((void*)p, 0, bytes, 0x00020000); }

__global__ __launch_bounds__(256, 1) void k_fused(const u16* __restrict__ x, const u16* __restrict__ k, const u16* __restrict__ v, const u16* __restrict__ sk,
                                                  const u16* __restrict__ hk, const u16* __restrict__ Dp, const u16* __restrict__ w1, const u16* __restrict__ w3,
                                                  const u16* __restrict__ w2, u16* __restrict__ out, char* ws) {
  const int b = blockIdx.x, xg = b & 7, j = b >> 3, tid = threadIdx.x, lane = tid & 63;
  size_t cpc; unsigned csz;
  asm volatile("s_getpc_b64 %0\n.Lh3_begin:\n\ts_mov_b32 %1, .Lh3_end-.Lh3_begin" : "=s"(cpc), "=s"(csz));
  touch<false>((const void*)(cpc & ~(size_t)127), csz, tid < 16 ? (tid * 32 + j) * 128 : OOB);
  __builtin_amdgcn_sched_barrier(0);
  const unsigned w = rfl(tid >> 6);
  const rsrc_t wr = mkrs(ws, WS_END);
  const int d0 = 128 * xg + 4 * j;
  const int rs = lane & 7, rrow = 8 * b + rs, rch = 256 * w + 8 * (lane >> 3);
  unsigned draw; u64 tag;
  {
    const int cq = lane & 31, rg = (lane >> 5) + 2 * w, c0 = 128 * xg + 4 * cq, r0 = 64 * j + 8 * rg;
    const rsrc_t kr = mkrs(k, S * D * 2), vr = mkrs(v, S * D * 2);
    const unsigned vo = (r0 * D + c0) * 2, vh = r0 ? vo - 3 * D * 2 : OOB;
    u32x2 kk[11], vv[8];
#pragma unroll
    for (int i = 0; i < 3; ++i) kk[i] = __builtin_amdgcn_raw_buffer_load_b64(kr, vh, i * D * 2, 0);
#pragma unroll
    for (int i = 0; i < 8; ++i) { kk[3 + i] = __builtin_amdgcn_raw_buffer_load_b64(kr, vo, i * D * 2, 0); vv[i] = __builtin_amdgcn_raw_buffer_load_b64(vr, vo, i * D * 2, 0); }
    const u32x4 s01 = *(const u32x4*)(sk + c0 * 4), s23 = *(const u32x4*)(sk + c0 * 4 + 8);
    const u64 ep = __hip_atomic_load((const GAS u64*)(ws + WS_EP), __ATOMIC_RELAXED, __HIP_MEMORY_SCOPE_AGENT);
    draw = Dp[d0 + w];
    const rsrc_t hr = mkrs(hk + (size_t)(d0 + w) * S, S * 2);
    u32x4 hv[4]; unsigned hx[4];
#pragma unroll
    for (int i = 0; i < 4; ++i) { hv[i] = __builtin_amdgcn_raw_buffer_load_b128(hr, lane * 16 + i * 1024, 0, 0); hx[i] = __builtin_amdgcn_raw_buffer_load_b32(hr, lane * 16 + i * 1024 + 16, 0, 0); }
    if (w == 2) {
      const int pm_ = 4 * (xg & 1) + (j & 3), t4 = (pm_ * 8 + (j >> 2)) * 4 + (xg >> 1);
      const int f4 = (pm_ * 32 + 8 * (xg >> 1) + (j >> 2)) * 4;
      touch<true>(ws, WS_END, lane >= 32 ? (lane == 32 ? WS_F2 + xg * 4096 + j * 128 : OOB) : lane < 8 ? OOB : lane < 12 ? WS_F5 + (t4 * 4 + lane - 8) * 128 : lane < 16 ? WS_F4 + (f4 + lane - 12) * 128
                       : lane < 20 ? WS_F4B + (f4 + lane - 16) * 128 : lane == 20 ? WS_F1 + (xg * 32 + j) * 128 : lane == 21 ? WS_F3 + b * 128 : OOB); }
    __builtin_amdgcn_sched_barrier(0);
    const unsigned sw[8] = {s01.x, s01.y, s01.z, s01.w, s23.x, s23.y, s23.z, s23.w};
#pragma unroll
    for (int cp = 0; cp < 2; ++cp) {
      const f32x2 t0 = {blo(sw[4 * cp]), blo(sw[4 * cp + 2])}, t1 = {bhi(sw[4 * cp]), bhi(sw[4 * cp + 2])}, t2 = {blo(sw[4 * cp + 1]), blo(sw[4 * cp + 3])}, t3 = {bhi(sw[4 * cp + 1]), bhi(sw[4 * cp + 3])};
      f32x2 kf[11], r[8];
#pragma unroll
      for (int i = 0; i < 11; ++i) kf[i] = unpk(kk[i][cp]);
#pragma unroll
      for (int i = 0; i < 8; ++i) r[i] = __builtin_elementwise_fma(kf[i + 3], t0, __builtin_elementwise_fma(kf[i + 2], t1, __builtin_elementwise_fma(kf[i + 1], t2, kf[i] * t3))) * unpk(vv[i][cp]);
      const unsigned la = L_Y + (4 * cq + 2 * cp) * 128 + ((7 - rg) ^ (cq & 7)) * 16;
      lds_st<u32x4>(la, (u32x4){cvtpk(r[7].x, r[6].x), cvtpk(r[5].x, r[4].x), cvtpk(r[3].x, r[2].x), cvtpk(r[1].x, r[0].x)});
      lds_st<u32x4>(la + 128, (u32x4){cvtpk(r[7].y, r[6].y), cvtpk(r[5].y, r[4].y), cvtpk(r[3].y, r[2].y), cvtpk(r[1].y, r[0].y)});
    }
    lds_bar();
    u32x4 tv[4];
#pragma unroll
    for (int i = 0; i < 4; ++i) { const int ch = (tid >> 3) + 32 * i; tv[i] = lds_ld<u32x4>(L_Y + ch * 128 + ((tid & 7) ^ ((ch >> 2) & 7)) * 16); }
#pragma unroll
    for (int i = 0; i < 4; ++i) __builtin_amdgcn_raw_buffer_store_b128(tv[i], wr, WS_UR + (128 * xg + (tid >> 3) + 32 * i) * 4096 + (1984 - 64 * j) * 2 + (tid & 7) * 16, 0, 16);
    __builtin_amdgcn_sched_barrier(0);
    tag = ep * 6364136223846793005ull + 1442695040888963407ull;
    drain_bar();
    if (tid == 0) put_flag(ws, WS_F1 + (xg * 32 + j) * 128, tag);
    const unsigned g = L_G + w * CV_CH + 32 + lane * 16;
#pragma unroll
    for (int i = 0; i < 4; ++i) {
      lds_st<u32x4>(g + i * 1024, hv[i]);
      lds_st<u32x4>(g + i * 1024 + CV_G1, (u32x4){__builtin_amdgcn_alignbit(hv[i].y, hv[i].x, 16), __builtin_amdgcn_alignbit(hv[i].z, hv[i].y, 16), __builtin_amdgcn_alignbit(hv[i].w, hv[i].z, 16), __builtin_amdgcn_alignbit(hx[i], hv[i].w, 16)}); }
    const unsigned za = lane >= 32 ? L_U + w * CV_UCH + 4096 + (lane - 32) * 16 : L_G + w * CV_CH + (lane & 2 ? CV_G1 : 0) + (lane & 1 ? 0 : 16);
    const unsigned h0 = rfl(hv[0].x) << 16;
    if (lane < 4 || lane >= 32) lds_st<u32x4>(za, (u32x4){0u, 0u, 0u, lane == 2 ? h0 : 0u});
  }
  __builtin_amdgcn_s_sleep(D1);
  wait_each(ws, WS_F1 + xg * 4096 + (lane & 31) * 128, tag);
  const int pm = 4 * (xg & 1) + (j & 3), pn = 8 * (xg >> 1) + (j >> 2);
  f32x2 xf[16];
  {
    u32x4 uv[4];
#pragma unroll
    for (int i = 0; i < 4; ++i) uv[i] = __builtin_amdgcn_raw_buffer_load_b128(wr, WS_UR + (d0 + w) * 4096 + lane * 16 + i * 1024, 0, 16);
    const int fi = lane & 15, q = lane >> 4;
    const unsigned ab0 = L_G + w * CV_CH + ((fi + 1) & 1) * CV_G1 + 4 * ((fi + 1) >> 1) + 16 * q;
    const unsigned bb = L_U + w * CV_UCH + 480 - 32 * fi + 16 * q;
    const unsigned yb = L_Y + w * 4096 + 128 * (fi >> 2) + 8 * ((4 * (fi & 3) + q) ^ (fi >> 2));
    const size_t pw1 = (size_t)w1 + pn * 224, pw3 = (size_t)w3 + pn * 224;
    const int c0 = fi ^ (2 * q), c1 = fi ^ (2 * (q | 4)), wrb = (16 * w + q) * 7168;
    const unsigned vow0 = c0 < 14 ? wrb + c0 * 16 : OOB, vow1 = c1 < 14 ? wrb + c1 * 16 : OOB;
#pragma unroll
    for (int i = 0; i < 4; ++i) lds_st<u32x4>(L_U + w * CV_UCH + lane * 16 + i * 1024, uv[i]);
    __builtin_amdgcn_sched_barrier(0);
    if (w < 2) touch<false>(w == 0 ? w1 : w3, D * F * 2, fi < 14 ? (4 * (32 * (xg & 1) + j) + q) * 7168 + ((xg >> 1) * 14 + fi) * 128 : OOB);
    u32x4 xv[4];
#pragma unroll
    for (int i = 0; i < 4; ++i) xv[i] = *(const u32x4*)(x + rrow * D + rch + 64 * i);
    __builtin_amdgcn_sched_barrier(0);
    const unsigned dk = (15 - fi) - 8 * q, dw = (dk & 1) ? draw << 16 : draw;
    const u32x4 dg = {dk >> 1 == 0 ? dw : 0u, dk >> 1 == 1 ? dw : 0u, dk >> 1 == 2 ? dw : 0u, dk >> 1 == 3 ? dw : 0u};
    f32x32 Y;
    asm volatile(CONV_ASM : "=&{v[32:63]}"(Y)
      : [ab0] "v"(ab0), [ab1] "v"(ab0 + 1024), [ab2] "v"(ab0 + 2048), [ab3] "v"(ab0 + 3072), [bb] "v"(bb), [dg] "v"(dg), [pw00] "s"((unsigned)pw1), [pw01] "s"((unsigned)(pw1 >> 32)), [pw10] "s"((unsigned)pw3), [pw11] "s"((unsigned)(pw3 >> 32)), [szw] "s"(1024u * 7168u - pn * 224),
        [ldsw] "s"(w * 4096), [vow0] "v"(vow0), [vow1] "v"(vow1) : CONV_CLOB);
#pragma unroll
    for (int n = 0; n < 8; ++n) lds_st<u32x2>(yb + 512 * n, (u32x2){cvtpk(Y[4 * n], Y[4 * n + 1]), cvtpk(Y[4 * n + 2], Y[4 * n + 3])});
    lds_bar();
    const int a2 = (tid >> 5) & 3, sl = (tid >> 1) & 15;
    const unsigned rb = L_Y + 512 * (tid >> 7) + 128 * a2 + 8 * (sl ^ a2) + 4 * (tid & 1);
    unsigned yc[4][4];
#pragma unroll
    for (int i = 0; i < 4; ++i)
#pragma unroll
      for (int e = 0; e < 4; ++e) yc[i][e] = lds_ld<unsigned>(rb + e * 4096 + i * 1024);
#pragma unroll
    for (int i = 0; i < 4; ++i) { const unsigned* c = yc[i];
      const u32x4 o = {__builtin_amdgcn_perm(c[1], c[0], 0x05040100u), __builtin_amdgcn_perm(c[3], c[2], 0x05040100u), __builtin_amdgcn_perm(c[1], c[0], 0x07060302u), __builtin_amdgcn_perm(c[3], c[2], 0x07060302u)};
      __builtin_amdgcn_raw_buffer_store_b128(o, wr, WS_Z + (64 * i + (tid >> 2)) * 16384 + (d0 >> 2) * 64 + (tid & 3) * 16, 0, 16); }
    __builtin_amdgcn_sched_barrier(0);
#pragma unroll
    for (int i = 0; i < 16; ++i) xf[i] = unpk(xv[i >> 2][i & 3]);
    stats_put(xf, L_ST, w, lane);
  }
  drain_bar();
  if (tid < NREP) put_flag(ws, WS_F2 + tid * 4096 + j * 128 + xg * 8, tag);
  {
    const unsigned fo = WS_F2 + (b & (NREP - 1)) * 4096 + (lane >> 2) * 128 + (lane & 3) * 16, tlo = (unsigned)tag, thi = (unsigned)(tag >> 32);
    float mean1, rstd1; stats_get(L_ST, rs, mean1, rstd1);
    f32x2 qf[16];
#pragma unroll
    for (int i = 0; i < 16; ++i) { qf[i] = (xf[i] - mean1) * rstd1; asm volatile("" : "+v"(qf[i])); }
    if (w == 0) {
      __builtin_amdgcn_s_sleep(D2);
#pragma unroll 1
      for (unsigned spins = 0; spins < SPIN_LIMIT; ++spins) {
        const u32x4 f0 = __builtin_amdgcn_raw_buffer_load_b128(wr, fo, 0, 16), f1 = __builtin_amdgcn_raw_buffer_load_b128(wr, fo + 2048, 0, 16);
        if (__all(f0.x == tlo && f0.y == thi && f0.z == tlo && f0.w == thi && f1.x == tlo && f1.y == thi && f1.z == tlo && f1.w == thi)) break;
        asm volatile("" ::: "memory");
      }
    }
    asm volatile("s_barrier" ::: "memory");
    u32x4 zr[4];
#pragma unroll
    for (int i = 0; i < 4; ++i) zr[i] = __builtin_amdgcn_raw_buffer_load_b128(wr, WS_Z + b * 16384 + ((rch >> 2) + (rs & 1)) * 64 + (rs >> 1) * 16 + i * 1024, 0, 16);
    if (b == 0 && tid == 0) put_flag(ws, WS_EP, tag);
#pragma unroll
    for (int i = 0; i < 4; ++i) { u32x4 o;
      const bool odd = rs & 1;
      const unsigned g0 = __builtin_amdgcn_update_dpp(0u, odd ? zr[i].x : zr[i].z, 0xb1  , 0xf, 0xf, false), g1 = __builtin_amdgcn_update_dpp(0u, odd ? zr[i].y : zr[i].w, 0xb1, 0xf, 0xf, false);
      const unsigned zz[4] = {odd ? g0 : zr[i].x, odd ? g1 : zr[i].y, odd ? zr[i].z : g0, odd ? zr[i].w : g1};
#pragma unroll
      for (int e = 0; e < 4; ++e) { o[e] = pk2(__builtin_elementwise_fma(qf[4 * i + e], unpk(zz[e]), xf[4 * i + e])); xf[4 * i + e] = unpk(o[e]); }
      __builtin_amdgcn_raw_buffer_store_b128(o, wr, WS_H + (rrow * D + rch + 64 * i) * 2, 0, 16); }
    stats_put(xf, L_ST + 256, w, lane);
    lds_bar();
    float mean, rstd; stats_get(L_ST + 256, rs, mean, rstd);
#pragma unroll
    for (int i = 0; i < 4; ++i) { u32x4 o;
#pragma unroll
      for (int e = 0; e < 4; ++e) o[e] = pk2((xf[4 * i + e] - mean) * rstd);
      const int kc = rch + 64 * i;
      __builtin_amdgcn_raw_buffer_store_b128(o, wr, WS_HN + ((rrow >> 4) * 32 + (kc >> 5)) * 1024 + (((kc >> 3) & 3) * 16 + (rrow & 15)) * 16, 0, 16); }
  }
  drain_bar();
  if (tid == 0) put_flag(ws, WS_F3 + b * 128, tag);
  const int ks = xg >> 1, tn = j >> 2;
  u64 fa_v; const GAS u64* fa_p;
  const u64 stag = ((u64)rfl((unsigned)(tag >> 32)) << 32) | rfl((unsigned)tag);
  {
    int tz = threadIdx.x; asm volatile("" : "+v"(tz)); const int ln = tz & 63;
    const size_t pa = (size_t)ws + WS_HN + (size_t)pm * 524288, pw1 = (size_t)w1 + pn * 224, pw3 = (size_t)w3 + pn * 224;
    const unsigned voa = (4 * w) * 32768 + ln * 16;
    unsigned vow0, vow1;
    { const int lr = ln >> 4, slot = ln & 15, c0 = slot ^ (2 * lr), c1 = slot ^ (2 * (lr | 4)), rb = (16 * w + lr) * 7168;
      vow0 = c0 < 14 ? rb + c0 * 16 : OOB; vow1 = c1 < 14 ? rb + c1 * 16 : OOB; }
    unsigned vl[7];
    { const int g = ln >> 4, q = (ln & 15) >> 2, p = ln & 3, row = 8 * g + q, f = 2 * (q | ((g & 1) << 2)), rb = row * 256 + 8 * (p & 1);
#pragma unroll
      for (int i = 0; i < 7; ++i) vl[i] = rb + 16 * ((2 * i + (p >> 1)) ^ f); }
    const size_t pt = w == 0 ? (size_t)w1 : w == 1 ? (size_t)w3 : (size_t)w2 + (size_t)ks * (896 * 2048);
    const u32x4 srdt = {(unsigned)pt, (unsigned)(pt >> 32), w < 2 ? D * F * 2u : 896u * 2048u, 0x20000u};
    const int tr_ = 32 * (xg & 1) + j;
    unsigned vt = w < 2 ? ((ln & 15) < 14 && ln < 32 ? (256 + 2 * tr_ + (ln >> 4)) * 7168 + ((xg >> 1) * 14 + (ln & 15)) * 128 : OOB) : (ln < 16 ? (2 * tr_ + w - 2 - 7 * 128) * 2048 + ln * 128 : OOB);
    const u32x4 srdc = {(unsigned)cpc & ~127u, (unsigned)(cpc >> 32), csz, 0x20000u};
    const char* fa = ws + WS_F3 + (pm * 32 + w * 8 + (ln & 7)) * 128;
    f32x32 A[7]; u64 ft;
    asm volatile(G1_ASM "s_barrier\n"
      : "=&{v[32:63]}"(A[0]), "=&{v[64:95]}"(A[1]), "=&{v[96:127]}"(A[2]), "=&{v[128:159]}"(A[3]), "=&{v[160:191]}"(A[4]), "=&{v[192:223]}"(A[5]), "=&{v[224:255]}"(A[6]),
        [vt] "+v"(vt), [ft] "=&v"(ft)
      : [srdt] "s"(srdt), [srdo] "s"(srdc), [vo3] "v"(tz < 16 ? (tz * 32 + j) * 128 : OOB), [st] "s"(w < 2 ? 128u * 7168u : 128u * 2048u), [pa0] "s"((unsigned)pa), [pa1] "s"((unsigned)(pa >> 32)), [sza] "s"(524288u),
        [pw00] "s"((unsigned)pw1), [pw01] "s"((unsigned)(pw1 >> 32)), [pw10] "s"((unsigned)pw3), [pw11] "s"((unsigned)(pw3 >> 32)), [szw] "s"(1024u * 7168u - pn * 224),
        [ldsw] "s"(w * 4096), [voa] "v"(voa), [vow0] "v"(vow0), [vow1] "v"(vow1), [fa] "v"(fa), [tag] "s"(stag),
        [vl0] "v"(vl[0]), [vl1] "v"(vl[1]), [vl2] "v"(vl[2]), [vl3] "v"(vl[3]), [vl4] "v"(vl[4]), [vl5] "v"(vl[5]), [vl6] "v"(vl[6])
      : G1_CLOB);
    tz = threadIdx.x; asm volatile("" : "+v"(tz));
    const size_t pw2 = (size_t)w2 + (size_t)ks * (896 * 2048) + tn * 256;
    const u32x4 srd2 = {(unsigned)pw2, (unsigned)(pw2 >> 32), 896u * 2048u - tn * 256, 0x20000u};
    const unsigned wv0 = (16 * w + ((tz >> 4) & 3)) * 2048 + ((tz & 15) ^ (2 * ((tz >> 4) & 3))) * 16, wv1 = (16 * w + ((tz >> 4) & 3)) * 2048 + ((tz & 15) ^ (2 * (((tz >> 4) & 3) | 4))) * 16;
    const int fr = tz & 15, r4 = (tz >> 4) & 3;
    const unsigned ob = WS_HH + ((16 * pm + 4 * w) * 448 + 112 * ks) * 256 + fr * 16;
    unsigned o01 = ob + (8 * tn + 2 * (r4 & 1) + (r4 >> 1)) * 256;
    unsigned ho0 = ob + (64 + 6 * tn + 2 * (r4 & 1) + (r4 >> 1)) * 256, ho1 = ob + ((r4 & 1) * 448 + 68 + 6 * tn + (r4 >> 1)) * 256;
    fa_p = (const GAS u64*)(ws + WS_F4 + ((pm * 32 + ks * 8 + (tz & 7)) * 4 + w) * 128);
    const u32x4 srdw = {(unsigned)(size_t)ws, (unsigned)((size_t)ws >> 32), WS_END, 0x20000u};
    asm volatile(EPI_ASM
      : "+{v[32:63]}"(A[0]), "+{v[64:95]}"(A[1]), "+{v[96:127]}"(A[2]), "+{v[128:159]}"(A[3]), "+{v[160:191]}"(A[4]), "+{v[192:223]}"(A[5]), "+{v[224:255]}"(A[6]),
        [o01] "+v"(o01), [ho0] "+v"(ho0), [ho1] "+v"(ho1), [ft] "=&{v[22:23]}"(fa_v)
      : [srdw] "s"(srdw), [srd2] "s"(srd2), [wv0] "v"(wv0), [wv1] "v"(wv1), [ldsw] "s"(w * 4096), [tagv] "v"(stag), [fap] "v"(fa_p),
        [fo4] "v"((tz & 63) == 0 ? WS_F4 + ((pm * 32 + pn) * 4 + w) * 128 : OOB)
      : "s60", "s61", "s62", "scc", "memory");
  }
  {
    int tz = threadIdx.x; asm volatile("" : "+v"(tz)); int ln = tz & 63;
    const size_t pa = (size_t)ws + WS_HH + (size_t)pm * (16 * 114688) + ks * 28672, pw = (size_t)w2 + (size_t)ks * (896 * 2048) + tn * 256;
    const unsigned voa = (4 * w) * 114688 + ln * 16;
    unsigned vow0, vow1;
    { const int lr = ln >> 4, slot = ln & 15, rb = (16 * w + lr) * 2048; vow0 = rb + (slot ^ (2 * lr)) * 16; vow1 = rb + (slot ^ (2 * (lr | 4))) * 16; }
    unsigned vl[8];
    { const int g = ln >> 4, q = (ln & 15) >> 2, p = ln & 3, row = 8 * g + q, f = 2 * (q | ((g & 1) << 2)), rb = row * 256 + 8 * (p & 1);
#pragma unroll
      for (int i = 0; i < 8; ++i) vl[i] = rb + 16 * ((2 * i + (p >> 1)) ^ f); }
    const int tile = pm * 8 + tn, fr = ln & 15, r4 = ln >> 4;
    const size_t pp = (size_t)ws + WS_PART + (size_t)tile * 196608 + ks * 49152 + w * 12288;
    const u32x4 srdp = {(unsigned)pp, (unsigned)(pp >> 32), 12288u, 0x20000u};
    const char* fa = ws + WS_F4B + ((pm * 32 + ks * 8 + (ln & 7)) * 4 + w) * 128;
    const unsigned w2t = w == 0 ? 0x0u : w == 1 ? 0x500500u : w == 2 ? 0x550550u : 0x555555u;
    const unsigned eo = (256 * pm + 64 * w + 16 * ks + fr) * 2048 + (128 * tn + 16 * (r4 & 1) + 8 * (r4 >> 1)) * 2;
    const u32x4 srdw = {(unsigned)(size_t)ws, (unsigned)((size_t)ws >> 32), WS_END, 0x20000u};
    const unsigned pb = WS_PART + tile * 196608 + w * 12288;
    const GAS u64* fp = (const GAS u64*)(ws + WS_F5 + ((tile * 4 + ((ks + 3 - (ln & 3)) & 3)) * 4 + w) * 128);
    f32x32 A0, A1, A2, A3; unsigned got; u32x16 P0, P1, P2;
    unsigned va0 = voa + ks * 114688, va1 = voa + ((ks + 1) & 3) * 114688, va2 = voa + ((ks + 2) & 3) * 114688, va3 = voa + ((ks + 3) & 3) * 114688;
    asm volatile(G2_ASM
      : "=&{v[32:63]}"(A0), "=&{v[64:95]}"(A1), "=&{v[96:127]}"(A2), "=&{v[128:159]}"(A3), "=&{v[160:175]}"(P0), "=&{v[176:191]}"(P1), "=&{v[192:207]}"(P2),
        [ft] "+{v[22:23]}"(fa_v), [got] "=&s"(got)
      : [pa0] "s"((unsigned)pa), [pa1] "s"((unsigned)(pa >> 32)), [sza] "s"(16u * 114688u - ks * 28672),
        [pw00] "s"((unsigned)pw), [pw01] "s"((unsigned)(pw >> 32)), [szw] "s"(896u * 2048u - tn * 256),
        [ra] "s"(0), [ra0] "s"(4 * 2048), [w2t] "s"(w2t), [fb] "v"(ln == 0 ? WS_F4B + ((pm * 32 + pn) * 4 + w) * 128 : OOB),
        [voa0] "v"(va0), [voa1] "v"(va1), [voa2] "v"(va2), [voa3] "v"(va3),
        [ldsw] "s"(w * 4096), [vow0] "v"(vow0), [vow1] "v"(vow1), [fa] "v"(fa), [fa0] "v"(fa_p), [tag] "s"(stag), [srdp] "s"(srdp),
        [po1] "v"(ln * 16), [po2] "v"(4096 + ln * 16), [po3] "v"(8192 + ln * 16),
        [tagv] "v"(stag), [fo] "v"(ln == 0 ? WS_F5 + ((tile * 4 + ks) * 4 + w) * 128 : OOB), [srdw] "s"(srdw), [fp] "v"(fp),
        [ws0] "s"((unsigned)(size_t)ws), [ws1] "s"((unsigned)((size_t)ws >> 32)), [wsz] "s"(WS_END),
        [ps0] "s"(pb + ((ks + 3) & 3) * 49152), [ps1] "s"(pb + ((ks + 2) & 3) * 49152 + 4096), [ps2] "s"(pb + ((ks + 1) & 3) * 49152 + 8192),
        [vl0] "v"(vl[0]), [vl1] "v"(vl[1]), [vl2] "v"(vl[2]), [vl3] "v"(vl[3]), [vl4] "v"(vl[4]), [vl5] "v"(vl[5]), [vl6] "v"(vl[6]), [vl7] "v"(vl[7])
      : G2_CLOB);
    u32x4 hr[4];
#pragma unroll
    for (int e = 0; e < 4; ++e) hr[e] = __builtin_amdgcn_raw_buffer_load_b128(wr, eo + e * 64, WS_H, 16);
    f32x2 a[16];
#pragma unroll
    for (int e = 0; e < 4; ++e) {
      auto s0 = __builtin_amdgcn_permlane16_swap(hr[e].x, hr[e].z, false, false); auto s1 = __builtin_amdgcn_permlane16_swap(hr[e].y, hr[e].w, false, false);
      const unsigned hq[4] = {s0[0], s1[0], s0[1], s1[1]};
#pragma unroll
      for (int t = 0; t < 4; ++t) { a[4 * e + t] = (f32x2){A0[8 * e + 2 * t], A0[8 * e + 2 * t + 1]} + unpk(hq[t]); asm volatile("" : "+v"(a[4 * e + t])); } }
    asm volatile("s_waitcnt vmcnt(0)" : "+v"(P0), "+v"(P1), "+v"(P2) :: "memory");
    u32x4 p[4][3];
#pragma unroll
    for (int e = 0; e < 4; ++e)
#pragma unroll
      for (int i = 0; i < 3; ++i) { const int r = (e * 3 + i) * 4; const u32x16& P = r < 16 ? P0 : r < 32 ? P1 : P2; p[e][i] = (u32x4){P[r & 15], P[(r & 15) + 1], P[(r & 15) + 2], P[(r & 15) + 3]}; }
#pragma unroll 1
    for (unsigned spins = 0; spins < SPIN_LIMIT && got != 7u; ++spins) {
      const u64 fv = __hip_atomic_load(fp, __ATOMIC_RELAXED, __HIP_MEMORY_SCOPE_AGENT);
      const unsigned up = (unsigned)__ballot(fv == stag) & 7u & ~got;
      asm volatile("" ::: "memory");
#pragma unroll
      for (int i = 0; i < 3; ++i) if (up & (1u << i)) {
#pragma unroll
        for (int e = 0; e < 4; ++e) p[e][i] = __builtin_amdgcn_raw_buffer_load_b128(wr, ln * 16 + e * 1024, pb + ((ks + 3 - i) & 3) * 49152 + i * 4096, 16); }
      got |= up;
    }
#pragma unroll
    for (int e = 0; e < 4; ++e) { u32x4 o;
#pragma unroll
      for (int t = 0; t < 4; ++t) o[t] = pk2(((a[4 * e + t] + unpk(p[e][0][t])) + unpk(p[e][1][t])) + unpk(p[e][2][t]));
      auto s0 = __builtin_amdgcn_permlane16_swap(o.x, o.z, false, false); auto s1 = __builtin_amdgcn_permlane16_swap(o.y, o.w, false, false);
      __builtin_amdgcn_raw_buffer_store_b128((u32x4){s0[0], s1[0], s0[1], s1[1]}, mkrs(out, S * D * 2), eo + e * 64, 0, 2  ); }
  }
  asm volatile(".Lh3_end:");
}

extern "C" void kernel_launch(void* const* d_in, const int* in_sizes, int n_in, void* d_out, int out_size, void* d_ws, size_t ws_size, hipStream_t stream) {
  (void)hipFuncSetAttribute((const void*)k_fused, hipFuncAttributeMaxDynamicSharedMemorySize, LDS_BYTES);
  k_fused<<<256, 256, LDS_BYTES, stream>>>((const u16*)d_in[0], (const u16*)d_in[1], (const u16*)d_in[2], (const u16*)d_in[3], (const u16*)d_in[4], (const u16*)d_in[5],
                                           (const u16*)d_in[7], (const u16*)d_in[8], (const u16*)d_in[9], (u16*)d_out, (char*)d_ws);
}
```
